```python
import torch

def _softmax(x, axis=-1):
    return torch.softmax(x, dim=axis)

def _gelu(x):
    return torch.nn.functional.gelu(x, approximate='tanh')

def _silu(x):
    return torch.nn.functional.silu(x)

def _sigmoid(x):
    return torch.sigmoid(x)

def _layernorm(x, axis=-1, eps=1e-05):
    m = x.mean(dim=axis, keepdim=True)
    v = x.var(dim=axis, keepdim=True, unbiased=False)
    return (x - m) / torch.sqrt(v + eps)

def _scatter_add(tgt, idx, src, axis=0):
    return tgt.clone().index_add_(axis, idx, src)

def _segment_sum(src, seg, n):
    out = torch.zeros((n,) + src.shape[1:], dtype=src.dtype)
    return out.index_add_(0, seg, src)

def _pad(x, pw):
    flat = tuple((v for pair in reversed(pw) for v in pair))
    return torch.nn.functional.pad(x, flat)
kernel_name = 'recurrent_scan__memory_aug_recurrent_mem_tr__glu_gated_swiglu__prompt'

def setup_inputs():

    _w = lambda a: (lambda t: t.to(torch.bfloat16) if t.is_floating_point() else t)(torch.as_tensor(a))

    def _A_setup():
        B, S, Dm = (1, 2048, 640)
        H, Dh = (32, 128)
        M, seg = (8, 32)
        seg = min(seg, S)
        S = S // seg * seg
        return {'x': _w((lambda *s: torch.randn(*s, dtype=torch.bfloat16) * 0.1)(B, S, Dm)), 'mem0': _w((lambda *s: torch.randn(*s, dtype=torch.bfloat16) * 1.6)(B, M, Dm)), 'Wq': _w((lambda *s: torch.randn(*s, dtype=torch.bfloat16) * 0.8)(Dm, H, Dh) * Dm ** (-0.5)), 'Wk': _w((lambda *s: torch.randn(*s, dtype=torch.bfloat16) * 0.8)(Dm, H, Dh) * Dm ** (-0.5)), 'Wv': _w((lambda *s: torch.randn(*s, dtype=torch.bfloat16) * 1.6)(Dm, H, Dh) * Dm ** (-0.5)), 'Wo': _w((lambda *s: torch.randn(*s, dtype=torch.bfloat16) * 1.6)(H, Dh, Dm) * (H * Dh) ** (-0.5)), 'seg_len': seg}
    _A_in = _A_setup()

    def _B_setup():
        B, S, D, F = (1, 2048, 640, 1728)
        return {'x': _w((lambda *s: torch.randn(*s, dtype=torch.bfloat16) * 0.1)(B, S, D)), 'w1': _w((lambda *s: torch.randn(*s, dtype=torch.bfloat16) * 0.4)(D, F) * (2.0 / D) ** 0.5), 'w3': _w((lambda *s: torch.randn(*s, dtype=torch.bfloat16) * 0.4)(D, F) * (2.0 / D) ** 0.5), 'w2': _w((lambda *s: torch.randn(*s, dtype=torch.bfloat16) * 0.4)(F, D) * (2.0 / F) ** 0.5)}
    _B_in = _B_setup()
    return {'A_x': _A_in['x'], 'A_mem0': _A_in['mem0'], 'A_Wq': _A_in['Wq'], 'A_Wk': _A_in['Wk'], 'A_Wv': _A_in['Wv'], 'A_Wo': _A_in['Wo'], 'A_seg_len': _A_in['seg_len'], 'B_x': _B_in['x'], 'B_w1': _B_in['w1'], 'B_w3': _B_in['w3'], 'B_w2': _B_in['w2']}

def reference(A_x, A_mem0, A_Wq, A_Wk, A_Wv, A_Wo, A_seg_len, B_x, B_w1, B_w3, B_w2):

    def _A_ref(x, mem0, Wq, Wk, Wv, Wo, seg_len):
        B, S, Dm = x.shape
        H, Dh = (Wq.shape[1], Wq.shape[2])
        M = mem0.shape[1]
        scale = Dh ** (-0.5)
        n_seg = S // seg_len
        L = M + seg_len
        idx = torch.arange(L)
        mask = (idx[:, None] < M) | (idx[:, None] >= idx[None, :])
        mem = mem0
        outs = []
        for s in range(n_seg):
            xs = x[:, s * seg_len:(s + 1) * seg_len, :].clone()
            h = torch.concatenate([mem, xs], axis=1)
            q = torch.einsum('btd,dhe->bhte', h, Wq)
            k = torch.einsum('btd,dhe->bhte', h, Wk)
            v = torch.einsum('btd,dhe->bhte', h, Wv)
            scores = torch.einsum('bhqe,bhke->bhqk', q, k) * scale
            scores = torch.where(mask[None, None], scores, -1000000000.0)
            p = _softmax(scores, axis=-1)
            o = torch.einsum('bhqk,bhke->bhqe', p, v)
            o = torch.einsum('bhte,hed->btd', o, Wo)
            mem = o[:, :M, :].clone()
            outs.append(o[:, M:, :])
        return torch.concatenate(outs, axis=1)

    def _B_ref(x, w1, w3, w2):
        gate = torch.einsum('bsd,df->bsf', x, w1)
        up = torch.einsum('bsd,df->bsf', x, w3)
        h = _silu(gate) * up
        out = torch.einsum('bsf,fd->bsd', h, w2)
        return out
    x = A_x
    xn = _layernorm(x, axis=-1)
    s = _A_ref(xn, A_mem0, A_Wq, A_Wk, A_Wv, A_Wo, A_seg_len)
    g = _silu(_B_ref(xn, B_w1, B_w3, B_w2))
    return x + s * g
```

```cpp
#include <hip/hip_runtime.h>
#include <hip/hip_bf16.h>
#include <stdint.h>
#define HOLD 0
#define EARLY 0

#define K30 68u
#define PREFETCH 0

typedef unsigned v4u __attribute__((ext_vector_type(4)));
typedef unsigned v2u __attribute__((ext_vector_type(2)));
typedef unsigned v8u __attribute__((ext_vector_type(8)));
typedef float f32x4 __attribute__((ext_vector_type(4)));
typedef __bf16 bf16x8 __attribute__((ext_vector_type(8)));
typedef __bf16 bf16x16 __attribute__((ext_vector_type(16)));
typedef __bf16 bf16x2 __attribute__((ext_vector_type(2)));
typedef short s16x4 __attribute__((ext_vector_type(4)));
typedef unsigned short u16;
#define LAS __attribute__((address_space(3)))

constexpr int S = 2048, DM = 640, H = 32, DH = 128, MM = 8, SEG = 32, NSEG = 64, FF = 1728, HD = H * DH;
constexpr unsigned SENT = 0x7F817F81u;
constexpr unsigned SPINCAP = 1u << 17;

constexpr size_t PAGE = 2u << 20;
constexpr size_t OFF_IN = 0;
constexpr size_t OFF_MEM = 2 * PAGE;
constexpr int NCAL = 3;
constexpr size_t OFF_CTL = 6 * PAGE;
constexpr size_t OFF_XN = 7 * PAGE;
constexpr size_t OFF_G = 9 * PAGE;
constexpr size_t OFF_HH = 12 * PAGE;
constexpr size_t OFF_OX = 16 * PAGE;
constexpr size_t OFF_QKV = 24 * PAGE;
constexpr size_t OFF_DBG = 56 * PAGE;

__device__ __forceinline__ float bf2f(u16 v) { return __uint_as_float((unsigned)v << 16); }
__device__ __forceinline__ u16 f2bf(float f) { __bf16 b = (__bf16)f; return __builtin_bit_cast(u16, b); }
__device__ __forceinline__ unsigned pack2(float lo, float hi) { bf16x2 p; p[0] = (__bf16)lo; p[1] = (__bf16)hi; return __builtin_bit_cast(unsigned, p); }
__device__ __forceinline__ unsigned nearmap(unsigned l) { return ((l >> 13) << 14) | (l & 8191u); }
__device__ __forceinline__ v4u ld16s(__amdgpu_buffer_rsrc_t r, unsigned off) { return __builtin_amdgcn_raw_buffer_load_b128(r, off, 0, 16); }
__device__ __forceinline__ void st16s(__amdgpu_buffer_rsrc_t r, unsigned off, v4u v) { __builtin_amdgcn_raw_buffer_store_b128(v, r, off, 0, 16); }
__device__ __forceinline__ void st8s(__amdgpu_buffer_rsrc_t r, unsigned off, v2u v) { __builtin_amdgcn_raw_buffer_store_b64(v, r, off, 0, 16); }
__device__ __forceinline__ void st4s(__amdgpu_buffer_rsrc_t r, unsigned off, unsigned v) { __builtin_amdgcn_raw_buffer_store_b32(v, r, off, 0, 16); }
__device__ __forceinline__ unsigned umax4(v4u v) { return max(max(v.x, v.y), max(v.z, v.w)); }

__device__ __forceinline__ void start_part(const u16* x, const u16* mem0, const u16* w1, const u16* w3, const u16* w2, const u16* Wq, const u16* Wk, const u16* Wv, unsigned char* ws) {
  const int b = blockIdx.x, tid = threadIdx.x, lane = tid & 63, wave = __builtin_amdgcn_readfirstlane(tid >> 6);
  {
    unsigned t0; unsigned long long pc;
    asm volatile("s_getpc_b64 %1\nKPC0:\n v_lshlrev_b32 %0, 7, %2\n v_min_u32 %0, KEND-KPC0-4, %0\n global_load_dword v70, %0, %1\n"
                 : "=&v"(t0), "=&s"(pc) : "v"(tid < 128 ? min(lane, 23) + wave * 24 : ((b >> 3) & 3) * 128 + tid - 80) : "v70", "memory");
  }
  const __amdgpu_buffer_rsrc_t r = __builtin_amdgcn_make_buffer_rsrc(ws, 0, 0x08000000, 0x00020000);
  const v4u sv = {SENT, SENT, SENT, SENT}, zv = {0u, 0u, 0u, 0u};
  const int gt = b * 128 + (tid & 127);
  if (wave >= 2) {
#pragma unroll
  for (int k = 0; k < 5; ++k) { const int i = gt + k * 32768, sl = i >= 81920, o = i - sl * 81920; st16s(r, (unsigned)OFF_IN + sl * (unsigned)PAGE + o * 16, sv); }
  {
    const int g2 = 32767 - gt, cp = g2 / 640, ch = g2 % 640, j = g2 - 30720, cq = j >> 3, k = g2 - 31232;
    unsigned o = 0xF0000000u; v4u val = zv;
    if (g2 < 30720) { o = (unsigned)OFF_MEM + (1 + (cp >> 4)) * 262144 + (nearmap(((cp >> 1) & 7) * 16384 + ch * 16) | ((cp & 1) << 13)); val = sv; }
    else if (g2 < 31232) { o = (unsigned)OFF_MEM + (cq >> 4) * 262144 + ((cq >> 1) & 7) * 32768 + 16384 + (cq & 1) * 8192 + 2048 + (j & 7) * 16; val = (v4u){0u, K30, 0u, K30}; }
    else o = (unsigned)(k >> 9) * (unsigned)PAGE + (unsigned)PAGE - 32768 + ((k >> 6) & 7) * 4096 + (k & 63) * 16;
    st16s(r, o, val);
    st16s(r, g2 < 1024 ? (unsigned)OFF_CTL + g2 * 16 : g2 < 4096 ? (unsigned)OFF_MEM + 4 * 262144 + (g2 - 1024) * 16 : 0xF0000000u, zv);
  }
    const unsigned fo = min((unsigned)((b * 2 + (wave & 1)) * 34 + min(lane, 33)), 17279u) * 128u;
    asm volatile("global_load_dword v70, %0, %1\n global_load_dword v70, %0, %2\n global_load_dword v70, %0, %3\n" :: "v"(fo), "s"(w1), "s"(w3), "s"(w2) : "v70", "memory");
    __builtin_amdgcn_s_barrier();
    return;
  }
  const int row = b * 8 + wave * 4 + (lane & 3), l16 = lane >> 2;
  const int mi = min(gt, 16 * 640 - 1), cp0 = mi / 640, ch0 = mi % 640;
  v4u xin[5];
#pragma unroll
  for (int i = 0; i < 5; ++i) xin[i] = *(const v4u*)(x + row * DM + (i * 16 + l16) * 8);
  v4u d = *(const v4u*)(mem0 + (2 * ((ch0 & 15) >> 2) + (ch0 & 1)) * DM + 64 * (ch0 >> 6) + 16 * ((ch0 & 63) >> 4) + 8 * ((ch0 >> 1) & 1));
  __builtin_amdgcn_sched_barrier(0);
  float v[40]; float s = 0.f;
#pragma unroll
  for (int i = 0; i < 5; ++i)
#pragma unroll
    for (int j = 0; j < 4; ++j) { v[i * 8 + 2 * j] = __uint_as_float(xin[i][j] << 16); v[i * 8 + 2 * j + 1] = __uint_as_float(xin[i][j] & 0xffff0000u); }
#pragma unroll
  for (int i = 0; i < 40; ++i) s += v[i];
  for (int m = 32; m >= 4; m >>= 1) s += __shfl_xor(s, m);
  const float mean = s * (1.f / DM);
  float q = 0.f;
#pragma unroll
  for (int i = 0; i < 40; ++i) { v[i] -= mean; q += v[i] * v[i]; }
  for (int m = 32; m >= 4; m >>= 1) q += __shfl_xor(q, m);
  const float rstd = 1.f / sqrtf(q * (1.f / DM) + 1e-5f);
#pragma unroll
  for (int i = 0; i < 5; ++i) {
    const int ch = i * 16 + l16;
    v4u o;
#pragma unroll
    for (int j = 0; j < 4; ++j) o[j] = pack2(v[i * 8 + 2 * j] * rstd, v[i * 8 + 2 * j + 1] * rstd);
    st16s(r, (unsigned)OFF_XN + (row >> 4) * 20480 + (ch >> 2) * 1024 + ((ch & 3) * 16 + (row & 15)) * 16, o);
  }
#pragma unroll
  for (int m = 0; m < 4; ++m) if (d[m] == SENT) d[m] |= 0x40u;
  st16s(r, gt < 16 * 640 ? (unsigned)OFF_MEM + (nearmap((cp0 >> 1) * 16384 + ch0 * 16) | ((cp0 & 1) << 13)) : 0xF0000000u, d);
  __builtin_amdgcn_s_barrier();
}

#define CHAIN_ASM_TEXT \
  "s_load_dwordx8 s[64:71], %[ka], 8\n" \
  "s_load_dwordx2 s[36:37], %[ka], 64\n" \
  "s_load_dwordx4 s[72:75], %[ka], 40\n" \
  "s_load_dwordx2 s[76:77], %[ka], 56\n" \
  "s_lshr_b32 s25, s93, 8\n" \
  "s_and_b32 s84, s93, 0xff\n" \
  "s_lshr_b32 s80, s84, 2\n" \
  "s_lshr_b32 s85, s80, 2\n" \
  "s_mul_i32 s85, s85, 20\n" \
  "s_and_b32 s82, s80, 3\n" \
  "s_add_u32 s85, s85, s82\n" \
  "s_and_b32 s82, s84, 3\n" \
  "s_add_u32 s83, s82, 1\n" \
  "s_cmp_eq_u32 s82, 0\n" \
  "s_cselect_b32 s83, 0, s83\n" \
  "s_lshl2_add_u32 s86, s83, s85\n" \
  "s_cmp_eq_u32 s25, 1\n" \
  "s_cselect_b32 s83, 1, s83\n" \
  "s_lshl2_add_u32 s81, s83, s85\n" \
  "s_lshl_b32 s83, s83, 2\n" \
  "s_lshr_b32 s62, 0x1245, s83\n" \
  "s_and_b32 s62, s62, 15\n" \
  "s_sub_u32 s83, s25, 1\n" \
  "s_or_b32 s82, s82, s83\n" \
  "s_cmp_eq_u32 s82, 0\n" \
  "s_cselect_b32 s82, s81, -1\n" \
  "s_cmp_eq_u32 s25, 0\n" \
  "s_cselect_b32 s81, s81, s82\n" \
  "s_lshr_b32 s82, s81, 1\n" \
  "s_lshl_b32 s82, s82, 14\n" \
  "s_and_b32 s83, s81, 1\n" \
  "s_lshl_b32 s83, s83, 12\n" \
  "s_or_b32 s16, s82, s83\n" \
  "s_cmp_lt_i32 s81, 0\n" \
  "s_cselect_b32 s16, -1, s16\n" \
  "s_cselect_b32 s81, 0, s81\n" \
  "s_lshl_b32 s17, s81, 6\n" \
  "s_and_b32 s82, s17, 8192\n" \
  "s_add_u32 s17, s17, s82\n" \
  "s_and_b32 s82, s84, 1\n" \
  "s_lshl_b32 s22, s82, 12\n" \
  "s_lshl_b32 s82, s82, 2\n" \
  "s_add_u32 s82, s82, s25\n" \
  "s_mul_i32 s18, s82, 163840\n" \
  "s_lshr_b32 s83, s84, 1\n" \
  "s_lshl_b32 s83, s83, 6\n" \
  "s_add_u32 s18, s18, s83\n" \
  "s_lshr_b32 s19, s84, 5\n" \
  "s_lshl_b32 s19, s19, 15\n" \
  "s_mul_i32 s20, s80, 2113536\n" \
  "s_add_u32 s20, s20, 50331648\n" \
  "s_and_b32 s21, s84, 2\n" \
  "s_lshl_b32 s21, s21, 11\n" \
  "s_add_u32 s21, s21, 16384\n" \
  "s_lshl_b32 s23, s80, 6\n" \
  "s_add_u32 s23, s23, 12587008\n" \
  "s_lshl_b32 s24, s84, 6\n" \
  "s_add_u32 s24, s24, 12591104\n" \
  "s_lshl_b32 s53, s80, 19\n" \
  "s_add_u32 s53, s53, s22\n" \
  "s_and_b32 s82, s84, 2\n" \
  "s_add_u32 s82, s82, s25\n" \
  "s_lshl_b32 s82, s82, 10\n" \
  "s_add_u32 s53, s53, s82\n" \
  "s_add_u32 s53, s53, 33552384\n" \
  "s_lshr_b32 s82, s86, 1\n" \
  "s_lshl_b32 s82, s82, 14\n" \
  "s_and_b32 s83, s86, 1\n" \
  "s_lshl_b32 s83, s83, 12\n" \
  "s_or_b32 s14, s82, s83\n" \
  "s_and_b32 s83, s25, 1\n" \
  "s_lshl_b32 s83, s83, 11\n" \
  "s_add_u32 s14, s14, s83\n" \
  "s_add_u32 s13, s14, 32768\n" \
  "s_and_b32 s82, s84, 3\n" \
  "s_cmp_eq_u32 s82, 0\n" \
  "s_cselect_b32 s13, s13, -1\n" \
  "s_mov_b32 s15, 0\n" \
  "s_lshl_b32 s56, s25, 12\n" \
  "s_lshl_b32 s57, s25, 11\n" \
  "s_lshl_b32 s27, s25, 10\n" \
  "s_lshl_b32 s85, s25, 17\n" \
  "v_mbcnt_lo_u32_b32 v16, -1, 0\n" \
  "v_mbcnt_hi_u32_b32 v16, -1, v16\n" \
  "v_lshrrev_b32 v17, 4, v16\n" \
  "v_and_b32 v18, 15, v16\n" \
  "v_and_b32 v19, 1, v17\n" \
  "v_lshlrev_b32 v19, 1, v19\n" \
  "v_xor_b32 v19, v19, v18\n" \
  "v_lshlrev_b32 v0, 13, v17\n" \
  "v_lshl_add_u32 v0, v19, 4, v0\n" \
  "v_add_u32 v0, s85, v0\n" \
  "v_xor_b32 v1, 64, v0\n" \
  "v_xor_b32 v2, 128, v0\n" \
  "v_xor_b32 v3, 192, v0\n" \
  "v_lshrrev_b32 v19, 3, v16\n" \
  "v_lshlrev_b32 v4, 13, v19\n" \
  "v_lshrrev_b32 v19, 1, v19\n" \
  "v_and_b32 v19, 2, v19\n" \
  "v_and_b32 v20, 7, v16\n" \
  "v_xor_b32 v19, v19, v20\n" \
  "v_lshl_add_u32 v4, v19, 4, v4\n" \
  "v_add_u32 v4, s85, v4\n" \
  "v_xor_b32 v5, 64, v4\n" \
  "s_waitcnt lgkmcnt(0)\n" \
  "s_and_b32 s37, s37, 0xffff\n" \
  "s_mov_b32 s38, 0x08000000\n" \
  "s_mov_b32 s39, 0x20000\n" \
  "s_lshr_b32 s82, s84, 2\n" \
  "s_lshl_b32 s82, s82, 8\n" \
  "s_add_u32 s64, s64, s82\n" \
  "s_addc_u32 s65, s65, 0\n" \
  "s_add_u32 s66, s66, s82\n" \
  "s_addc_u32 s67, s67, 0\n" \
  "s_and_b32 s83, s84, 2\n" \
  "s_lshl_b32 s83, s83, 6\n" \
  "s_add_u32 s82, s82, s83\n" \
  "s_add_u32 s68, s68, s82\n" \
  "s_addc_u32 s69, s69, 0\n" \
  "s_lshr_b32 s82, s84, 1\n" \
  "s_mul_i32 s82, s82, 81920\n" \
  "s_and_b32 s83, s84, 1\n" \
  "s_mul_i32 s83, s83, 640\n" \
  "s_add_u32 s82, s82, s83\n" \
  "s_add_u32 s70, s70, s82\n" \
  "s_addc_u32 s71, s71, 0\n" \
  "s_mul_i32 s82, s25, 640\n" \
  "s_mul_i32 s83, s25, 10240\n" \
  "v_add_u32 v19, s82, v16\n" \
  "v_add_u32 v20, 0, v19\n" \
  "v_mul_u32_u24 v21, 1639, v20\n" \
  "v_lshrrev_b32 v21, 16, v21\n" \
  "v_mul_u32_u24 v21, 640, v21\n" \
  "v_lshl_add_u32 v21, v20, 4, v21\n" \
  "s_add_u32 m0, s83, 0\n" \
  "s_nop 0\n" \
  "global_load_lds_dwordx4 v21, s[70:71]\n" \
  "v_add_u32 v20, 64, v19\n" \
  "v_mul_u32_u24 v21, 1639, v20\n" \
  "v_lshrrev_b32 v21, 16, v21\n" \
  "v_mul_u32_u24 v21, 640, v21\n" \
  "v_lshl_add_u32 v21, v20, 4, v21\n" \
  "s_add_u32 m0, s83, 1024\n" \
  "s_nop 0\n" \
  "global_load_lds_dwordx4 v21, s[70:71]\n" \
  "v_add_u32 v20, 128, v19\n" \
  "v_mul_u32_u24 v21, 1639, v20\n" \
  "v_lshrrev_b32 v21, 16, v21\n" \
  "v_mul_u32_u24 v21, 640, v21\n" \
  "v_lshl_add_u32 v21, v20, 4, v21\n" \
  "s_add_u32 m0, s83, 2048\n" \
  "s_nop 0\n" \
  "global_load_lds_dwordx4 v21, s[70:71]\n" \
  "v_add_u32 v20, 192, v19\n" \
  "v_mul_u32_u24 v21, 1639, v20\n" \
  "v_lshrrev_b32 v21, 16, v21\n" \
  "v_mul_u32_u24 v21, 640, v21\n" \
  "v_lshl_add_u32 v21, v20, 4, v21\n" \
  "s_add_u32 m0, s83, 3072\n" \
  "s_nop 0\n" \
  "global_load_lds_dwordx4 v21, s[70:71]\n" \
  "v_add_u32 v20, 256, v19\n" \
  "v_mul_u32_u24 v21, 1639, v20\n" \
  "v_lshrrev_b32 v21, 16, v21\n" \
  "v_mul_u32_u24 v21, 640, v21\n" \
  "v_lshl_add_u32 v21, v20, 4, v21\n" \
  "s_add_u32 m0, s83, 4096\n" \
  "s_nop 0\n" \
  "global_load_lds_dwordx4 v21, s[70:71]\n" \
  "v_add_u32 v20, 320, v19\n" \
  "v_mul_u32_u24 v21, 1639, v20\n" \
  "v_lshrrev_b32 v21, 16, v21\n" \
  "v_mul_u32_u24 v21, 640, v21\n" \
  "v_lshl_add_u32 v21, v20, 4, v21\n" \
  "s_add_u32 m0, s83, 5120\n" \
  "s_nop 0\n" \
  "global_load_lds_dwordx4 v21, s[70:71]\n" \
  "v_add_u32 v20, 384, v19\n" \
  "v_mul_u32_u24 v21, 1639, v20\n" \
  "v_lshrrev_b32 v21, 16, v21\n" \
  "v_mul_u32_u24 v21, 640, v21\n" \
  "v_lshl_add_u32 v21, v20, 4, v21\n" \
  "s_add_u32 m0, s83, 6144\n" \
  "s_nop 0\n" \
  "global_load_lds_dwordx4 v21, s[70:71]\n" \
  "v_add_u32 v20, 448, v19\n" \
  "v_mul_u32_u24 v21, 1639, v20\n" \
  "v_lshrrev_b32 v21, 16, v21\n" \
  "v_mul_u32_u24 v21, 640, v21\n" \
  "v_lshl_add_u32 v21, v20, 4, v21\n" \
  "s_add_u32 m0, s83, 7168\n" \
  "s_nop 0\n" \
  "global_load_lds_dwordx4 v21, s[70:71]\n" \
  "v_add_u32 v20, 512, v19\n" \
  "v_mul_u32_u24 v21, 1639, v20\n" \
  "v_lshrrev_b32 v21, 16, v21\n" \
  "v_mul_u32_u24 v21, 640, v21\n" \
  "v_lshl_add_u32 v21, v20, 4, v21\n" \
  "s_add_u32 m0, s83, 8192\n" \
  "s_nop 0\n" \
  "global_load_lds_dwordx4 v21, s[70:71]\n" \
  "v_add_u32 v20, 576, v19\n" \
  "v_mul_u32_u24 v21, 1639, v20\n" \
  "v_lshrrev_b32 v21, 16, v21\n" \
  "v_mul_u32_u24 v21, 640, v21\n" \
  "v_lshl_add_u32 v21, v20, 4, v21\n" \
  "s_add_u32 m0, s83, 9216\n" \
  "s_nop 0\n" \
  "global_load_lds_dwordx4 v21, s[70:71]\n" \
  "s_getreg_b32 s82, hwreg(20, 0, 3)\n" \
  "s_or_b32 s82, s82, 0x3c96e1a8\n" \
  "v_mov_b32 v40, 0x5a17c0de\n" \
  "v_mov_b32 v41, s82\n" \
  "s_lshl_b32 s82, %[b], 6\n" \
  "s_lshl_b32 s83, s25, 3\n" \
  "s_add_u32 s82, s82, s83\n" \
  "s_add_u32 s82, s82, 12615680\n" \
  "s_waitcnt vmcnt(10)\n" \
  "buffer_store_dwordx2 v[40:41], off, s[36:39], s82 sc1\n" \
  "s_add_u32 s80, s64, 0\n" \
  "s_addc_u32 s81, s65, 0\n" \
  "s_add_u32 m0, s56, 40960\n" \
  "s_nop 0\n" \
  "global_load_lds_dwordx4 v0, s[80:81]\n" \
  "s_add_u32 s80, s66, 0\n" \
  "s_addc_u32 s81, s67, 0\n" \
  "s_add_u32 m0, s56, 57344\n" \
  "s_nop 0\n" \
  "global_load_lds_dwordx4 v0, s[80:81]\n" \
  "s_add_u32 s80, s64, 32768\n" \
  "s_addc_u32 s81, s65, 0\n" \
  "s_add_u32 m0, s56, 41984\n" \
  "s_nop 0\n" \
  "global_load_lds_dwordx4 v1, s[80:81]\n" \
  "s_add_u32 s80, s66, 32768\n" \
  "s_addc_u32 s81, s67, 0\n" \
  "s_add_u32 m0, s56, 58368\n" \
  "s_nop 0\n" \
  "global_load_lds_dwordx4 v1, s[80:81]\n" \
  "s_add_u32 s80, s64, 65536\n" \
  "s_addc_u32 s81, s65, 0\n" \
  "s_add_u32 m0, s56, 43008\n" \
  "s_nop 0\n" \
  "global_load_lds_dwordx4 v2, s[80:81]\n" \
  "s_add_u32 s80, s66, 65536\n" \
  "s_addc_u32 s81, s67, 0\n" \
  "s_add_u32 m0, s56, 59392\n" \
  "s_nop 0\n" \
  "global_load_lds_dwordx4 v2, s[80:81]\n" \
  "s_add_u32 s80, s64, 98304\n" \
  "s_addc_u32 s81, s65, 0\n" \
  "s_add_u32 m0, s56, 44032\n" \
  "s_nop 0\n" \
  "global_load_lds_dwordx4 v3, s[80:81]\n" \
  "s_add_u32 s80, s66, 98304\n" \
  "s_addc_u32 s81, s67, 0\n" \
  "s_add_u32 m0, s56, 60416\n" \
  "s_nop 0\n" \
  "global_load_lds_dwordx4 v3, s[80:81]\n" \
  "s_add_u32 s80, s68, 0\n" \
  "s_addc_u32 s81, s69, 0\n" \
  "s_add_u32 m0, s57, 73728\n" \
  "s_nop 0\n" \
  "global_load_lds_dwordx4 v4, s[80:81]\n" \
  "s_add_u32 s80, s68, 65536\n" \
  "s_addc_u32 s81, s69, 0\n" \
  "s_add_u32 m0, s57, 74752\n" \
  "s_nop 0\n" \
  "global_load_lds_dwordx4 v5, s[80:81]\n" \
  "s_add_u32 s80, s64, 524288\n" \
  "s_addc_u32 s81, s65, 0\n" \
  "s_add_u32 m0, s56, 81920\n" \
  "s_nop 0\n" \
  "global_load_lds_dwordx4 v0, s[80:81]\n" \
  "s_add_u32 s80, s66, 524288\n" \
  "s_addc_u32 s81, s67, 0\n" \
  "s_add_u32 m0, s56, 98304\n" \
  "s_nop 0\n" \
  "global_load_lds_dwordx4 v0, s[80:81]\n" \
  "s_add_u32 s80, s64, 557056\n" \
  "s_addc_u32 s81, s65, 0\n" \
  "s_add_u32 m0, s56, 82944\n" \
  "s_nop 0\n" \
  "global_load_lds_dwordx4 v1, s[80:81]\n" \
  "s_add_u32 s80, s66, 557056\n" \
  "s_addc_u32 s81, s67, 0\n" \
  "s_add_u32 m0, s56, 99328\n" \
  "s_nop 0\n" \
  "global_load_lds_dwordx4 v1, s[80:81]\n" \
  "s_add_u32 s80, s64, 589824\n" \
  "s_addc_u32 s81, s65, 0\n" \
  "s_add_u32 m0, s56, 83968\n" \
  "s_nop 0\n" \
  "global_load_lds_dwordx4 v2, s[80:81]\n" \
  "s_add_u32 s80, s66, 589824\n" \
  "s_addc_u32 s81, s67, 0\n" \
  "s_add_u32 m0, s56, 100352\n" \
  "s_nop 0\n" \
  "global_load_lds_dwordx4 v2, s[80:81]\n" \
  "s_add_u32 s80, s64, 622592\n" \
  "s_addc_u32 s81, s65, 0\n" \
  "s_add_u32 m0, s56, 84992\n" \
  "s_nop 0\n" \
  "global_load_lds_dwordx4 v3, s[80:81]\n" \
  "s_add_u32 s80, s66, 622592\n" \
  "s_addc_u32 s81, s67, 0\n" \
  "s_add_u32 m0, s56, 101376\n" \
  "s_nop 0\n" \
  "global_load_lds_dwordx4 v3, s[80:81]\n" \
  "s_add_u32 s80, s68, 524288\n" \
  "s_addc_u32 s81, s69, 0\n" \
  "s_add_u32 m0, s57, 114688\n" \
  "s_nop 0\n" \
  "global_load_lds_dwordx4 v4, s[80:81]\n" \
  "s_add_u32 s80, s68, 589824\n" \
  "s_addc_u32 s81, s69, 0\n" \
  "s_add_u32 m0, s57, 115712\n" \
  "s_nop 0\n" \
  "global_load_lds_dwordx4 v5, s[80:81]\n" \
  "s_add_u32 s80, s64, 1048576\n" \
  "s_addc_u32 s81, s65, 0\n" \
  "s_add_u32 m0, s56, 122880\n" \
  "s_nop 0\n" \
  "global_load_lds_dwordx4 v0, s[80:81]\n" \
  "s_add_u32 s80, s66, 1048576\n" \
  "s_addc_u32 s81, s67, 0\n" \
  "s_add_u32 m0, s56, 139264\n" \
  "s_nop 0\n" \
  "global_load_lds_dwordx4 v0, s[80:81]\n" \
  "s_add_u32 s80, s64, 1081344\n" \
  "s_addc_u32 s81, s65, 0\n" \
  "s_add_u32 m0, s56, 123904\n" \
  "s_nop 0\n" \
  "global_load_lds_dwordx4 v1, s[80:81]\n" \
  "s_add_u32 s80, s66, 1081344\n" \
  "s_addc_u32 s81, s67, 0\n" \
  "s_add_u32 m0, s56, 140288\n" \
  "s_nop 0\n" \
  "global_load_lds_dwordx4 v1, s[80:81]\n" \
  "s_add_u32 s80, s64, 1114112\n" \
  "s_addc_u32 s81, s65, 0\n" \
  "s_add_u32 m0, s56, 124928\n" \
  "s_nop 0\n" \
  "global_load_lds_dwordx4 v2, s[80:81]\n" \
  "s_add_u32 s80, s66, 1114112\n" \
  "s_addc_u32 s81, s67, 0\n" \
  "s_add_u32 m0, s56, 141312\n" \
  "s_nop 0\n" \
  "global_load_lds_dwordx4 v2, s[80:81]\n" \
  "s_add_u32 s80, s64, 1146880\n" \
  "s_addc_u32 s81, s65, 0\n" \
  "s_add_u32 m0, s56, 125952\n" \
  "s_nop 0\n" \
  "global_load_lds_dwordx4 v3, s[80:81]\n" \
  "s_add_u32 s80, s66, 1146880\n" \
  "s_addc_u32 s81, s67, 0\n" \
  "s_add_u32 m0, s56, 142336\n" \
  "s_nop 0\n" \
  "global_load_lds_dwordx4 v3, s[80:81]\n" \
  "s_add_u32 s80, s68, 1048576\n" \
  "s_addc_u32 s81, s69, 0\n" \
  "s_add_u32 m0, s57, 155648\n" \
  "s_nop 0\n" \
  "global_load_lds_dwordx4 v4, s[80:81]\n" \
  "s_add_u32 s80, s68, 1114112\n" \
  "s_addc_u32 s81, s69, 0\n" \
  "s_add_u32 m0, s57, 156672\n" \
  "s_nop 0\n" \
  "global_load_lds_dwordx4 v5, s[80:81]\n" \
  "v_bfe_u32 v19, v16, 2, 1\n" \
  "v_bfe_u32 v20, v16, 3, 1\n" \
  "v_bfe_u32 v21, v16, 4, 1\n" \
  "v_lshrrev_b32 v22, 5, v16\n" \
  "v_lshl_add_u32 v23, v21, 2, v19\n" \
  "v_lshl_add_u32 v23, v20, 3, v23\n" \
  "v_lshl_add_u32 v23, v22, 4, v23\n" \
  "v_lshl_add_u32 v24, v20, 1, v21\n" \
  "v_lshl_add_u32 v25, v24, 1, v19\n" \
  "v_and_b32 v26, 3, v16\n" \
  "v_lshlrev_b32 v26, 3, v26\n" \
  "s_lshl_b32 s82, s25, 1\n" \
  "v_xor_b32 v25, s82, v25\n" \
  "v_lshl_add_u32 v6, v23, 8, v26\n" \
  "v_lshl_add_u32 v6, v25, 5, v6\n" \
  "v_xor_b32 v7, 32, v6\n" \
  "v_xor_b32 v24, s25, v24\n" \
  "v_lshl_add_u32 v8, v23, 7, v26\n" \
  "v_lshl_add_u32 v8, v24, 5, v8\n" \
  "v_add_u32 v8, 32768, v8\n" \
  "v_lshrrev_b32 v19, 2, v16\n" \
  "v_mul_u32_u24 v9, 640, v19\n" \
  "v_add_u32 v9, v9, v26\n" \
  "s_mul_i32 s82, s25, 160\n" \
  "v_add_u32 v9, s82, v9\n" \
  "v_lshlrev_b32 v15, 4, v16\n" \
  "v_add_u32 v15, s27, v15\n" \
  "s_mov_b32 s54, 0\n" \
  "s_waitcnt vmcnt(30) lgkmcnt(0)\n" \
  "s_barrier\n" \
  "ds_read_b64_tr_b16 a[0:1], v9 offset:0\n" \
  "ds_read_b64_tr_b16 a[2:3], v9 offset:10240\n" \
  "ds_read_b64_tr_b16 a[4:5], v9 offset:20480\n" \
  "ds_read_b64_tr_b16 a[6:7], v9 offset:30720\n" \
  "ds_read_b64_tr_b16 a[8:9], v9 offset:32\n" \
  "ds_read_b64_tr_b16 a[10:11], v9 offset:10272\n" \
  "ds_read_b64_tr_b16 a[12:13], v9 offset:20512\n" \
  "ds_read_b64_tr_b16 a[14:15], v9 offset:30752\n" \
  "ds_read_b64_tr_b16 a[16:17], v9 offset:64\n" \
  "ds_read_b64_tr_b16 a[18:19], v9 offset:10304\n" \
  "ds_read_b64_tr_b16 a[20:21], v9 offset:20544\n" \
  "ds_read_b64_tr_b16 a[22:23], v9 offset:30784\n" \
  "ds_read_b64_tr_b16 a[24:25], v9 offset:96\n" \
  "ds_read_b64_tr_b16 a[26:27], v9 offset:10336\n" \
  "ds_read_b64_tr_b16 a[28:29], v9 offset:20576\n" \
  "ds_read_b64_tr_b16 a[30:31], v9 offset:30816\n" \
  "ds_read_b64_tr_b16 a[32:33], v9 offset:128\n" \
  "ds_read_b64_tr_b16 a[34:35], v9 offset:10368\n" \
  "ds_read_b64_tr_b16 a[36:37], v9 offset:20608\n" \
  "ds_read_b64_tr_b16 a[38:39], v9 offset:30848\n" \
  "v_mov_b32 v61, 0x4444\n" \
  "s_waitcnt vmcnt(20) lgkmcnt(0)\n" \
  "s_barrier\n" \
  "s_add_u32 s80, s64, 1572864\n" \
  "s_addc_u32 s81, s65, 0\n" \
  "s_add_u32 m0, s56, 0\n" \
  "s_nop 0\n" \
  "global_load_lds_dwordx4 v0, s[80:81]\n" \
  "s_add_u32 s80, s66, 1572864\n" \
  "s_addc_u32 s81, s67, 0\n" \
  "s_add_u32 m0, s56, 16384\n" \
  "s_nop 0\n" \
  "global_load_lds_dwordx4 v0, s[80:81]\n" \
  "s_add_u32 s80, s64, 1605632\n" \
  "s_addc_u32 s81, s65, 0\n" \
  "s_add_u32 m0, s56, 1024\n" \
  "s_nop 0\n" \
  "global_load_lds_dwordx4 v1, s[80:81]\n" \
  "s_add_u32 s80, s66, 1605632\n" \
  "s_addc_u32 s81, s67, 0\n" \
  "s_add_u32 m0, s56, 17408\n" \
  "s_nop 0\n" \
  "global_load_lds_dwordx4 v1, s[80:81]\n" \
  "s_add_u32 s80, s64, 1638400\n" \
  "s_addc_u32 s81, s65, 0\n" \
  "s_add_u32 m0, s56, 2048\n" \
  "s_nop 0\n" \
  "global_load_lds_dwordx4 v2, s[80:81]\n" \
  "s_add_u32 s80, s66, 1638400\n" \
  "s_addc_u32 s81, s67, 0\n" \
  "s_add_u32 m0, s56, 18432\n" \
  "s_nop 0\n" \
  "global_load_lds_dwordx4 v2, s[80:81]\n" \
  "s_add_u32 s80, s64, 1671168\n" \
  "s_addc_u32 s81, s65, 0\n" \
  "s_add_u32 m0, s56, 3072\n" \
  "s_nop 0\n" \
  "global_load_lds_dwordx4 v3, s[80:81]\n" \
  "s_add_u32 s80, s66, 1671168\n" \
  "s_addc_u32 s81, s67, 0\n" \
  "s_add_u32 m0, s56, 19456\n" \
  "s_nop 0\n" \
  "global_load_lds_dwordx4 v3, s[80:81]\n" \
  "s_add_u32 s80, s68, 1572864\n" \
  "s_addc_u32 s81, s69, 0\n" \
  "s_add_u32 m0, s57, 32768\n" \
  "s_nop 0\n" \
  "global_load_lds_dwordx4 v4, s[80:81]\n" \
  "s_add_u32 s80, s68, 1638400\n" \
  "s_addc_u32 s81, s69, 0\n" \
  "s_add_u32 m0, s57, 33792\n" \
  "s_nop 0\n" \
  "global_load_lds_dwordx4 v5, s[80:81]\n" \
  "v_add_u32 v12, 40960, v6\n" \
  "v_add_u32 v13, 40960, v7\n" \
  "v_add_u32 v14, 40960, v8\n" \
  "ds_read_b64_tr_b16 v[72:73], v12 offset:0\n" \
  "ds_read_b64_tr_b16 v[74:75], v12 offset:512\n" \
  "ds_read_b64_tr_b16 v[76:77], v12 offset:8192\n" \
  "ds_read_b64_tr_b16 v[78:79], v12 offset:8704\n" \
  "ds_read_b64_tr_b16 a[80:81], v13 offset:0\n" \
  "ds_read_b64_tr_b16 a[82:83], v13 offset:512\n" \
  "ds_read_b64_tr_b16 a[84:85], v13 offset:8192\n" \
  "ds_read_b64_tr_b16 a[86:87], v13 offset:8704\n" \
  "ds_read_b64_tr_b16 a[160:161], v12 offset:16384\n" \
  "ds_read_b64_tr_b16 a[162:163], v12 offset:16896\n" \
  "ds_read_b64_tr_b16 a[164:165], v12 offset:24576\n" \
  "ds_read_b64_tr_b16 a[166:167], v12 offset:25088\n" \
  "ds_read_b64_tr_b16 a[240:241], v13 offset:16384\n" \
  "ds_read_b64_tr_b16 a[242:243], v13 offset:16896\n" \
  "ds_read_b64_tr_b16 a[244:245], v13 offset:24576\n" \
  "ds_read_b64_tr_b16 a[246:247], v13 offset:25088\n" \
  "ds_read_b64_tr_b16 v[176:177], v14 offset:0\n" \
  "ds_read_b64_tr_b16 v[178:179], v14 offset:256\n" \
  "ds_read_b64_tr_b16 v[180:181], v14 offset:4096\n" \
  "ds_read_b64_tr_b16 v[182:183], v14 offset:4352\n" \
  "s_waitcnt vmcnt(20) lgkmcnt(0)\n" \
  "s_barrier\n" \
  "s_add_u32 s80, s64, 2097152\n" \
  "s_addc_u32 s81, s65, 0\n" \
  "s_add_u32 m0, s56, 40960\n" \
  "s_nop 0\n" \
  "global_load_lds_dwordx4 v0, s[80:81]\n" \
  "s_add_u32 s80, s66, 2097152\n" \
  "s_addc_u32 s81, s67, 0\n" \
  "s_add_u32 m0, s56, 57344\n" \
  "s_nop 0\n" \
  "global_load_lds_dwordx4 v0, s[80:81]\n" \
  "s_add_u32 s80, s64, 2129920\n" \
  "s_addc_u32 s81, s65, 0\n" \
  "s_add_u32 m0, s56, 41984\n" \
  "s_nop 0\n" \
  "global_load_lds_dwordx4 v1, s[80:81]\n" \
  "s_add_u32 s80, s66, 2129920\n" \
  "s_addc_u32 s81, s67, 0\n" \
  "s_add_u32 m0, s56, 58368\n" \
  "s_nop 0\n" \
  "global_load_lds_dwordx4 v1, s[80:81]\n" \
  "s_add_u32 s80, s64, 2162688\n" \
  "s_addc_u32 s81, s65, 0\n" \
  "s_add_u32 m0, s56, 43008\n" \
  "s_nop 0\n" \
  "global_load_lds_dwordx4 v2, s[80:81]\n" \
  "s_add_u32 s80, s66, 2162688\n" \
  "s_addc_u32 s81, s67, 0\n" \
  "s_add_u32 m0, s56, 59392\n" \
  "s_nop 0\n" \
  "global_load_lds_dwordx4 v2, s[80:81]\n" \
  "s_add_u32 s80, s64, 2195456\n" \
  "s_addc_u32 s81, s65, 0\n" \
  "s_add_u32 m0, s56, 44032\n" \
  "s_nop 0\n" \
  "global_load_lds_dwordx4 v3, s[80:81]\n" \
  "s_add_u32 s80, s66, 2195456\n" \
  "s_addc_u32 s81, s67, 0\n" \
  "s_add_u32 m0, s56, 60416\n" \
  "s_nop 0\n" \
  "global_load_lds_dwordx4 v3, s[80:81]\n" \
  "s_add_u32 s80, s68, 2097152\n" \
  "s_addc_u32 s81, s69, 0\n" \
  "s_add_u32 m0, s57, 73728\n" \
  "s_nop 0\n" \
  "global_load_lds_dwordx4 v4, s[80:81]\n" \
  "s_add_u32 s80, s68, 2162688\n" \
  "s_addc_u32 s81, s69, 0\n" \
  "s_add_u32 m0, s57, 74752\n" \
  "s_nop 0\n" \
  "global_load_lds_dwordx4 v5, s[80:81]\n" \
  "v_add_u32 v12, 81920, v6\n" \
  "v_add_u32 v13, 81920, v7\n" \
  "v_add_u32 v14, 81920, v8\n" \
  "ds_read_b64_tr_b16 v[80:81], v12 offset:0\n" \
  "ds_read_b64_tr_b16 v[82:83], v12 offset:512\n" \
  "ds_read_b64_tr_b16 v[84:85], v12 offset:8192\n" \
  "ds_read_b64_tr_b16 v[86:87], v12 offset:8704\n" \
  "ds_read_b64_tr_b16 a[88:89], v13 offset:0\n" \
  "ds_read_b64_tr_b16 a[90:91], v13 offset:512\n" \
  "ds_read_b64_tr_b16 a[92:93], v13 offset:8192\n" \
  "ds_read_b64_tr_b16 a[94:95], v13 offset:8704\n" \
  "ds_read_b64_tr_b16 a[168:169], v12 offset:16384\n" \
  "ds_read_b64_tr_b16 a[170:171], v12 offset:16896\n" \
  "ds_read_b64_tr_b16 a[172:173], v12 offset:24576\n" \
  "ds_read_b64_tr_b16 a[174:175], v12 offset:25088\n" \
  "ds_read_b64_tr_b16 a[248:249], v13 offset:16384\n" \
  "ds_read_b64_tr_b16 a[250:251], v13 offset:16896\n" \
  "ds_read_b64_tr_b16 a[252:253], v13 offset:24576\n" \
  "ds_read_b64_tr_b16 a[254:255], v13 offset:25088\n" \
  "ds_read_b64_tr_b16 v[184:185], v14 offset:0\n" \
  "ds_read_b64_tr_b16 v[186:187], v14 offset:256\n" \
  "ds_read_b64_tr_b16 v[188:189], v14 offset:4096\n" \
  "ds_read_b64_tr_b16 v[190:191], v14 offset:4352\n" \
  "s_waitcnt vmcnt(20) lgkmcnt(0)\n" \
  "s_barrier\n" \
  "s_add_u32 s80, s64, 2621440\n" \
  "s_addc_u32 s81, s65, 0\n" \
  "s_add_u32 m0, s56, 81920\n" \
  "s_nop 0\n" \
  "global_load_lds_dwordx4 v0, s[80:81]\n" \
  "s_add_u32 s80, s66, 2621440\n" \
  "s_addc_u32 s81, s67, 0\n" \
  "s_add_u32 m0, s56, 98304\n" \
  "s_nop 0\n" \
  "global_load_lds_dwordx4 v0, s[80:81]\n" \
  "s_add_u32 s80, s64, 2654208\n" \
  "s_addc_u32 s81, s65, 0\n" \
  "s_add_u32 m0, s56, 82944\n" \
  "s_nop 0\n" \
  "global_load_lds_dwordx4 v1, s[80:81]\n" \
  "s_add_u32 s80, s66, 2654208\n" \
  "s_addc_u32 s81, s67, 0\n" \
  "s_add_u32 m0, s56, 99328\n" \
  "s_nop 0\n" \
  "global_load_lds_dwordx4 v1, s[80:81]\n" \
  "s_add_u32 s80, s64, 2686976\n" \
  "s_addc_u32 s81, s65, 0\n" \
  "s_add_u32 m0, s56, 83968\n" \
  "s_nop 0\n" \
  "global_load_lds_dwordx4 v2, s[80:81]\n" \
  "s_add_u32 s80, s66, 2686976\n" \
  "s_addc_u32 s81, s67, 0\n" \
  "s_add_u32 m0, s56, 100352\n" \
  "s_nop 0\n" \
  "global_load_lds_dwordx4 v2, s[80:81]\n" \
  "s_add_u32 s80, s64, 2719744\n" \
  "s_addc_u32 s81, s65, 0\n" \
  "s_add_u32 m0, s56, 84992\n" \
  "s_nop 0\n" \
  "global_load_lds_dwordx4 v3, s[80:81]\n" \
  "s_add_u32 s80, s66, 2719744\n" \
  "s_addc_u32 s81, s67, 0\n" \
  "s_add_u32 m0, s56, 101376\n" \
  "s_nop 0\n" \
  "global_load_lds_dwordx4 v3, s[80:81]\n" \
  "s_add_u32 s80, s68, 2621440\n" \
  "s_addc_u32 s81, s69, 0\n" \
  "s_add_u32 m0, s57, 114688\n" \
  "s_nop 0\n" \
  "global_load_lds_dwordx4 v4, s[80:81]\n" \
  "s_add_u32 s80, s68, 2686976\n" \
  "s_addc_u32 s81, s69, 0\n" \
  "s_add_u32 m0, s57, 115712\n" \
  "s_nop 0\n" \
  "global_load_lds_dwordx4 v5, s[80:81]\n" \
  "v_add_u32 v12, 122880, v6\n" \
  "v_add_u32 v13, 122880, v7\n" \
  "v_add_u32 v14, 122880, v8\n" \
  "ds_read_b64_tr_b16 v[88:89], v12 offset:0\n" \
  "ds_read_b64_tr_b16 v[90:91], v12 offset:512\n" \
  "ds_read_b64_tr_b16 v[92:93], v12 offset:8192\n" \
  "ds_read_b64_tr_b16 v[94:95], v12 offset:8704\n" \
  "ds_read_b64_tr_b16 a[96:97], v13 offset:0\n" \
  "ds_read_b64_tr_b16 a[98:99], v13 offset:512\n" \
  "ds_read_b64_tr_b16 a[100:101], v13 offset:8192\n" \
  "ds_read_b64_tr_b16 a[102:103], v13 offset:8704\n" \
  "ds_read_b64_tr_b16 a[176:177], v12 offset:16384\n" \
  "ds_read_b64_tr_b16 a[178:179], v12 offset:16896\n" \
  "ds_read_b64_tr_b16 a[180:181], v12 offset:24576\n" \
  "ds_read_b64_tr_b16 a[182:183], v12 offset:25088\n" \
  "ds_read_b64_tr_b16 v[112:113], v13 offset:16384\n" \
  "ds_read_b64_tr_b16 v[114:115], v13 offset:16896\n" \
  "ds_read_b64_tr_b16 v[116:117], v13 offset:24576\n" \
  "ds_read_b64_tr_b16 v[118:119], v13 offset:25088\n" \
  "ds_read_b64_tr_b16 v[192:193], v14 offset:0\n" \
  "ds_read_b64_tr_b16 v[194:195], v14 offset:256\n" \
  "ds_read_b64_tr_b16 v[196:197], v14 offset:4096\n" \
  "ds_read_b64_tr_b16 v[198:199], v14 offset:4352\n" \
  "s_waitcnt vmcnt(20) lgkmcnt(0)\n" \
  "s_barrier\n" \
  "s_add_u32 s80, s64, 3145728\n" \
  "s_addc_u32 s81, s65, 0\n" \
  "s_add_u32 m0, s56, 122880\n" \
  "s_nop 0\n" \
  "global_load_lds_dwordx4 v0, s[80:81]\n" \
  "s_add_u32 s80, s66, 3145728\n" \
  "s_addc_u32 s81, s67, 0\n" \
  "s_add_u32 m0, s56, 139264\n" \
  "s_nop 0\n" \
  "global_load_lds_dwordx4 v0, s[80:81]\n" \
  "s_add_u32 s80, s64, 3178496\n" \
  "s_addc_u32 s81, s65, 0\n" \
  "s_add_u32 m0, s56, 123904\n" \
  "s_nop 0\n" \
  "global_load_lds_dwordx4 v1, s[80:81]\n" \
  "s_add_u32 s80, s66, 3178496\n" \
  "s_addc_u32 s81, s67, 0\n" \
  "s_add_u32 m0, s56, 140288\n" \
  "s_nop 0\n" \
  "global_load_lds_dwordx4 v1, s[80:81]\n" \
  "s_add_u32 s80, s64, 3211264\n" \
  "s_addc_u32 s81, s65, 0\n" \
  "s_add_u32 m0, s56, 124928\n" \
  "s_nop 0\n" \
  "global_load_lds_dwordx4 v2, s[80:81]\n" \
  "s_add_u32 s80, s66, 3211264\n" \
  "s_addc_u32 s81, s67, 0\n" \
  "s_add_u32 m0, s56, 141312\n" \
  "s_nop 0\n" \
  "global_load_lds_dwordx4 v2, s[80:81]\n" \
  "s_add_u32 s80, s64, 3244032\n" \
  "s_addc_u32 s81, s65, 0\n" \
  "s_add_u32 m0, s56, 125952\n" \
  "s_nop 0\n" \
  "global_load_lds_dwordx4 v3, s[80:81]\n" \
  "s_add_u32 s80, s66, 3244032\n" \
  "s_addc_u32 s81, s67, 0\n" \
  "s_add_u32 m0, s56, 142336\n" \
  "s_nop 0\n" \
  "global_load_lds_dwordx4 v3, s[80:81]\n" \
  "s_add_u32 s80, s68, 3145728\n" \
  "s_addc_u32 s81, s69, 0\n" \
  "s_add_u32 m0, s57, 155648\n" \
  "s_nop 0\n" \
  "global_load_lds_dwordx4 v4, s[80:81]\n" \
  "s_add_u32 s80, s68, 3211264\n" \
  "s_addc_u32 s81, s69, 0\n" \
  "s_add_u32 m0, s57, 156672\n" \
  "s_nop 0\n" \
  "global_load_lds_dwordx4 v5, s[80:81]\n" \
  "v_add_u32 v12, 0, v6\n" \
  "v_add_u32 v13, 0, v7\n" \
  "v_add_u32 v14, 0, v8\n" \
  "ds_read_b64_tr_b16 v[96:97], v12 offset:0\n" \
  "ds_read_b64_tr_b16 v[98:99], v12 offset:512\n" \
  "ds_read_b64_tr_b16 v[100:101], v12 offset:8192\n" \
  "ds_read_b64_tr_b16 v[102:103], v12 offset:8704\n" \
  "ds_read_b64_tr_b16 a[104:105], v13 offset:0\n" \
  "ds_read_b64_tr_b16 a[106:107], v13 offset:512\n" \
  "ds_read_b64_tr_b16 a[108:109], v13 offset:8192\n" \
  "ds_read_b64_tr_b16 a[110:111], v13 offset:8704\n" \
  "ds_read_b64_tr_b16 a[184:185], v12 offset:16384\n" \
  "ds_read_b64_tr_b16 a[186:187], v12 offset:16896\n" \
  "ds_read_b64_tr_b16 a[188:189], v12 offset:24576\n" \
  "ds_read_b64_tr_b16 a[190:191], v12 offset:25088\n" \
  "ds_read_b64_tr_b16 v[120:121], v13 offset:16384\n" \
  "ds_read_b64_tr_b16 v[122:123], v13 offset:16896\n" \
  "ds_read_b64_tr_b16 v[124:125], v13 offset:24576\n" \
  "ds_read_b64_tr_b16 v[126:127], v13 offset:25088\n" \
  "ds_read_b64_tr_b16 v[200:201], v14 offset:0\n" \
  "ds_read_b64_tr_b16 v[202:203], v14 offset:256\n" \
  "ds_read_b64_tr_b16 v[204:205], v14 offset:4096\n" \
  "ds_read_b64_tr_b16 v[206:207], v14 offset:4352\n" \
  "s_waitcnt vmcnt(20) lgkmcnt(0)\n" \
  "s_barrier\n" \
  "s_add_u32 s80, s64, 3670016\n" \
  "s_addc_u32 s81, s65, 0\n" \
  "s_add_u32 m0, s56, 0\n" \
  "s_nop 0\n" \
  "global_load_lds_dwordx4 v0, s[80:81]\n" \
  "s_add_u32 s80, s66, 3670016\n" \
  "s_addc_u32 s81, s67, 0\n" \
  "s_add_u32 m0, s56, 16384\n" \
  "s_nop 0\n" \
  "global_load_lds_dwordx4 v0, s[80:81]\n" \
  "s_add_u32 s80, s64, 3702784\n" \
  "s_addc_u32 s81, s65, 0\n" \
  "s_add_u32 m0, s56, 1024\n" \
  "s_nop 0\n" \
  "global_load_lds_dwordx4 v1, s[80:81]\n" \
  "s_add_u32 s80, s66, 3702784\n" \
  "s_addc_u32 s81, s67, 0\n" \
  "s_add_u32 m0, s56, 17408\n" \
  "s_nop 0\n" \
  "global_load_lds_dwordx4 v1, s[80:81]\n" \
  "s_add_u32 s80, s64, 3735552\n" \
  "s_addc_u32 s81, s65, 0\n" \
  "s_add_u32 m0, s56, 2048\n" \
  "s_nop 0\n" \
  "global_load_lds_dwordx4 v2, s[80:81]\n" \
  "s_add_u32 s80, s66, 3735552\n" \
  "s_addc_u32 s81, s67, 0\n" \
  "s_add_u32 m0, s56, 18432\n" \
  "s_nop 0\n" \
  "global_load_lds_dwordx4 v2, s[80:81]\n" \
  "s_add_u32 s80, s64, 3768320\n" \
  "s_addc_u32 s81, s65, 0\n" \
  "s_add_u32 m0, s56, 3072\n" \
  "s_nop 0\n" \
  "global_load_lds_dwordx4 v3, s[80:81]\n" \
  "s_add_u32 s80, s66, 3768320\n" \
  "s_addc_u32 s81, s67, 0\n" \
  "s_add_u32 m0, s56, 19456\n" \
  "s_nop 0\n" \
  "global_load_lds_dwordx4 v3, s[80:81]\n" \
  "s_add_u32 s80, s68, 3670016\n" \
  "s_addc_u32 s81, s69, 0\n" \
  "s_add_u32 m0, s57, 32768\n" \
  "s_nop 0\n" \
  "global_load_lds_dwordx4 v4, s[80:81]\n" \
  "s_add_u32 s80, s68, 3735552\n" \
  "s_addc_u32 s81, s69, 0\n" \
  "s_add_u32 m0, s57, 33792\n" \
  "s_nop 0\n" \
  "global_load_lds_dwordx4 v5, s[80:81]\n" \
  "v_add_u32 v12, 40960, v6\n" \
  "v_add_u32 v13, 40960, v7\n" \
  "v_add_u32 v14, 40960, v8\n" \
  "ds_read_b64_tr_b16 v[104:105], v12 offset:0\n" \
  "ds_read_b64_tr_b16 v[106:107], v12 offset:512\n" \
  "ds_read_b64_tr_b16 v[108:109], v12 offset:8192\n" \
  "ds_read_b64_tr_b16 v[110:111], v12 offset:8704\n" \
  "ds_read_b64_tr_b16 a[112:113], v13 offset:0\n" \
  "ds_read_b64_tr_b16 a[114:115], v13 offset:512\n" \
  "ds_read_b64_tr_b16 a[116:117], v13 offset:8192\n" \
  "ds_read_b64_tr_b16 a[118:119], v13 offset:8704\n" \
  "ds_read_b64_tr_b16 a[192:193], v12 offset:16384\n" \
  "ds_read_b64_tr_b16 a[194:195], v12 offset:16896\n" \
  "ds_read_b64_tr_b16 a[196:197], v12 offset:24576\n" \
  "ds_read_b64_tr_b16 a[198:199], v12 offset:25088\n" \
  "ds_read_b64_tr_b16 v[128:129], v13 offset:16384\n" \
  "ds_read_b64_tr_b16 v[130:131], v13 offset:16896\n" \
  "ds_read_b64_tr_b16 v[132:133], v13 offset:24576\n" \
  "ds_read_b64_tr_b16 v[134:135], v13 offset:25088\n" \
  "ds_read_b64_tr_b16 v[208:209], v14 offset:0\n" \
  "ds_read_b64_tr_b16 v[210:211], v14 offset:256\n" \
  "ds_read_b64_tr_b16 v[212:213], v14 offset:4096\n" \
  "ds_read_b64_tr_b16 v[214:215], v14 offset:4352\n" \
  "s_waitcnt vmcnt(20) lgkmcnt(0)\n" \
  "s_barrier\n" \
  "s_add_u32 s80, s64, 4194304\n" \
  "s_addc_u32 s81, s65, 0\n" \
  "s_add_u32 m0, s56, 40960\n" \
  "s_nop 0\n" \
  "global_load_lds_dwordx4 v0, s[80:81]\n" \
  "s_add_u32 s80, s66, 4194304\n" \
  "s_addc_u32 s81, s67, 0\n" \
  "s_add_u32 m0, s56, 57344\n" \
  "s_nop 0\n" \
  "global_load_lds_dwordx4 v0, s[80:81]\n" \
  "s_add_u32 s80, s64, 4227072\n" \
  "s_addc_u32 s81, s65, 0\n" \
  "s_add_u32 m0, s56, 41984\n" \
  "s_nop 0\n" \
  "global_load_lds_dwordx4 v1, s[80:81]\n" \
  "s_add_u32 s80, s66, 4227072\n" \
  "s_addc_u32 s81, s67, 0\n" \
  "s_add_u32 m0, s56, 58368\n" \
  "s_nop 0\n" \
  "global_load_lds_dwordx4 v1, s[80:81]\n" \
  "s_add_u32 s80, s64, 4259840\n" \
  "s_addc_u32 s81, s65, 0\n" \
  "s_add_u32 m0, s56, 43008\n" \
  "s_nop 0\n" \
  "global_load_lds_dwordx4 v2, s[80:81]\n" \
  "s_add_u32 s80, s66, 4259840\n" \
  "s_addc_u32 s81, s67, 0\n" \
  "s_add_u32 m0, s56, 59392\n" \
  "s_nop 0\n" \
  "global_load_lds_dwordx4 v2, s[80:81]\n" \
  "s_add_u32 s80, s64, 4292608\n" \
  "s_addc_u32 s81, s65, 0\n" \
  "s_add_u32 m0, s56, 44032\n" \
  "s_nop 0\n" \
  "global_load_lds_dwordx4 v3, s[80:81]\n" \
  "s_add_u32 s80, s66, 4292608\n" \
  "s_addc_u32 s81, s67, 0\n" \
  "s_add_u32 m0, s56, 60416\n" \
  "s_nop 0\n" \
  "global_load_lds_dwordx4 v3, s[80:81]\n" \
  "s_add_u32 s80, s68, 4194304\n" \
  "s_addc_u32 s81, s69, 0\n" \
  "s_add_u32 m0, s57, 73728\n" \
  "s_nop 0\n" \
  "global_load_lds_dwordx4 v4, s[80:81]\n" \
  "s_add_u32 s80, s68, 4259840\n" \
  "s_addc_u32 s81, s69, 0\n" \
  "s_add_u32 m0, s57, 74752\n" \
  "s_nop 0\n" \
  "global_load_lds_dwordx4 v5, s[80:81]\n" \
  "v_add_u32 v12, 81920, v6\n" \
  "v_add_u32 v13, 81920, v7\n" \
  "v_add_u32 v14, 81920, v8\n" \
  "ds_read_b64_tr_b16 a[40:41], v12 offset:0\n" \
  "ds_read_b64_tr_b16 a[42:43], v12 offset:512\n" \
  "ds_read_b64_tr_b16 a[44:45], v12 offset:8192\n" \
  "ds_read_b64_tr_b16 a[46:47], v12 offset:8704\n" \
  "ds_read_b64_tr_b16 a[120:121], v13 offset:0\n" \
  "ds_read_b64_tr_b16 a[122:123], v13 offset:512\n" \
  "ds_read_b64_tr_b16 a[124:125], v13 offset:8192\n" \
  "ds_read_b64_tr_b16 a[126:127], v13 offset:8704\n" \
  "ds_read_b64_tr_b16 a[200:201], v12 offset:16384\n" \
  "ds_read_b64_tr_b16 a[202:203], v12 offset:16896\n" \
  "ds_read_b64_tr_b16 a[204:205], v12 offset:24576\n" \
  "ds_read_b64_tr_b16 a[206:207], v12 offset:25088\n" \
  "ds_read_b64_tr_b16 v[136:137], v13 offset:16384\n" \
  "ds_read_b64_tr_b16 v[138:139], v13 offset:16896\n" \
  "ds_read_b64_tr_b16 v[140:141], v13 offset:24576\n" \
  "ds_read_b64_tr_b16 v[142:143], v13 offset:25088\n" \
  "ds_read_b64_tr_b16 v[216:217], v14 offset:0\n" \
  "ds_read_b64_tr_b16 v[218:219], v14 offset:256\n" \
  "ds_read_b64_tr_b16 v[220:221], v14 offset:4096\n" \
  "ds_read_b64_tr_b16 v[222:223], v14 offset:4352\n" \
  "s_waitcnt vmcnt(20) lgkmcnt(0)\n" \
  "s_barrier\n" \
  "s_add_u32 s80, s64, 4718592\n" \
  "s_addc_u32 s81, s65, 0\n" \
  "s_add_u32 m0, s56, 81920\n" \
  "s_nop 0\n" \
  "global_load_lds_dwordx4 v0, s[80:81]\n" \
  "s_add_u32 s80, s66, 4718592\n" \
  "s_addc_u32 s81, s67, 0\n" \
  "s_add_u32 m0, s56, 98304\n" \
  "s_nop 0\n" \
  "global_load_lds_dwordx4 v0, s[80:81]\n" \
  "s_add_u32 s80, s64, 4751360\n" \
  "s_addc_u32 s81, s65, 0\n" \
  "s_add_u32 m0, s56, 82944\n" \
  "s_nop 0\n" \
  "global_load_lds_dwordx4 v1, s[80:81]\n" \
  "s_add_u32 s80, s66, 4751360\n" \
  "s_addc_u32 s81, s67, 0\n" \
  "s_add_u32 m0, s56, 99328\n" \
  "s_nop 0\n" \
  "global_load_lds_dwordx4 v1, s[80:81]\n" \
  "s_add_u32 s80, s64, 4784128\n" \
  "s_addc_u32 s81, s65, 0\n" \
  "s_add_u32 m0, s56, 83968\n" \
  "s_nop 0\n" \
  "global_load_lds_dwordx4 v2, s[80:81]\n" \
  "s_add_u32 s80, s66, 4784128\n" \
  "s_addc_u32 s81, s67, 0\n" \
  "s_add_u32 m0, s56, 100352\n" \
  "s_nop 0\n" \
  "global_load_lds_dwordx4 v2, s[80:81]\n" \
  "s_add_u32 s80, s64, 4816896\n" \
  "s_addc_u32 s81, s65, 0\n" \
  "s_add_u32 m0, s56, 84992\n" \
  "s_nop 0\n" \
  "global_load_lds_dwordx4 v3, s[80:81]\n" \
  "s_add_u32 s80, s66, 4816896\n" \
  "s_addc_u32 s81, s67, 0\n" \
  "s_add_u32 m0, s56, 101376\n" \
  "s_nop 0\n" \
  "global_load_lds_dwordx4 v3, s[80:81]\n" \
  "s_add_u32 s80, s68, 4718592\n" \
  "s_addc_u32 s81, s69, 0\n" \
  "s_add_u32 m0, s57, 114688\n" \
  "s_nop 0\n" \
  "global_load_lds_dwordx4 v4, s[80:81]\n" \
  "s_add_u32 s80, s68, 4784128\n" \
  "s_addc_u32 s81, s69, 0\n" \
  "s_add_u32 m0, s57, 115712\n" \
  "s_nop 0\n" \
  "global_load_lds_dwordx4 v5, s[80:81]\n" \
  "v_add_u32 v12, 122880, v6\n" \
  "v_add_u32 v13, 122880, v7\n" \
  "v_add_u32 v14, 122880, v8\n" \
  "ds_read_b64_tr_b16 a[48:49], v12 offset:0\n" \
  "ds_read_b64_tr_b16 a[50:51], v12 offset:512\n" \
  "ds_read_b64_tr_b16 a[52:53], v12 offset:8192\n" \
  "ds_read_b64_tr_b16 a[54:55], v12 offset:8704\n" \
  "ds_read_b64_tr_b16 a[128:129], v13 offset:0\n" \
  "ds_read_b64_tr_b16 a[130:131], v13 offset:512\n" \
  "ds_read_b64_tr_b16 a[132:133], v13 offset:8192\n" \
  "ds_read_b64_tr_b16 a[134:135], v13 offset:8704\n" \
  "ds_read_b64_tr_b16 a[208:209], v12 offset:16384\n" \
  "ds_read_b64_tr_b16 a[210:211], v12 offset:16896\n" \
  "ds_read_b64_tr_b16 a[212:213], v12 offset:24576\n" \
  "ds_read_b64_tr_b16 a[214:215], v12 offset:25088\n" \
  "ds_read_b64_tr_b16 v[144:145], v13 offset:16384\n" \
  "ds_read_b64_tr_b16 v[146:147], v13 offset:16896\n" \
  "ds_read_b64_tr_b16 v[148:149], v13 offset:24576\n" \
  "ds_read_b64_tr_b16 v[150:151], v13 offset:25088\n" \
  "ds_read_b64_tr_b16 v[224:225], v14 offset:0\n" \
  "ds_read_b64_tr_b16 v[226:227], v14 offset:256\n" \
  "ds_read_b64_tr_b16 v[228:229], v14 offset:4096\n" \
  "ds_read_b64_tr_b16 v[230:231], v14 offset:4352\n" \
  "s_waitcnt vmcnt(20) lgkmcnt(0)\n" \
  "s_barrier\n" \
  "v_add_u32 v12, 0, v6\n" \
  "v_add_u32 v13, 0, v7\n" \
  "v_add_u32 v14, 0, v8\n" \
  "ds_read_b64_tr_b16 a[56:57], v12 offset:0\n" \
  "ds_read_b64_tr_b16 a[58:59], v12 offset:512\n" \
  "ds_read_b64_tr_b16 a[60:61], v12 offset:8192\n" \
  "ds_read_b64_tr_b16 a[62:63], v12 offset:8704\n" \
  "ds_read_b64_tr_b16 a[136:137], v13 offset:0\n" \
  "ds_read_b64_tr_b16 a[138:139], v13 offset:512\n" \
  "ds_read_b64_tr_b16 a[140:141], v13 offset:8192\n" \
  "ds_read_b64_tr_b16 a[142:143], v13 offset:8704\n" \
  "ds_read_b64_tr_b16 a[216:217], v12 offset:16384\n" \
  "ds_read_b64_tr_b16 a[218:219], v12 offset:16896\n" \
  "ds_read_b64_tr_b16 a[220:221], v12 offset:24576\n" \
  "ds_read_b64_tr_b16 a[222:223], v12 offset:25088\n" \
  "ds_read_b64_tr_b16 v[152:153], v13 offset:16384\n" \
  "ds_read_b64_tr_b16 v[154:155], v13 offset:16896\n" \
  "ds_read_b64_tr_b16 v[156:157], v13 offset:24576\n" \
  "ds_read_b64_tr_b16 v[158:159], v13 offset:25088\n" \
  "ds_read_b64_tr_b16 v[232:233], v14 offset:0\n" \
  "ds_read_b64_tr_b16 v[234:235], v14 offset:256\n" \
  "ds_read_b64_tr_b16 v[236:237], v14 offset:4096\n" \
  "ds_read_b64_tr_b16 v[238:239], v14 offset:4352\n" \
  "s_waitcnt vmcnt(10) lgkmcnt(0)\n" \
  "s_barrier\n" \
  "v_lshlrev_b32 v47, 4, v16\n" \
  "v_lshlrev_b32 v46, 2, v47\n" \
  "s_lshl_b32 s82, s25, 12\n" \
  "s_add_u32 s82, s82, 12615680\n" \
  "s_mov_b32 s83, 12615680\n" \
  "buffer_load_dwordx4 v[52:55], v46, s[36:39], s82 offen sc1\n" \
  "buffer_load_dwordx4 v[56:59], v46, s[36:39], s82 offen offset:16 sc1\n" \
  "buffer_load_dwordx2 v[44:45], off, s[36:39], s83 sc1\n" \
  "v_add_u32 v12, 40960, v6\n" \
  "v_add_u32 v13, 40960, v7\n" \
  "v_add_u32 v14, 40960, v8\n" \
  "ds_read_b64_tr_b16 a[64:65], v12 offset:0\n" \
  "ds_read_b64_tr_b16 a[66:67], v12 offset:512\n" \
  "ds_read_b64_tr_b16 a[68:69], v12 offset:8192\n" \
  "ds_read_b64_tr_b16 a[70:71], v12 offset:8704\n" \
  "ds_read_b64_tr_b16 a[144:145], v13 offset:0\n" \
  "ds_read_b64_tr_b16 a[146:147], v13 offset:512\n" \
  "ds_read_b64_tr_b16 a[148:149], v13 offset:8192\n" \
  "ds_read_b64_tr_b16 a[150:151], v13 offset:8704\n" \
  "ds_read_b64_tr_b16 a[224:225], v12 offset:16384\n" \
  "ds_read_b64_tr_b16 a[226:227], v12 offset:16896\n" \
  "ds_read_b64_tr_b16 a[228:229], v12 offset:24576\n" \
  "ds_read_b64_tr_b16 a[230:231], v12 offset:25088\n" \
  "ds_read_b64_tr_b16 v[160:161], v13 offset:16384\n" \
  "ds_read_b64_tr_b16 v[162:163], v13 offset:16896\n" \
  "ds_read_b64_tr_b16 v[164:165], v13 offset:24576\n" \
  "ds_read_b64_tr_b16 v[166:167], v13 offset:25088\n" \
  "ds_read_b64_tr_b16 v[240:241], v14 offset:0\n" \
  "ds_read_b64_tr_b16 v[242:243], v14 offset:256\n" \
  "ds_read_b64_tr_b16 v[244:245], v14 offset:4096\n" \
  "ds_read_b64_tr_b16 v[246:247], v14 offset:4352\n" \
  "s_waitcnt vmcnt(0) lgkmcnt(0)\n" \
  "s_barrier\n" \
  "v_add_u32 v12, 81920, v6\n" \
  "v_add_u32 v13, 81920, v7\n" \
  "v_add_u32 v14, 81920, v8\n" \
  "ds_read_b64_tr_b16 a[72:73], v12 offset:0\n" \
  "ds_read_b64_tr_b16 a[74:75], v12 offset:512\n" \
  "ds_read_b64_tr_b16 a[76:77], v12 offset:8192\n" \
  "ds_read_b64_tr_b16 a[78:79], v12 offset:8704\n" \
  "ds_read_b64_tr_b16 a[152:153], v13 offset:0\n" \
  "ds_read_b64_tr_b16 a[154:155], v13 offset:512\n" \
  "ds_read_b64_tr_b16 a[156:157], v13 offset:8192\n" \
  "ds_read_b64_tr_b16 a[158:159], v13 offset:8704\n" \
  "ds_read_b64_tr_b16 a[232:233], v12 offset:16384\n" \
  "ds_read_b64_tr_b16 a[234:235], v12 offset:16896\n" \
  "ds_read_b64_tr_b16 a[236:237], v12 offset:24576\n" \
  "ds_read_b64_tr_b16 a[238:239], v12 offset:25088\n" \
  "ds_read_b64_tr_b16 v[168:169], v13 offset:16384\n" \
  "ds_read_b64_tr_b16 v[170:171], v13 offset:16896\n" \
  "ds_read_b64_tr_b16 v[172:173], v13 offset:24576\n" \
  "ds_read_b64_tr_b16 v[174:175], v13 offset:25088\n" \
  "ds_read_b64_tr_b16 v[248:249], v14 offset:0\n" \
  "ds_read_b64_tr_b16 v[250:251], v14 offset:256\n" \
  "ds_read_b64_tr_b16 v[252:253], v14 offset:4096\n" \
  "ds_read_b64_tr_b16 v[254:255], v14 offset:4352\n" \
  "CARA_%=:\n" \
  "s_waitcnt vmcnt(0)\n" \
  "v_xor_b32 v42, 0x5a17c0de, v44\n" \
  "v_xor_b32 v52, 0x5a17c0de, v52\n" \
  "v_xor_b32 v53, 0x3c96e1a8, v53\n" \
  "v_and_b32 v53, -8, v53\n" \
  "v_or3_b32 v42, v42, v52, v53\n" \
  "v_xor_b32 v54, 0x5a17c0de, v54\n" \
  "v_xor_b32 v55, 0x3c96e1a8, v55\n" \
  "v_and_b32 v55, -8, v55\n" \
  "v_or3_b32 v42, v42, v54, v55\n" \
  "v_xor_b32 v56, 0x5a17c0de, v56\n" \
  "v_xor_b32 v57, 0x3c96e1a8, v57\n" \
  "v_and_b32 v57, -8, v57\n" \
  "v_or3_b32 v42, v42, v56, v57\n" \
  "v_xor_b32 v58, 0x5a17c0de, v58\n" \
  "v_xor_b32 v59, 0x3c96e1a8, v59\n" \
  "v_and_b32 v59, -8, v59\n" \
  "v_or3_b32 v42, v42, v58, v59\n" \
  "v_cmp_ne_u32 vcc, 0, v42\n" \
  "v_readfirstlane_b32 s94, v45\n" \
  "s_nop 3\n" \
  "s_cbranch_vccz CARB_%=\n" \
  "s_add_u32 s54, s54, 1\n" \
  "s_cmp_gt_u32 s54, 131072\n" \
  "s_cbranch_scc1 FAIL_%=\n" \
  "s_sleep 4\n" \
  "v_lshlrev_b32 v46, 2, v47\n" \
  "s_lshl_b32 s82, s25, 12\n" \
  "s_add_u32 s82, s82, 12615680\n" \
  "s_mov_b32 s83, 12615680\n" \
  "buffer_load_dwordx4 v[52:55], v46, s[36:39], s82 offen sc1\n" \
  "buffer_load_dwordx4 v[56:59], v46, s[36:39], s82 offen offset:16 sc1\n" \
  "buffer_load_dwordx2 v[44:45], off, s[36:39], s83 sc1\n" \
  "s_branch CARA_%=\n" \
  "CARB_%=:\n" \
  "s_add_u32 s94, s94, %[b]\n" \
  "s_add_u32 s94, s94, 0\n" \
  "s_and_b32 s94, s94, 7\n" \
  "s_cmp_lg_u32 s94, s92\n" \
  "s_cbranch_scc1 REDO_%=\n" \
  "s_waitcnt lgkmcnt(0)\n" \
  "s_barrier\n" \
  "s_sub_u32 s82, s84, 8\n" \
  "s_cmp_ge_u32 s82, 3\n" \
  "s_cbranch_scc1 CALD_%=\n" \
  "CAL_%=:\n" \
  "s_lshl_b32 s83, s82, 21\n" \
  "s_add_u32 s83, s83, 2064384\n" \
  "s_lshr_b32 s85, s25, 1\n" \
  "s_lshl_b32 s85, s85, 14\n" \
  "s_add_u32 s83, s83, s85\n" \
  "s_and_b32 s85, s25, 1\n" \
  "s_lshl_b32 s85, s85, 12\n" \
  "s_add_u32 s83, s83, s85\n" \
  "s_add_u32 s85, s83, 8192\n" \
  "s_mov_b32 s90, -1\n" \
  "s_mov_b32 s91, -1\n" \
  "s_memtime s[86:87]\n" \
  "s_waitcnt lgkmcnt(0)\n" \
  "buffer_load_dword v50, off, s[36:39], s83 offset:0 sc1\n" \
  "s_waitcnt vmcnt(0)\n" \
  "s_memtime s[88:89]\n" \
  "s_waitcnt lgkmcnt(0)\n" \
  "s_sub_u32 s88, s88, s86\n" \
  "s_min_u32 s90, s90, s88\n" \
  "s_barrier\n" \
  "s_memtime s[86:87]\n" \
  "s_waitcnt lgkmcnt(0)\n" \
  "buffer_load_dword v50, off, s[36:39], s85 offset:0 sc1\n" \
  "s_waitcnt vmcnt(0)\n" \
  "s_memtime s[88:89]\n" \
  "s_waitcnt lgkmcnt(0)\n" \
  "s_sub_u32 s88, s88, s86\n" \
  "s_min_u32 s91, s91, s88\n" \
  "s_barrier\n" \
  "s_memtime s[86:87]\n" \
  "s_waitcnt lgkmcnt(0)\n" \
  "buffer_load_dword v50, off, s[36:39], s83 offset:256 sc1\n" \
  "s_waitcnt vmcnt(0)\n" \
  "s_memtime s[88:89]\n" \
  "s_waitcnt lgkmcnt(0)\n" \
  "s_sub_u32 s88, s88, s86\n" \
  "s_min_u32 s90, s90, s88\n" \
  "s_barrier\n" \
  "s_memtime s[86:87]\n" \
  "s_waitcnt lgkmcnt(0)\n" \
  "buffer_load_dword v50, off, s[36:39], s85 offset:256 sc1\n" \
  "s_waitcnt vmcnt(0)\n" \
  "s_memtime s[88:89]\n" \
  "s_waitcnt lgkmcnt(0)\n" \
  "s_sub_u32 s88, s88, s86\n" \
  "s_min_u32 s91, s91, s88\n" \
  "s_barrier\n" \
  "s_memtime s[86:87]\n" \
  "s_waitcnt lgkmcnt(0)\n" \
  "buffer_load_dword v50, off, s[36:39], s83 offset:512 sc1\n" \
  "s_waitcnt vmcnt(0)\n" \
  "s_memtime s[88:89]\n" \
  "s_waitcnt lgkmcnt(0)\n" \
  "s_sub_u32 s88, s88, s86\n" \
  "s_min_u32 s90, s90, s88\n" \
  "s_barrier\n" \
  "s_memtime s[86:87]\n" \
  "s_waitcnt lgkmcnt(0)\n" \
  "buffer_load_dword v50, off, s[36:39], s85 offset:512 sc1\n" \
  "s_waitcnt vmcnt(0)\n" \
  "s_memtime s[88:89]\n" \
  "s_waitcnt lgkmcnt(0)\n" \
  "s_sub_u32 s88, s88, s86\n" \
  "s_min_u32 s91, s91, s88\n" \
  "s_barrier\n" \
  "v_mov_b32 v20, s90\n" \
  "v_mov_b32 v21, s91\n" \
  "s_lshl_b32 s85, s25, 3\n" \
  "v_mov_b32 v22, s85\n" \
  "ds_write_b64 v22, v[20:21]\n" \
  "s_waitcnt lgkmcnt(0)\n" \
  "s_barrier\n" \
  "v_mov_b32 v22, 0\n" \
  "ds_read_b128 v[24:27], v22\n" \
  "ds_read_b128 v[28:31], v22 offset:16\n" \
  "s_waitcnt lgkmcnt(0)\n" \
  "s_barrier\n" \
  "v_min3_u32 v24, v24, v26, v28\n" \
  "v_min_u32 v24, v24, v30\n" \
  "v_min3_u32 v25, v25, v27, v29\n" \
  "v_min_u32 v25, v25, v31\n" \
  "v_cmp_lt_u32 vcc, v25, v24\n" \
  "v_mov_b32 v25, 3\n" \
  "s_nop 1\n" \
  "v_cndmask_b32 v24, 2, v25, vcc\n" \
  "s_lshl_b32 s82, s82, 6\n" \
  "s_add_u32 s82, s82, 12584960\n" \
  "v_lshrrev_b32 v22, 4, v16\n" \
  "v_lshlrev_b32 v22, 8, v22\n" \
  "v_and_b32 v23, 15, v16\n" \
  "v_lshl_add_u32 v22, v23, 2, v22\n" \
  "buffer_store_dword v24, v22, s[36:39], s82 offen sc1\n" \
  "CALD_%=:\n" \
  "v_add_u32 v15, 65536, v15\n" \
  "ds_write_b128 v15, v[72:75] offset:0\n" \
  "ds_write_b128 v15, v[76:79] offset:4096\n" \
  "ds_write_b128 v15, v[80:83] offset:8192\n" \
  "ds_write_b128 v15, v[84:87] offset:12288\n" \
  "ds_write_b128 v15, v[88:91] offset:16384\n" \
  "ds_write_b128 v15, v[92:95] offset:20480\n" \
  "ds_write_b128 v15, v[96:99] offset:24576\n" \
  "ds_write_b128 v15, v[100:103] offset:28672\n" \
  "ds_write_b128 v15, v[104:107] offset:32768\n" \
  "ds_write_b128 v15, v[108:111] offset:36864\n" \
  "s_waitcnt lgkmcnt(0)\n" \
  "v_mbcnt_lo_u32_b32 v18, -1, 0\n" \
  "v_mbcnt_hi_u32_b32 v18, -1, v18\n" \
  "v_and_b32 v16, 15, v18\n" \
  "v_lshrrev_b32 v17, 4, v18\n" \
  "v_lshlrev_b32 v0, 4, v18\n" \
  "v_add_u32 v1, s27, v0\n" \
  "v_mul_u32_u24 v3, 2112, v17\n" \
  "v_lshl_add_u32 v3, v16, 3, v3\n" \
  "s_lshl_b32 s56, s25, 9\n" \
  "s_add_u32 s56, s56, 12288\n" \
  "v_add_u32 v3, s56, v3\n" \
  "v_and_b32 v13, 3, v18\n" \
  "v_mul_u32_u24 v4, 2112, v13\n" \
  "v_lshl_add_u32 v4, v17, 5, v4\n" \
  "v_bfe_u32 v12, v18, 2, 2\n" \
  "v_lshl_add_u32 v4, v12, 3, v4\n" \
  "v_add_u32 v5, 32768, v0\n" \
  "v_bfe_u32 v12, v18, 1, 1\n" \
  "v_cmp_eq_u32 vcc, 1, v12\n" \
  "v_mov_b32 v13, 0x4444\n" \
  "v_mov_b32 v14, 0xeeee\n" \
  "v_cndmask_b32 v6, v13, v14, vcc\n" \
  "s_and_b32 s12, s25, 1\n" \
  "s_lshl_b32 s12, s12, 11\n" \
  "v_add_u32 v2, s12, v0\n" \
  "v_lshrrev_b32 v12, 1, v17\n" \
  "v_lshlrev_b32 v15, 14, v12\n" \
  "v_and_b32 v12, 1, v17\n" \
  "v_lshl_add_u32 v15, v12, 12, v15\n" \
  "v_lshrrev_b32 v12, 2, v16\n" \
  "v_lshl_add_u32 v7, v12, 15, v15\n" \
  "v_and_b32 v12, 3, v16\n" \
  "v_lshl_add_u32 v7, v12, 4, v7\n" \
  "v_lshl_add_u32 v8, v16, 2, v15\n" \
  "v_add_u32 v7, 32768, v8\n" \
  "v_lshrrev_b32 v12, 2, v16\n" \
  "v_lshlrev_b32 v9, 15, v12\n" \
  "v_lshrrev_b32 v12, 1, v17\n" \
  "v_lshl_add_u32 v9, v12, 17, v9\n" \
  "v_bfe_u32 v12, v18, 1, 1\n" \
  "v_lshl_add_u32 v9, v12, 5, v9\n" \
  "v_and_b32 v12, 1, v17\n" \
  "v_lshl_add_u32 v9, v12, 4, v9\n" \
  "v_and_b32 v12, 1, v18\n" \
  "v_lshl_add_u32 v9, v12, 3, v9\n" \
  "v_bfe_u32 v12, v18, 2, 1\n" \
  "v_lshlrev_b32 v12, 4, v12\n" \
  "v_mov_b32 v13, 0x3f80\n" \
  "v_lshlrev_b32 v13, v12, v13\n" \
  "v_and_b32 v12, 3, v18\n" \
  "v_cmp_eq_u32 vcc, 0, v12\n" \
  "s_nop 1\n" \
  "v_cndmask_b32 v20, 0, v13, vcc\n" \
  "v_cmp_eq_u32 vcc, 1, v12\n" \
  "s_nop 1\n" \
  "v_cndmask_b32 v21, 0, v13, vcc\n" \
  "v_cmp_eq_u32 vcc, 2, v12\n" \
  "s_nop 1\n" \
  "v_cndmask_b32 v22, 0, v13, vcc\n" \
  "v_cmp_eq_u32 vcc, 3, v12\n" \
  "s_nop 1\n" \
  "v_cndmask_b32 v23, 0, v13, vcc\n" \
  "ds_write_b128 v0, v[20:23] offset:27648\n" \
  "v_mov_b32 v24, 0\n" \
  "v_mov_b32 v28, 0x3f803f80\n" \
  "v_mov_b32 v25, 0\n" \
  "v_mov_b32 v29, 0x3f803f80\n" \
  "v_mov_b32 v26, 0\n" \
  "v_mov_b32 v30, 0x3f803f80\n" \
  "v_mov_b32 v27, 0\n" \
  "v_mov_b32 v31, 0x3f803f80\n" \
  "ds_write_b128 v0, v[24:27] offset:25600\n" \
  "ds_write_b128 v0, v[28:31] offset:26624\n" \
  "ds_write_b128 v1, v[24:27] offset:21504\n" \
  "s_mov_b32 s35, 0x47800000\n" \
  "s_mov_b32 s34, 0x37800000\n" \
  "s_mov_b32 s60, 0x3e0293ee\n" \
  "s_mov_b32 s32, 0x7f817f81\n" \
  "s_mov_b32 s33, 0x7f817f81\n" \
  "s_lshr_b32 s56, s22, 8\n" \
  "v_add_u32 v12, s56, v16\n" \
  "v_lshlrev_b32 v13, 2, v17\n" \
  "v_add_u32 v14, 0, v13\n" \
  "v_cmp_gt_u32 s[64:65], v14, v12\n" \
  "v_add_u32 v14, 16, v13\n" \
  "v_cmp_gt_u32 s[72:73], v14, v12\n" \
  "v_add_u32 v14, 1, v13\n" \
  "v_cmp_gt_u32 s[66:67], v14, v12\n" \
  "v_add_u32 v14, 17, v13\n" \
  "v_cmp_gt_u32 s[74:75], v14, v12\n" \
  "v_add_u32 v14, 2, v13\n" \
  "v_cmp_gt_u32 s[68:69], v14, v12\n" \
  "v_add_u32 v14, 18, v13\n" \
  "v_cmp_gt_u32 s[76:77], v14, v12\n" \
  "v_add_u32 v14, 3, v13\n" \
  "v_cmp_gt_u32 s[70:71], v14, v12\n" \
  "v_add_u32 v14, 19, v13\n" \
  "v_cmp_gt_u32 s[78:79], v14, v12\n" \
  "s_mov_b32 s97, 0\n" \
  "s_mov_b32 s40, 0\n" \
  "s_mov_b32 s55, 0\n" \
  "s_nop 1\n" \
  "s_waitcnt lgkmcnt(0)\n" \
  "s_add_u32 s28, s12, 32768\n" \
  "s_mov_b32 s81, 0\n" \
  "s_mov_b32 s11, -1\n" \
  "s_mov_b32 s10, -1\n" \
  "s_lshr_b32 s80, s24, 6\n" \
  "s_and_b32 s80, s80, 3\n" \
  "s_lshl_b32 s80, s80, 1\n" \
  "s_and_b32 s56, s25, 1\n" \
  "s_add_u32 s80, s80, s56\n" \
  "s_mul_i32 s80, s80, 3072\n" \
  "s_cmp_lg_u32 s25, 0\n" \
  "s_cbranch_scc1 NOX0_%=\n" \
  "s_add_u32 s56, s40, 2\n" \
  "s_min_u32 s56, s56, 64\n" \
  "s_cmp_ge_u32 s55, s56\n" \
  "s_cbranch_scc1 RDY0_%=\n" \
  "RDP0_%=:\n" \
  "buffer_load_dwordx4 v[44:47], off, s[36:39], s23 sc1\n" \
  "s_waitcnt vmcnt(0)\n" \
  "v_min_u32 v44, v44, v45\n" \
  "v_min3_u32 v44, v44, v46, v47\n" \
  "s_nop 0\n" \
  "v_readfirstlane_b32 s55, v44\n" \
  "s_cmp_ge_u32 s55, s56\n" \
  "s_cbranch_scc1 RDY0_%=\n" \
  "s_add_u32 s54, s54, 1\n" \
  "s_cmp_gt_u32 s54, 131072\n" \
  "s_cbranch_scc1 FAIL_%=\n" \
  "s_sleep 2\n" \
  "s_branch RDP0_%=\n" \
  "RDY0_%=:\n" \
  "s_add_u32 s56, s40, 0\n" \
  "s_min_u32 s56, s56, 63\n" \
  "s_mul_i32 s56, s56, 24576\n" \
  "s_add_u32 s56, s56, s20\n" \
  "s_add_u32 s49, s56, 8192\n" \
  "s_add_u32 s50, s56, 12288\n" \
  "s_add_u32 s51, s56, s21\n" \
  "s_add_u32 s52, s56, s22\n" \
  "s_add_u32 m0, s28, 0\n" \
  "s_nop 0\n" \
  "buffer_load_dwordx4 v2, s[36:39], s49 offen offset:0 sc1 lds\n" \
  "buffer_load_dwordx4 v2, s[36:39], s49 offen offset:1024 sc1 lds\n" \
  "s_add_u32 m0, s28, 4096\n" \
  "s_nop 0\n" \
  "buffer_load_dwordx4 v2, s[36:39], s50 offen offset:0 sc1 lds\n" \
  "buffer_load_dwordx4 v2, s[36:39], s50 offen offset:1024 sc1 lds\n" \
  "s_add_u32 m0, s28, 8192\n" \
  "s_nop 0\n" \
  "buffer_load_dwordx4 v2, s[36:39], s51 offen offset:0 sc1 lds\n" \
  "buffer_load_dwordx4 v2, s[36:39], s51 offen offset:1024 sc1 lds\n" \
  "s_add_u32 m0, s28, 12288\n" \
  "s_nop 0\n" \
  "buffer_load_dwordx4 v2, s[36:39], s52 offen offset:0 sc1 lds\n" \
  "buffer_load_dwordx4 v2, s[36:39], s52 offen offset:1024 sc1 lds\n" \
  "v_add_u32 v2, 2048, v2\n" \
  "s_add_u32 s28, s28, 2048\n" \
  "s_add_u32 m0, s28, 0\n" \
  "s_nop 0\n" \
  "buffer_load_dwordx4 v2, s[36:39], s49 offen offset:0 sc1 lds\n" \
  "buffer_load_dwordx4 v2, s[36:39], s49 offen offset:1024 sc1 lds\n" \
  "s_add_u32 m0, s28, 4096\n" \
  "s_nop 0\n" \
  "buffer_load_dwordx4 v2, s[36:39], s50 offen offset:0 sc1 lds\n" \
  "buffer_load_dwordx4 v2, s[36:39], s50 offen offset:1024 sc1 lds\n" \
  "s_add_u32 m0, s28, 8192\n" \
  "s_nop 0\n" \
  "buffer_load_dwordx4 v2, s[36:39], s51 offen offset:0 sc1 lds\n" \
  "buffer_load_dwordx4 v2, s[36:39], s51 offen offset:1024 sc1 lds\n" \
  "s_add_u32 m0, s28, 12288\n" \
  "s_nop 0\n" \
  "buffer_load_dwordx4 v2, s[36:39], s52 offen offset:0 sc1 lds\n" \
  "buffer_load_dwordx4 v2, s[36:39], s52 offen offset:1024 sc1 lds\n" \
  "v_mov_b32 v2, v0\n" \
  "s_sub_u32 s28, s28, 2048\n" \
  "NOX0_%=:\n" \
  "s_cmp_eq_u32 s25, 3\n" \
  "s_cselect_b32 s55, 0, 64\n" \
  "v_lshlrev_b32 v12, 2, v0\n" \
  "v_min_u32 v12, 128, v12\n" \
  "s_lshr_b32 s57, s19, 7\n" \
  "s_add_u32 s57, s57, 12584960\n" \
  "POLP_%=:\n" \
  "buffer_load_dword v13, v12, s[36:39], s57 offen sc1\n" \
  "s_waitcnt vmcnt(0)\n" \
  "v_cmp_eq_u32 vcc, 0, v13\n" \
  "v_and_b32 v13, 1, v13\n" \
  "s_nop 3\n" \
  "s_cbranch_vccz POLD_%=\n" \
  "s_add_u32 s54, s54, 1\n" \
  "s_cmp_gt_u32 s54, 131072\n" \
  "s_cbranch_scc1 FAIL_%=\n" \
  "s_sleep 3\n" \
  "s_branch POLP_%=\n" \
  "POLD_%=:\n" \
  "v_cmp_eq_u32 vcc, 1, v13\n" \
  "s_nop 3\n" \
  "s_and_b32 s56, vcc_lo, 7\n" \
  "s_and_b32 s58, s56, 1\n" \
  "s_lshl_b32 s58, s58, 13\n" \
  "s_and_b32 s59, s56, 2\n" \
  "s_lshl_b32 s59, s59, 12\n" \
  "s_add_u32 s59, s59, 2097152\n" \
  "s_and_b32 s26, s56, 4\n" \
  "s_lshl_b32 s26, s26, 11\n" \
  "s_add_u32 s26, s26, 4194304\n" \
  "s_add_u32 s56, s40, 0\n" \
  "s_and_b32 s56, s56, 3\n" \
  "s_lshl_b32 s56, s56, 18\n" \
  "s_add_u32 s56, s56, s26\n" \
  "s_add_u32 s41, s56, s19\n" \
  "s_add_u32 s42, s41, 4096\n" \
  "s_xor_b32 s43, s41, 16384\n" \
  "s_cmp_eq_u32 s16, 65536\n" \
  "s_cselect_b32 s96, 0, 0xf0000000\n" \
  "v_mov_b32 v12, 126\n" \
  "v_mov_b32 v13, 50\n" \
  "v_mov_b32 v14, 0\n" \
  "ds_write_b64 v14, v[12:13] offset:29696\n" \
  "v_mov_b64 v[16:17], 0\n" \
  "v_mov_b64 v[18:19], 0\n" \
  "ds_write_b128 v0, v[16:19] offset:29760\n" \
  "s_cmp_lg_u32 s25, 1\n" \
  "s_cbranch_scc1 NOHI_%=\n" \
  "s_mov_b32 s64, 126\n" \
  "s_mov_b32 s65, 0\n" \
  "s_mov_b32 s66, 0\n" \
  "s_mov_b32 s67, 0\n" \
  "s_mov_b32 s68, 0\n" \
  "s_mov_b32 s69, 50\n" \
  "s_mov_b32 s70, 0\n" \
  "s_mov_b32 s71, 0\n" \
  "s_mov_b32 s72, 0\n" \
  "s_mov_b32 s73, 0\n" \
  "NOHI_%=:\n" \
  "s_memrealtime s[82:83]\n" \
  "s_waitcnt lgkmcnt(0)\n" \
  "s_mov_b32 s30, s82\n" \
  "s_mov_b32 s48, 0\n" \
  "v_add_u32 v11, 65536, v1\n" \
  "ds_read_b128 v[72:75], v11 offset:0\n" \
  "v_mov_b64 v[52:53], 0\n" \
  "ds_read_b128 v[76:79], v11 offset:4096\n" \
  "v_mov_b64 v[54:55], 0\n" \
  "ds_read_b128 v[80:83], v11 offset:8192\n" \
  "v_mov_b64 v[56:57], 0\n" \
  "ds_read_b128 v[84:87], v11 offset:12288\n" \
  "v_mov_b64 v[58:59], 0\n" \
  "ds_read_b128 v[88:91], v11 offset:16384\n" \
  "v_mov_b64 v[60:61], 0\n" \
  "ds_read_b128 v[92:95], v11 offset:20480\n" \
  "v_mov_b64 v[62:63], 0\n" \
  "ds_read_b128 v[96:99], v11 offset:24576\n" \
  "v_mov_b64 v[64:65], 0\n" \
  "ds_read_b128 v[100:103], v11 offset:28672\n" \
  "v_mov_b64 v[66:67], 0\n" \
  "ds_read_b128 v[104:107], v11 offset:32768\n" \
  "v_mov_b64 v[68:69], 0\n" \
  "ds_read_b128 v[108:111], v11 offset:36864\n" \
  "v_mov_b64 v[70:71], 0\n" \
  "s_waitcnt vmcnt(0) lgkmcnt(0)\n" \
  "s_barrier\n" \
  "s_cmp_lg_u32 s25, 3\n" \
  "s_cbranch_scc1 NOAR_%=\n" \
  "s_memrealtime s[82:83]\n" \
  "v_bfe_u32 v12, v0, 2, 6\n" \
  "s_sub_u32 s57, s24, 12591104\n" \
  "s_add_u32 s57, s57, s26\n" \
  "s_add_u32 s57, s57, 1081344\n" \
  "s_waitcnt lgkmcnt(0)\n" \
  "s_or_b32 s56, s82, 1\n" \
  "v_mov_b32 v13, s56\n" \
  "buffer_store_dword v13, v12, s[36:39], s57 offen offset:0 sc1\n" \
  "NOAR_%=:\n" \
  "s_cmp_ge_u32 s25, 2\n" \
  "s_cbranch_scc1 HEADX_%=\n" \
  "LOOP_%=:\n" \
  "s_add_u32 m0, s12, 0\n" \
  "s_nop 0\n" \
  "buffer_load_dwordx4 v2, s[36:39], s41 offen offset:0 sc1 lds\n" \
  "buffer_load_dwordx4 v2, s[36:39], s41 offen offset:1024 sc1 lds\n" \
  "s_add_u32 m0, s12, 4096\n" \
  "s_nop 0\n" \
  "buffer_load_dwordx4 v2, s[36:39], s42 offen offset:0 sc1 lds\n" \
  "buffer_load_dwordx4 v2, s[36:39], s42 offen offset:1024 sc1 lds\n" \
  "s_add_u32 m0, s27, 8192\n" \
  "s_nop 0\n" \
  "buffer_load_dwordx4 v1, s[36:39], s43 offen sc1 lds\n" \
  "v_bfe_u32 v11, v0, 2, 4\n" \
  "s_add_u32 s56, s43, 2048\n" \
  "s_add_u32 m0, 0, 10240\n" \
  "s_nop 0\n" \
  "buffer_load_dword v11, s[36:39], s56 offen sc1 lds\n" \
  "s_add_u32 s45, s58, s18\n" \
  "s_add_u32 s46, s45, 131072\n" \
  "s_add_u32 s47, s58, s16\n" \
  "s_mov_b32 s56, s58\n" \
  "s_mov_b32 s58, s59\n" \
  "s_mov_b32 s59, s56\n" \
  "s_add_u32 s56, s40, 1\n" \
  "s_min_u32 s56, s56, 63\n" \
  "s_mul_i32 s56, s56, 24576\n" \
  "s_add_u32 s56, s56, s20\n" \
  "s_add_u32 s49, s56, 8192\n" \
  "s_add_u32 s50, s56, 12288\n" \
  "s_add_u32 s51, s56, s21\n" \
  "s_add_u32 s52, s56, s22\n" \
  "s_xor_b32 s28, s28, 16384\n" \
  "s_add_u32 s56, s40, 1\n" \
  "s_and_b32 s56, s56, 3\n" \
  "s_lshl_b32 s56, s56, 18\n" \
  "s_add_u32 s56, s56, s26\n" \
  "s_add_u32 s44, s56, s17\n" \
  "s_add_u32 s56, s40, 3\n" \
  "s_and_b32 s56, s56, 3\n" \
  "s_lshl_b32 s56, s56, 18\n" \
  "s_add_u32 s56, s56, s26\n" \
  "s_add_u32 s98, s56, s17\n" \
  "s_cmp_ge_u32 s25, 2\n" \
  "s_waitcnt vmcnt(4)\n" \
  "GW_%=:\n" \
  "s_barrier\n" \
  "ds_read_b128 v[12:15], v0 offset:0\n" \
  "ds_read_b128 v[16:19], v0 offset:1024\n" \
  "ds_read_b128 v[20:23], v0 offset:2048\n" \
  "ds_read_b128 v[24:27], v0 offset:3072\n" \
  "s_waitcnt lgkmcnt(3)\n" \
  "v_smfmac_f32_16x16x64_bf16 v[52:55], v[12:15], v[72:79], v6\n" \
  "v_smfmac_f32_16x16x64_bf16 v[56:59], v[12:15], a[80:87], v6\n" \
  "s_waitcnt lgkmcnt(2)\n" \
  "v_smfmac_f32_16x16x64_bf16 v[52:55], v[16:19], v[80:87], v6\n" \
  "v_smfmac_f32_16x16x64_bf16 v[56:59], v[16:19], a[88:95], v6\n" \
  "v_smfmac_f32_16x16x64_bf16 v[60:63], v[12:15], a[160:167], v6\n" \
  "s_waitcnt vmcnt(3)\n" \
  "s_cbranch_scc1 GW2_%=\n" \
  "s_waitcnt vmcnt(0)\n" \
  "GW2_%=:\n" \
  "s_barrier\n" \
  "s_waitcnt lgkmcnt(1)\n" \
  "v_smfmac_f32_16x16x64_bf16 v[52:55], v[20:23], v[88:95], v6\n" \
  "ds_read_b128 v[28:31], v0 offset:4096\n" \
  "v_smfmac_f32_16x16x64_bf16 v[56:59], v[20:23], a[96:103], v6\n" \
  "ds_read_b128 v[32:35], v0 offset:5120\n" \
  "s_waitcnt lgkmcnt(2)\n" \
  "v_smfmac_f32_16x16x64_bf16 v[52:55], v[24:27], v[96:103], v6\n" \
  "ds_read_b128 v[36:39], v0 offset:6144\n" \
  "v_smfmac_f32_16x16x64_bf16 v[56:59], v[24:27], a[104:111], v6\n" \
  "ds_read_b128 v[40:43], v0 offset:7168\n" \
  "v_smfmac_f32_16x16x64_bf16 v[60:63], v[16:19], a[168:175], v6\n" \
  "ds_read_b128 v[44:47], v0 offset:8192\n" \
  "s_waitcnt lgkmcnt(4)\n" \
  "v_smfmac_f32_16x16x64_bf16 v[52:55], v[28:31], v[104:111], v6\n" \
  "ds_read_b128 v[48:51], v0 offset:9216\n" \
  "v_smfmac_f32_16x16x64_bf16 v[56:59], v[28:31], a[112:119], v6\n" \
  "ds_read_b64 v[10:11], v0 offset:10240\n" \
  "s_waitcnt lgkmcnt(5)\n" \
  "v_smfmac_f32_16x16x64_bf16 v[52:55], v[32:35], a[40:47], v6\n" \
  "ds_read_b128 v[88:91], v0 offset:25600\n" \
  "v_smfmac_f32_16x16x64_bf16 v[56:59], v[32:35], a[120:127], v6\n" \
  "ds_read_b128 v[92:95], v0 offset:26624\n" \
  "s_waitcnt lgkmcnt(6)\n" \
  "v_smfmac_f32_16x16x64_bf16 v[52:55], v[36:39], a[48:55], v6\n" \
  "ds_read_b128 v[96:99], v5 offset:0\n" \
  "s_waitcnt lgkmcnt(6)\n" \
  "v_smfmac_f32_16x16x64_bf16 v[52:55], v[40:43], a[56:63], v6\n" \
  "ds_read_b128 v[100:103], v5 offset:4096\n" \
  "s_waitcnt lgkmcnt(6)\n" \
  "v_smfmac_f32_16x16x64_bf16 v[52:55], v[44:47], a[64:71], v6\n" \
  "ds_read_b128 v[104:107], v5 offset:1024\n" \
  "s_waitcnt lgkmcnt(6)\n" \
  "v_smfmac_f32_16x16x64_bf16 v[52:55], v[48:51], a[72:79], v6\n" \
  "ds_read_b128 v[108:111], v5 offset:5120\n" \
  "v_smfmac_f32_16x16x64_bf16 v[56:59], v[36:39], a[128:135], v6\n" \
  "s_waitcnt lgkmcnt(6)\n" \
  "v_readfirstlane_b32 s31, v10\n" \
  "v_smfmac_f32_16x16x64_bf16 v[56:59], v[40:43], a[136:143], v6\n" \
  "s_nop 0\n" \
  "v_readfirstlane_b32 s99, v11\n" \
  "v_smfmac_f32_16x16x64_bf16 v[56:59], v[44:47], a[144:151], v6\n" \
  "s_nop 1\n" \
  "v_add_f32 v52, v52, v54\n" \
  "v_smfmac_f32_16x16x64_bf16 v[56:59], v[48:51], a[152:159], v6\n" \
  "v_add_f32 v53, v53, v55\n" \
  "v_smfmac_f32_16x16x64_bf16 v[60:63], v[20:23], a[176:183], v6\n" \
  "v_cvt_pk_bf16_f32 v54, v52, v53\n" \
  "v_smfmac_f32_16x16x64_bf16 v[60:63], v[24:27], a[184:191], v6\n" \
  "ds_write_b32 v3, v54 offset:0\n" \
  "v_cmp_u_f32 vcc, v52, v53\n" \
  "v_smfmac_f32_16x16x64_bf16 v[60:63], v[28:31], a[192:199], v6\n" \
  "s_nop 1\n" \
  "v_add_f32 v56, v56, v58\n" \
  "v_smfmac_f32_16x16x64_bf16 v[60:63], v[32:35], a[200:207], v6\n" \
  "v_add_f32 v57, v57, v59\n" \
  "v_smfmac_f32_16x16x64_bf16 v[60:63], v[36:39], a[208:215], v6\n" \
  "v_cvt_pk_bf16_f32 v58, v56, v57\n" \
  "v_smfmac_f32_16x16x64_bf16 v[60:63], v[40:43], a[216:223], v6\n" \
  "ds_write_b32 v3, v58 offset:128\n" \
  "v_smfmac_f32_16x16x64_bf16 v[60:63], v[44:47], a[224:231], v6\n" \
  "s_cbranch_vccnz GCHK_%=\n" \
  "GOK_%=:\n" \
  "v_smfmac_f32_16x16x64_bf16 v[60:63], v[48:51], a[232:239], v6\n" \
  "ds_read_b64_tr_b16 v[72:73], v4 offset:12288\n" \
  "v_smfmac_f32_16x16x64_bf16 v[64:67], v[12:15], a[240:247], v6\n" \
  "ds_read_b64_tr_b16 v[76:77], v4 offset:12800\n" \
  "v_smfmac_f32_16x16x64_bf16 v[64:67], v[16:19], a[248:255], v6\n" \
  "ds_read_b64_tr_b16 v[74:75], v4 offset:12416\n" \
  "ds_read_b64_tr_b16 v[78:79], v4 offset:12928\n" \
  "v_smfmac_f32_16x16x64_bf16 v[64:67], v[20:23], v[112:119], v6\n" \
  "ds_read_b64_tr_b16 v[80:81], v4 offset:13312\n" \
  "v_cvt_pk_bf16_f32 v60, v60, v61\n" \
  "v_smfmac_f32_16x16x64_bf16 v[64:67], v[24:27], v[120:127], v6\n" \
  "ds_read_b64_tr_b16 v[82:83], v4 offset:13440\n" \
  "v_cvt_pk_bf16_f32 v61, v62, v63\n" \
  "v_smfmac_f32_16x16x64_bf16 v[64:67], v[28:31], v[128:135], v6\n" \
  "ds_read_b64_tr_b16 v[84:85], v4 offset:13824\n" \
  "s_waitcnt lgkmcnt(4)\n" \
  "v_mfma_f32_16x16x32_bf16 v[52:55], v[96:99], v[72:75], 0\n" \
  "ds_read_b128 v[96:99], v5 offset:2048\n" \
  "v_mfma_f32_16x16x32_bf16 v[56:59], v[100:103], v[72:75], 0\n" \
  "ds_read_b128 v[100:103], v5 offset:6144\n" \
  "s_waitcnt lgkmcnt(5)\n" \
  "v_mfma_f32_16x16x32_bf16 v[52:55], v[104:107], v[76:79], v[52:55]\n" \
  "ds_read_b128 v[104:107], v5 offset:3072\n" \
  "v_mfma_f32_16x16x32_bf16 v[56:59], v[108:111], v[76:79], v[56:59]\n" \
  "ds_read_b128 v[108:111], v5 offset:7168\n" \
  "v_smfmac_f32_16x16x64_bf16 v[64:67], v[32:35], v[136:143], v6\n" \
  "ds_read_b64_tr_b16 v[86:87], v4 offset:13952\n" \
  "s_waitcnt lgkmcnt(4)\n" \
  "v_mfma_f32_16x16x32_bf16 v[52:55], v[96:99], v[80:83], v[52:55]\n" \
  "ds_write_b64 v3, v[60:61] offset:256\n" \
  "s_waitcnt lgkmcnt(4)\n" \
  "v_mfma_f32_16x16x32_bf16 v[56:59], v[100:103], v[80:83], v[56:59]\n" \
  "v_smfmac_f32_16x16x64_bf16 v[64:67], v[36:39], v[144:151], v6\n" \
  "s_waitcnt lgkmcnt(1)\n" \
  "v_mfma_f32_16x16x32_bf16 v[52:55], v[104:107], v[84:87], v[52:55]\n" \
  "ds_read_b128 v[96:99], v5 offset:8192\n" \
  "v_mfma_f32_16x16x32_bf16 v[56:59], v[108:111], v[84:87], v[56:59]\n" \
  "ds_read_b128 v[100:103], v5 offset:9216\n" \
  "v_smfmac_f32_16x16x64_bf16 v[64:67], v[40:43], v[152:159], v6\n" \
  "ds_read_b128 v[104:107], v5 offset:10240\n" \
  "s_nop 0\n" \
  "v_smfmac_f32_16x16x64_bf16 v[64:67], v[44:47], v[160:167], v6\n" \
  "ds_read_b128 v[108:111], v5 offset:11264\n" \
  "s_nop 0\n" \
  "v_max3_f32 v10, v52, v53, v54\n" \
  "v_smfmac_f32_16x16x64_bf16 v[64:67], v[48:51], v[168:175], v6\n" \
  "v_max3_f32 v10, v10, v55, v56\n" \
  "v_max3_f32 v10, v10, v57, v58\n" \
  "v_smfmac_f32_16x16x64_bf16 v[68:71], v[12:15], v[176:183], v6\n" \
  "v_max_f32 v10, v10, v59\n" \
  "v_mov_b32 v11, v10\n" \
  "v_smfmac_f32_16x16x64_bf16 v[68:71], v[16:19], v[184:191], v6\n" \
  "s_nop 0\n" \
  "v_permlane16_swap_b32 v10, v11\n" \
  "v_cvt_pk_bf16_f32 v64, v64, v65\n" \
  "v_cvt_pk_bf16_f32 v65, v66, v67\n" \
  "v_smfmac_f32_16x16x64_bf16 v[68:71], v[20:23], v[192:199], v6\n" \
  "ds_write_b64 v3, v[64:65] offset:384\n" \
  "v_max_f32 v10, v10, v11\n" \
  "v_mov_b32 v11, v10\n" \
  "v_smfmac_f32_16x16x64_bf16 v[68:71], v[24:27], v[200:207], v6\n" \
  "s_cmp_eq_u32 s40, 63\n" \
  "v_permlane32_swap_b32 v10, v11\n" \
  "v_max_f32 v10, v10, v11\n" \
  "s_cbranch_scc1 TAIL_%=\n" \
  "v_smfmac_f32_16x16x64_bf16 v[68:71], v[28:31], v[208:215], v6\n" \
  "ds_read_b64_tr_b16 v[60:61], v4 offset:12544\n" \
  "v_sub_f32 v52, v52, v10\n" \
  "v_sub_f32 v53, v53, v10\n" \
  "v_smfmac_f32_16x16x64_bf16 v[68:71], v[32:35], v[216:223], v6\n" \
  "ds_read_b64_tr_b16 v[64:65], v4 offset:13056\n" \
  "v_exp_f32 v52, v52\n" \
  "ds_read_b64_tr_b16 v[12:13], v4 offset:13568\n" \
  "v_sub_f32 v54, v54, v10\n" \
  "v_smfmac_f32_16x16x64_bf16 v[68:71], v[36:39], v[224:231], v6\n" \
  "ds_read_b64_tr_b16 v[16:17], v4 offset:14080\n" \
  "v_exp_f32 v53, v53\n" \
  "ds_read_b64_tr_b16 v[62:63], v4 offset:12672\n" \
  "v_sub_f32 v55, v55, v10\n" \
  "v_smfmac_f32_16x16x64_bf16 v[68:71], v[40:43], v[232:239], v6\n" \
  "ds_read_b64_tr_b16 v[66:67], v4 offset:13184\n" \
  "v_exp_f32 v54, v54\n" \
  "ds_read_b64_tr_b16 v[14:15], v4 offset:13696\n" \
  "v_sub_f32 v56, v56, v10\n" \
  "v_smfmac_f32_16x16x64_bf16 v[68:71], v[44:47], v[240:247], v6\n" \
  "ds_read_b64_tr_b16 v[18:19], v4 offset:14208\n" \
  "v_exp_f32 v55, v55\n" \
  "v_sub_f32 v57, v57, v10\n" \
  "v_smfmac_f32_16x16x64_bf16 v[68:71], v[48:51], v[248:255], v6\n" \
  "s_waitcnt lgkmcnt(2)\n" \
  "v_exp_f32 v56, v56\n" \
  "v_mfma_f32_16x16x32_bf16 v[24:27], v[60:63], v[72:75], 0\n" \
  "v_sub_f32 v58, v58, v10\n" \
  "v_sub_f32 v59, v59, v10\n" \
  "v_mfma_f32_16x16x32_bf16 v[24:27], v[64:67], v[76:79], v[24:27]\n" \
  "s_waitcnt lgkmcnt(0)\n" \
  "v_exp_f32 v57, v57\n" \
  "v_mfma_f32_16x16x32_bf16 v[24:27], v[12:15], v[80:83], v[24:27]\n" \
  "v_lshrrev_b32 v44, 1, v1\n" \
  "v_add_f32 v68, v68, v70\n" \
  "v_mfma_f32_16x16x32_bf16 v[24:27], v[16:19], v[84:87], v[24:27]\n" \
  "v_add_f32 v69, v69, v71\n" \
  "v_cvt_pk_bf16_f32 v68, v68, v69\n" \
  "ds_write_b32 v44, v68 offset:21504\n" \
  "v_exp_f32 v58, v58\n" \
  "v_exp_f32 v59, v59\n" \
  "v_cvt_pk_bf16_f32 v20, v52, v53\n" \
  "v_cvt_pk_bf16_f32 v21, v54, v55\n" \
  "v_cvt_pk_bf16_f32 v22, v56, v57\n" \
  "v_cvt_pk_bf16_f32 v23, v58, v59\n" \
  "v_lshrrev_b32 v45, 1, v0\n" \
  "v_add_f32 v24, v24, v26\n" \
  "v_mfma_f32_16x16x32_bf16 v[28:31], v[96:99], v[20:23], 0\n" \
  "v_add_f32 v25, v25, v27\n" \
  "v_fma_f32 v24, v24, s60, -v10\n" \
  "v_mfma_f32_16x16x32_bf16 v[32:35], v[100:103], v[20:23], 0\n" \
  "ds_read_b64 v[52:53], v45 offset:21504\n" \
  "v_fma_f32 v25, v25, s60, -v10\n" \
  "ds_read_b64 v[54:55], v45 offset:22016\n" \
  "v_mfma_f32_16x16x32_bf16 v[36:39], v[104:107], v[20:23], 0\n" \
  "ds_read_b64 v[56:57], v45 offset:22528\n" \
  "v_exp_f32 v24, v24\n" \
  "v_mfma_f32_16x16x32_bf16 v[40:43], v[108:111], v[20:23], 0\n" \
  "ds_read_b64 v[58:59], v45 offset:23040\n" \
  "v_exp_f32 v25, v25\n" \
  "v_mfma_f32_16x16x32_bf16 v[68:71], v[20:23], v[92:95], 0\n" \
  "s_add_u32 s57, s45, 65536\n" \
  "v_cvt_pk_bf16_f32 v88, v24, v25\n" \
  "v_max_f32 v11, v24, v25\n" \
  "s_waitcnt lgkmcnt(2)\n" \
  "v_mfma_f32_16x16x32_bf16 v[28:31], v[52:55], v[88:91], v[28:31]\n" \
  "v_cmp_lt_f32 vcc, s35, v11\n" \
  "s_waitcnt lgkmcnt(1)\n" \
  "v_mfma_f32_16x16x32_bf16 v[32:35], v[54:57], v[88:91], v[32:35]\n" \
  "s_nop 0\n" \
  "s_waitcnt lgkmcnt(0)\n" \
  "v_mfma_f32_16x16x32_bf16 v[36:39], v[56:59], v[88:91], v[36:39]\n" \
  "s_nop 1\n" \
  "v_mfma_f32_16x16x32_bf16 v[40:43], v[58:61], v[88:91], v[40:43]\n" \
  "s_cbranch_vccnz SLOW_%=\n" \
  "v_cvt_pk_bf16_f32 v44, v28, v29\n" \
  "v_cvt_pk_bf16_f32 v45, v30, v31\n" \
  "v_mfma_f32_16x16x32_bf16 v[68:71], v[88:91], v[92:95], v[68:71]\n" \
  "v_cvt_pk_bf16_f32 v46, v32, v33\n" \
  "v_cvt_pk_bf16_f32 v47, v34, v35\n" \
  "WOJ_%=:\n" \
  "v_cvt_pk_bf16_f32 v48, v36, v37\n" \
  "v_cvt_pk_bf16_f32 v49, v38, v39\n" \
  "v_mfma_f32_16x16x32_bf16 v[96:99], v[44:47], a[0:3], 0\n" \
  "v_cvt_pk_bf16_f32 v50, v40, v41\n" \
  "v_cvt_pk_bf16_f32 v51, v42, v43\n" \
  "v_mfma_f32_16x16x32_bf16 v[100:103], v[44:47], a[8:11], 0\n" \
  "s_nop 0\n" \
  "v_mfma_f32_16x16x32_bf16 v[96:99], v[48:51], a[4:7], v[96:99]\n" \
  "s_nop 0\n" \
  "v_rcp_f32 v10, v68\n" \
  "v_mfma_f32_16x16x32_bf16 v[100:103], v[48:51], a[12:15], v[100:103]\n" \
  "s_nop 0\n" \
  "v_rcp_f32 v11, v69\n" \
  "v_mfma_f32_16x16x32_bf16 v[104:107], v[44:47], a[16:19], 0\n" \
  "s_nop 1\n" \
  "v_cmp_u_f32 vcc, v96, v97\n" \
  "v_mfma_f32_16x16x32_bf16 v[104:107], v[48:51], a[20:23], v[104:107]\n" \
  "v_mul_f32 v96, v10, v96\n" \
  "v_mul_f32 v97, v11, v97\n" \
  "v_mfma_f32_16x16x32_bf16 v[108:111], v[44:47], a[24:27], 0\n" \
  "v_cvt_pk_bf16_f32 v96, v96, v97\n" \
  "v_mul_f32 v100, v10, v100\n" \
  "s_cbranch_vccnz SLOWN_%=\n" \
  "YOK_%=:\n" \
  "v_mfma_f32_16x16x32_bf16 v[108:111], v[48:51], a[28:31], v[108:111]\n" \
  "buffer_store_dword v96, v8, s[36:39], s45 offen offset:0 sc1\n" \
  "v_mul_f32 v101, v11, v101\n" \
  "v_mfma_f32_16x16x32_bf16 v[20:23], v[44:47], a[32:35], 0\n" \
  "v_cvt_pk_bf16_f32 v100, v100, v101\n" \
  "v_mul_f32 v104, v10, v104\n" \
  "v_mfma_f32_16x16x32_bf16 v[20:23], v[48:51], a[36:39], v[20:23]\n" \
  "buffer_store_dword v100, v7, s[36:39], s45 offen offset:0 sc1\n" \
  "v_mul_f32 v105, v11, v105\n" \
  "v_cvt_pk_bf16_f32 v104, v104, v105\n" \
  "v_mul_f32 v108, v10, v108\n" \
  "buffer_store_dword v104, v8, s[36:39], s57 offen offset:0 sc1\n" \
  "v_mul_f32 v109, v11, v109\n" \
  "v_cvt_pk_bf16_f32 v108, v108, v109\n" \
  "s_nop 0\n" \
  "v_mul_f32 v20, v10, v20\n" \
  "buffer_store_dword v108, v7, s[36:39], s57 offen offset:0 sc1\n" \
  "v_mul_f32 v21, v11, v21\n" \
  "v_cvt_pk_bf16_f32 v20, v20, v21\n" \
  "buffer_store_dword v20, v8, s[36:39], s46 offen offset:0 sc1\n" \
  "s_memrealtime s[82:83]\n" \
  "s_min_u32 s99, s99, 120\n" \
  "s_add_u32 s48, s31, s99\n" \
  "s_add_u32 s61, s31, 8\n" \
  "s_cmp_ge_u32 s25, 2\n" \
  "s_cbranch_scc1 XA_%=\n" \
  "s_cmp_lt_i32 s16, 0\n" \
  "s_cbranch_scc1 NORED_%=\n" \
  "s_waitcnt lgkmcnt(0)\n" \
  "s_sub_u32 s29, s82, s30\n" \
  "s_sub_u32 s56, s31, s82\n" \
  "s_cmp_ge_u32 s56, 400\n" \
  "s_cbranch_scc1 LATE_%=\n" \
  "WQR_%=:\n" \
  "s_sub_u32 s63, s31, s62\n" \
  "WUR_%=:\n" \
  "s_memrealtime s[82:83]\n" \
  "s_sub_u32 s57, s63, 4\n" \
  "s_waitcnt lgkmcnt(0)\n" \
  "s_sub_u32 s56, s57, s82\n" \
  "s_cbranch_scc1 WXR_%=\n" \
  "s_cmp_ge_u32 s56, 400\n" \
  "s_cbranch_scc1 WXR_%=\n" \
  "WLR_%=:\n" \
  "s_memrealtime s[82:83]\n" \
  "s_cmp_gt_u32 s82, s57\n" \
  "s_cbranch_scc1 WXR_%=\n" \
  "s_memrealtime s[82:83]\n" \
  "s_cmp_gt_u32 s82, s57\n" \
  "s_cbranch_scc1 WXR_%=\n" \
  "s_memrealtime s[82:83]\n" \
  "s_cmp_gt_u32 s82, s57\n" \
  "s_cbranch_scc1 WXR_%=\n" \
  "s_memrealtime s[82:83]\n" \
  "s_cmp_gt_u32 s82, s57\n" \
  "s_cbranch_scc1 WXR_%=\n" \
  "s_memrealtime s[82:83]\n" \
  "s_cmp_gt_u32 s82, s57\n" \
  "s_cbranch_scc1 WXR_%=\n" \
  "s_memrealtime s[82:83]\n" \
  "s_cmp_gt_u32 s82, s57\n" \
  "s_cbranch_scc0 WLR_%=\n" \
  "s_nop 2\n" \
  "WXR_%=:\n" \
  "s_sub_u32 s56, s63, s82\n" \
  "s_cmp_eq_u32 s56, 3\n" \
  "s_cbranch_scc1 WP3R_%=\n" \
  "s_cmp_eq_u32 s56, 2\n" \
  "s_cbranch_scc1 WP2R_%=\n" \
  "s_cmp_eq_u32 s56, 1\n" \
  "s_cbranch_scc1 WP1R_%=\n" \
  "s_branch WP0R_%=\n" \
  "WP3R_%=:\n" \
  "s_nop 7\n" \
  "WP2R_%=:\n" \
  "s_nop 7\n" \
  "WP1R_%=:\n" \
  "s_nop 6\n" \
  "WP0R_%=:\n" \
  "buffer_load_dwordx4 v[16:19], v0, s[36:39], s47 offen offset:0 sc1\n" \
  "buffer_load_dwordx4 v[20:23], v0, s[36:39], s47 offen offset:1024 sc1\n" \
  "buffer_load_dwordx4 v[24:27], v0, s[36:39], s47 offen offset:2048 sc1\n" \
  "buffer_load_dwordx4 v[28:31], v0, s[36:39], s47 offen offset:3072 sc1\n" \
  "ds_read_b128 v[12:15], v0 offset:27648\n" \
  "v_add_u32 v11, 65536, v1\n" \
  "ds_read_b128 v[72:75], v11 offset:0\n" \
  "ds_read_b128 v[76:79], v11 offset:4096\n" \
  "ds_read_b128 v[80:83], v11 offset:8192\n" \
  "ds_read_b128 v[84:87], v11 offset:12288\n" \
  "ds_read_b128 v[88:91], v11 offset:16384\n" \
  "ds_read_b128 v[92:95], v11 offset:20480\n" \
  "ds_read_b128 v[96:99], v11 offset:24576\n" \
  "ds_read_b128 v[100:103], v11 offset:28672\n" \
  "ds_read_b128 v[104:107], v11 offset:32768\n" \
  "ds_read_b128 v[108:111], v11 offset:36864\n" \
  "v_mov_b64 v[44:45], s[32:33]\n" \
  "buffer_store_dwordx2 v[44:45], v9, s[36:39], s98 offen offset:0 sc1\n" \
  "s_and_b32 s56, s29, s81\n" \
  "s_min_u32 s56, s56, 1000\n" \
  "s_min_u32 s57, s15, 1000\n" \
  "s_pack_ll_b32_b16 s56, s56, s57\n" \
  "v_mov_b32 v46, s56\n" \
  "v_bfe_u32 v48, v0, 2, 6\n" \
  "s_add_u32 s57, s26, s17\n" \
  "s_add_u32 s57, s57, 1048576\n" \
  "buffer_store_dword v46, v48, s[36:39], s57 offen offset:0 sc1\n" \
  "v_mov_b32 v41, 0\n" \
  "ds_read_b64 v[50:51], v41 offset:29696\n" \
  "v_or_b32 v41, s96, v9\n" \
  "s_add_u32 s56, s99, 10\n" \
  "s_add_u32 s56, s56, s31\n" \
  "s_waitcnt lgkmcnt(0)\n" \
  "v_add_u32 v42, s56, v50\n" \
  "v_add_u32 v43, 9, v51\n" \
  "s_add_u32 s57, s44, 17920\n" \
  "buffer_store_dwordx2 v[42:43], v41, s[36:39], s57 offen offset:0 sc1\n" \
  "s_add_u32 s56, s40, 1\n" \
  "s_and_b32 s56, s56, 3\n" \
  "s_lshl_b32 s56, s56, 18\n" \
  "s_add_u32 s56, s56, s26\n" \
  "s_add_u32 s41, s56, s19\n" \
  "s_add_u32 s42, s41, 4096\n" \
  "s_xor_b32 s43, s41, 16384\n" \
  "s_add_u32 m0, s28, 0\n" \
  "s_nop 0\n" \
  "buffer_load_dwordx4 v2, s[36:39], s49 offen offset:0 sc1 lds\n" \
  "buffer_load_dwordx4 v2, s[36:39], s49 offen offset:1024 sc1 lds\n" \
  "s_add_u32 m0, s28, 4096\n" \
  "s_nop 0\n" \
  "buffer_load_dwordx4 v2, s[36:39], s50 offen offset:0 sc1 lds\n" \
  "buffer_load_dwordx4 v2, s[36:39], s50 offen offset:1024 sc1 lds\n" \
  "s_add_u32 m0, s28, 8192\n" \
  "s_nop 0\n" \
  "buffer_load_dwordx4 v2, s[36:39], s51 offen offset:0 sc1 lds\n" \
  "buffer_load_dwordx4 v2, s[36:39], s51 offen offset:1024 sc1 lds\n" \
  "s_add_u32 m0, s28, 12288\n" \
  "s_nop 0\n" \
  "buffer_load_dwordx4 v2, s[36:39], s52 offen offset:0 sc1 lds\n" \
  "buffer_load_dwordx4 v2, s[36:39], s52 offen offset:1024 sc1 lds\n" \
  "v_mov_b64 v[52:53], 0\n" \
  "v_mov_b64 v[54:55], 0\n" \
  "v_mov_b64 v[56:57], 0\n" \
  "v_mov_b64 v[58:59], 0\n" \
  "v_mov_b64 v[60:61], 0\n" \
  "v_mov_b64 v[62:63], 0\n" \
  "v_mov_b64 v[64:65], 0\n" \
  "v_mov_b64 v[66:67], 0\n" \
  "v_mov_b64 v[68:69], 0\n" \
  "v_mov_b64 v[70:71], 0\n" \
  "s_waitcnt vmcnt(14)\n" \
  "v_mfma_f32_16x16x32_bf16 v[32:35], v[12:15], v[16:19], 0\n" \
  "v_fma_f32 v36, v16, v17, v18\n" \
  "v_add_f32 v36, v36, v19\n" \
  "s_waitcnt vmcnt(13)\n" \
  "v_mfma_f32_16x16x32_bf16 v[32:35], v[12:15], v[20:23], v[32:35]\n" \
  "v_fma_f32 v36, v20, v21, v36\n" \
  "v_fma_f32 v36, v22, v23, v36\n" \
  "s_waitcnt vmcnt(12)\n" \
  "v_mfma_f32_16x16x32_bf16 v[32:35], v[12:15], v[24:27], v[32:35]\n" \
  "v_fma_f32 v36, v24, v25, v36\n" \
  "v_fma_f32 v36, v26, v27, v36\n" \
  "s_waitcnt vmcnt(11)\n" \
  "v_mfma_f32_16x16x32_bf16 v[32:35], v[12:15], v[28:31], v[32:35]\n" \
  "v_fma_f32 v36, v28, v29, v36\n" \
  "v_fma_f32 v36, v30, v31, v36\n" \
  "v_cmp_u_f32 vcc, v36, v36\n" \
  "s_cbranch_vccnz RCHK_%=\n" \
  "RDONE_%=:\n" \
  "s_nop 3\n" \
  "v_add_f32_dpp v32, v32, v32 row_ror:4 row_mask:0xf bank_mask:0xf\n" \
  "v_add_f32_dpp v33, v33, v33 row_ror:4 row_mask:0xf bank_mask:0xf\n" \
  "v_add_f32_dpp v34, v34, v34 row_ror:4 row_mask:0xf bank_mask:0xf\n" \
  "v_add_f32_dpp v35, v35, v35 row_ror:4 row_mask:0xf bank_mask:0xf\n" \
  "v_add_f32_dpp v32, v32, v32 row_ror:8 row_mask:0xf bank_mask:0xf\n" \
  "v_add_f32_dpp v33, v33, v33 row_ror:8 row_mask:0xf bank_mask:0xf\n" \
  "v_add_f32_dpp v34, v34, v34 row_ror:8 row_mask:0xf bank_mask:0xf\n" \
  "v_add_f32_dpp v35, v35, v35 row_ror:8 row_mask:0xf bank_mask:0xf\n" \
  "v_cvt_pk_bf16_f32 v38, v32, v33\n" \
  "v_cvt_pk_bf16_f32 v39, v34, v35\n" \
  "buffer_store_dwordx2 v[38:39], v9, s[36:39], s44 offen offset:0 sc1\n" \
  "s_memrealtime s[82:83]\n" \
  "s_barrier\n" \
  "s_waitcnt lgkmcnt(0)\n" \
  "s_sub_u32 s15, s82, s31\n" \
  "s_add_u32 s56, s15, 6\n" \
  "s_max_u32 s56, s56, s99\n" \
  "s_add_u32 s30, s31, s56\n" \
  "s_and_b32 s15, s15, s81\n" \
  "s_mov_b32 s81, -1\n" \
  "v_xor_b32 v5, 0x4000, v5\n" \
  "s_add_u32 s40, s40, 1\n" \
  "WUG_%=:\n" \
  "s_memrealtime s[82:83]\n" \
  "s_sub_u32 s57, s30, 4\n" \
  "s_waitcnt lgkmcnt(0)\n" \
  "s_sub_u32 s56, s57, s82\n" \
  "s_cbranch_scc1 WXG_%=\n" \
  "s_cmp_ge_u32 s56, 400\n" \
  "s_cbranch_scc1 WXG_%=\n" \
  "WLG_%=:\n" \
  "s_memrealtime s[82:83]\n" \
  "s_cmp_gt_u32 s82, s57\n" \
  "s_cbranch_scc1 WXG_%=\n" \
  "s_memrealtime s[82:83]\n" \
  "s_cmp_gt_u32 s82, s57\n" \
  "s_cbranch_scc1 WXG_%=\n" \
  "s_memrealtime s[82:83]\n" \
  "s_cmp_gt_u32 s82, s57\n" \
  "s_cbranch_scc1 WXG_%=\n" \
  "s_memrealtime s[82:83]\n" \
  "s_cmp_gt_u32 s82, s57\n" \
  "s_cbranch_scc1 WXG_%=\n" \
  "s_memrealtime s[82:83]\n" \
  "s_cmp_gt_u32 s82, s57\n" \
  "s_cbranch_scc1 WXG_%=\n" \
  "s_memrealtime s[82:83]\n" \
  "s_cmp_gt_u32 s82, s57\n" \
  "s_cbranch_scc0 WLG_%=\n" \
  "s_nop 2\n" \
  "WXG_%=:\n" \
  "s_sub_u32 s56, s30, s82\n" \
  "s_cmp_eq_u32 s56, 3\n" \
  "s_cbranch_scc1 WP3G_%=\n" \
  "s_cmp_eq_u32 s56, 2\n" \
  "s_cbranch_scc1 WP2G_%=\n" \
  "s_cmp_eq_u32 s56, 1\n" \
  "s_cbranch_scc1 WP1G_%=\n" \
  "s_branch WP0G_%=\n" \
  "WP3G_%=:\n" \
  "s_nop 7\n" \
  "WP2G_%=:\n" \
  "s_nop 7\n" \
  "WP1G_%=:\n" \
  "s_nop 6\n" \
  "WP0G_%=:\n" \
  "s_barrier\n" \
  "s_branch LOOP_%=\n" \
  "NORED_%=:\n" \
  "s_cmp_lg_u32 s24, 12591168\n" \
  "s_cbranch_scc1 NOTK_%=\n" \
  "s_cmp_lt_u32 s40, 3\n" \
  "s_cbranch_scc1 NOTK_%=\n" \
  "v_lshrrev_b32 v18, 2, v0\n" \
  "ds_read_b32 v12, v18 offset:29760\n" \
  "ds_read_b32 v13, v18 offset:30016\n" \
  "ds_read_b32 v14, v18 offset:30272\n" \
  "s_waitcnt lgkmcnt(1)\n" \
  "v_pk_min_u16 v16, v12, v13\n" \
  "v_pk_max_u16 v15, v12, v13\n" \
  "s_waitcnt lgkmcnt(0)\n" \
  "v_pk_min_u16 v16, v16, v14\n" \
  "v_pk_max_u16 v15, v15, v14\n" \
  "v_lshrrev_b32 v17, 16, v16\n" \
  "v_and_b32 v16, 65535, v16\n" \
  "v_min_u32 v16, v16, v17\n" \
  "v_cmp_eq_u32 vcc, 0, v16\n" \
  "v_mov_b32_dpp v16, v15 row_ror:1 row_mask:0xf bank_mask:0xf\n" \
  "v_pk_max_u16 v15, v15, v16\n" \
  "s_nop 1\n" \
  "v_mov_b32_dpp v16, v15 row_ror:2 row_mask:0xf bank_mask:0xf\n" \
  "v_pk_max_u16 v15, v15, v16\n" \
  "s_nop 1\n" \
  "v_mov_b32_dpp v16, v15 row_ror:4 row_mask:0xf bank_mask:0xf\n" \
  "v_pk_max_u16 v15, v15, v16\n" \
  "s_nop 1\n" \
  "v_mov_b32_dpp v16, v15 row_ror:8 row_mask:0xf bank_mask:0xf\n" \
  "v_pk_max_u16 v15, v15, v16\n" \
  "s_nop 0\n" \
  "v_readlane_b32 s74, v15, 0\n" \
  "v_readlane_b32 s75, v15, 16\n" \
  "v_readlane_b32 s76, v15, 32\n" \
  "v_readlane_b32 s77, v15, 48\n" \
  "s_cmp_eq_u64 vcc, 0\n" \
  "s_cselect_b32 s78, 8, 2\n" \
  "s_and_b32 s56, s74, 65535\n" \
  "s_and_b32 s57, s75, 65535\n" \
  "s_max_u32 s56, s56, s57\n" \
  "s_and_b32 s57, s76, 65535\n" \
  "s_max_u32 s56, s56, s57\n" \
  "s_and_b32 s57, s77, 65535\n" \
  "s_max_u32 s56, s56, s57\n" \
  "s_add_u32 s57, s64, s78\n" \
  "s_min_u32 s56, s56, s57\n" \
  "s_cmp_le_u32 s56, s64\n" \
  "s_cselect_b32 s57, 0, s56\n" \
  "s_cmp_eq_u64 vcc, 0\n" \
  "s_cselect_b32 s56, s56, s57\n" \
  "s_add_u32 s64, s64, 1\n" \
  "s_cmp_eq_u32 s56, 0\n" \
  "s_cbranch_scc1 TK0_%=\n" \
  "s_max_u32 s57, s56, s68\n" \
  "s_min_u32 s64, s56, s68\n" \
  "s_min_u32 s79, s57, s67\n" \
  "s_max_u32 s64, s64, s79\n" \
  "s_max_u32 s57, s57, s67\n" \
  "s_mov_b32 s68, s67\n" \
  "s_min_u32 s79, s57, s66\n" \
  "s_max_u32 s64, s64, s79\n" \
  "s_max_u32 s57, s57, s66\n" \
  "s_mov_b32 s67, s66\n" \
  "s_min_u32 s79, s57, s65\n" \
  "s_max_u32 s64, s64, s79\n" \
  "s_max_u32 s57, s57, s65\n" \
  "s_mov_b32 s66, s65\n" \
  "s_mov_b32 s65, s56\n" \
  "s_cmp_eq_u32 s64, 0\n" \
  "s_cselect_b32 s64, s57, s64\n" \
  "TK0_%=:\n" \
  "s_lshr_b32 s56, s74, 16\n" \
  "s_lshr_b32 s57, s75, 16\n" \
  "s_max_u32 s56, s56, s57\n" \
  "s_lshr_b32 s57, s76, 16\n" \
  "s_max_u32 s56, s56, s57\n" \
  "s_lshr_b32 s57, s77, 16\n" \
  "s_max_u32 s56, s56, s57\n" \
  "s_add_u32 s57, s69, s78\n" \
  "s_min_u32 s56, s56, s57\n" \
  "s_cmp_le_u32 s56, s69\n" \
  "s_cselect_b32 s57, 0, s56\n" \
  "s_cmp_eq_u64 vcc, 0\n" \
  "s_cselect_b32 s56, s56, s57\n" \
  "s_add_u32 s69, s69, 1\n" \
  "s_cmp_eq_u32 s56, 0\n" \
  "s_cbranch_scc1 TK1_%=\n" \
  "s_max_u32 s57, s56, s73\n" \
  "s_min_u32 s69, s56, s73\n" \
  "s_min_u32 s79, s57, s72\n" \
  "s_max_u32 s69, s69, s79\n" \
  "s_max_u32 s57, s57, s72\n" \
  "s_mov_b32 s73, s72\n" \
  "s_min_u32 s79, s57, s71\n" \
  "s_max_u32 s69, s69, s79\n" \
  "s_max_u32 s57, s57, s71\n" \
  "s_mov_b32 s72, s71\n" \
  "s_min_u32 s79, s57, s70\n" \
  "s_max_u32 s69, s69, s79\n" \
  "s_max_u32 s57, s57, s70\n" \
  "s_mov_b32 s71, s70\n" \
  "s_mov_b32 s70, s56\n" \
  "s_cmp_eq_u32 s69, 0\n" \
  "s_cselect_b32 s69, s57, s69\n" \
  "TK1_%=:\n" \
  "v_mov_b32 v20, s64\n" \
  "v_mov_b32 v21, s69\n" \
  "v_mov_b32 v22, 0\n" \
  "ds_write_b64 v22, v[20:21] offset:29696\n" \
  "NOTK_%=:\n" \
  "s_nop 0\n" \
  "v_add_u32 v11, 65536, v1\n" \
  "ds_read_b128 v[72:75], v11 offset:0\n" \
  "v_mov_b64 v[52:53], 0\n" \
  "ds_read_b128 v[76:79], v11 offset:4096\n" \
  "v_mov_b64 v[54:55], 0\n" \
  "ds_read_b128 v[80:83], v11 offset:8192\n" \
  "v_mov_b64 v[56:57], 0\n" \
  "ds_read_b128 v[84:87], v11 offset:12288\n" \
  "v_mov_b64 v[58:59], 0\n" \
  "ds_read_b128 v[88:91], v11 offset:16384\n" \
  "v_mov_b64 v[60:61], 0\n" \
  "ds_read_b128 v[92:95], v11 offset:20480\n" \
  "v_mov_b64 v[62:63], 0\n" \
  "ds_read_b128 v[96:99], v11 offset:24576\n" \
  "v_mov_b64 v[64:65], 0\n" \
  "ds_read_b128 v[100:103], v11 offset:28672\n" \
  "v_mov_b64 v[66:67], 0\n" \
  "ds_read_b128 v[104:107], v11 offset:32768\n" \
  "v_mov_b64 v[68:69], 0\n" \
  "ds_read_b128 v[108:111], v11 offset:36864\n" \
  "v_mov_b64 v[70:71], 0\n" \
  "s_add_u32 s56, s40, 1\n" \
  "s_and_b32 s56, s56, 3\n" \
  "s_lshl_b32 s56, s56, 18\n" \
  "s_add_u32 s56, s56, s26\n" \
  "s_add_u32 s41, s56, s19\n" \
  "s_add_u32 s42, s41, 4096\n" \
  "s_xor_b32 s43, s41, 16384\n" \
  "s_add_u32 s31, s31, 4\n" \
  "WUY_%=:\n" \
  "s_memrealtime s[82:83]\n" \
  "s_waitcnt lgkmcnt(0)\n" \
  "s_sub_u32 s56, s31, s82\n" \
  "s_sub_u32 s56, s56, 1\n" \
  "s_cmp_lt_u32 s56, 400\n" \
  "s_cbranch_scc1 WUY_%=\n" \
  "s_add_u32 m0, s28, 0\n" \
  "s_nop 0\n" \
  "buffer_load_dwordx4 v2, s[36:39], s49 offen offset:0 sc1 lds\n" \
  "buffer_load_dwordx4 v2, s[36:39], s49 offen offset:1024 sc1 lds\n" \
  "s_add_u32 m0, s28, 4096\n" \
  "s_nop 0\n" \
  "buffer_load_dwordx4 v2, s[36:39], s50 offen offset:0 sc1 lds\n" \
  "buffer_load_dwordx4 v2, s[36:39], s50 offen offset:1024 sc1 lds\n" \
  "s_add_u32 m0, s28, 8192\n" \
  "s_nop 0\n" \
  "buffer_load_dwordx4 v2, s[36:39], s51 offen offset:0 sc1 lds\n" \
  "buffer_load_dwordx4 v2, s[36:39], s51 offen offset:1024 sc1 lds\n" \
  "s_add_u32 m0, s28, 12288\n" \
  "s_nop 0\n" \
  "buffer_load_dwordx4 v2, s[36:39], s52 offen offset:0 sc1 lds\n" \
  "buffer_load_dwordx4 v2, s[36:39], s52 offen offset:1024 sc1 lds\n" \
  "s_cmp_lg_u32 s24, 12591168\n" \
  "s_cbranch_scc1 NOTD_%=\n" \
  "v_lshlrev_b32 v18, 2, v0\n" \
  "v_and_b32 v19, 2047, v18\n" \
  "s_add_u32 s56, s26, 1048576\n" \
  "s_add_u32 m0, 0, 29760\n" \
  "s_nop 0\n" \
  "buffer_load_dword v18, s[36:39], s56 offen sc1 lds\n" \
  "s_add_u32 s57, s56, 4096\n" \
  "s_add_u32 m0, 0, 30016\n" \
  "s_nop 0\n" \
  "buffer_load_dword v18, s[36:39], s57 offen sc1 lds\n" \
  "s_add_u32 s57, s56, 16384\n" \
  "s_add_u32 m0, 0, 30272\n" \
  "s_nop 0\n" \
  "buffer_load_dword v19, s[36:39], s57 offen sc1 lds\n" \
  "NOTD_%=:\n" \
  "s_barrier\n" \
  "s_branch JOIN_%=\n" \
  "XA_%=:\n" \
  "s_cmp_ge_u32 s55, 64\n" \
  "s_cbranch_scc1 XF_%=\n" \
  "v_bfe_u32 v11, v0, 2, 4\n" \
  "s_add_u32 m0, 0, 28672\n" \
  "s_nop 0\n" \
  "buffer_load_dword v11, s[36:39], s23 offen sc1 lds\n" \
  "XF_%=:\n" \
  "s_nop 1\n" \
  "ds_read_b128 v[28:31], v5 offset:12288\n" \
  "ds_read_b128 v[36:39], v5 offset:0\n" \
  "ds_read_b128 v[40:43], v5 offset:4096\n" \
  "ds_read_b128 v[32:35], v5 offset:13312\n" \
  "ds_read_b128 v[44:47], v5 offset:1024\n" \
  "ds_read_b128 v[48:51], v5 offset:5120\n" \
  "v_add_u32 v20, s12, v5\n" \
  "v_lshrrev_b32 v21, 1, v2\n" \
  "v_mov_b32 v22, 0xf149f2ca\n" \
  "v_add_u32 v23, 65536, v1\n" \
  "s_waitcnt lgkmcnt(4)\n" \
  "v_mfma_f32_16x16x32_bf16 v[52:55], v[36:39], v[28:31], 0\n" \
  "s_waitcnt lgkmcnt(3)\n" \
  "v_mfma_f32_16x16x32_bf16 v[56:59], v[40:43], v[28:31], 0\n" \
  "v_mfma_f32_16x16x32_bf16 v[68:71], v[60:63], v[28:31], 0\n" \
  "ds_read_b128 v[28:31], v5 offset:14336\n" \
  "ds_read_b128 v[36:39], v5 offset:2048\n" \
  "ds_read_b128 v[40:43], v5 offset:6144\n" \
  "s_waitcnt lgkmcnt(4)\n" \
  "v_mfma_f32_16x16x32_bf16 v[52:55], v[44:47], v[32:35], v[52:55]\n" \
  "s_waitcnt lgkmcnt(3)\n" \
  "v_mfma_f32_16x16x32_bf16 v[56:59], v[48:51], v[32:35], v[56:59]\n" \
  "v_mfma_f32_16x16x32_bf16 v[68:71], v[64:67], v[32:35], v[68:71]\n" \
  "ds_read_b128 v[32:35], v5 offset:15360\n" \
  "ds_read_b128 v[44:47], v5 offset:3072\n" \
  "ds_read_b128 v[48:51], v5 offset:7168\n" \
  "s_waitcnt lgkmcnt(4)\n" \
  "v_mfma_f32_16x16x32_bf16 v[52:55], v[36:39], v[28:31], v[52:55]\n" \
  "s_waitcnt lgkmcnt(3)\n" \
  "v_mfma_f32_16x16x32_bf16 v[56:59], v[40:43], v[28:31], v[56:59]\n" \
  "v_mfma_f32_16x16x32_bf16 v[68:71], v[12:15], v[28:31], v[68:71]\n" \
  "s_waitcnt lgkmcnt(1)\n" \
  "v_mfma_f32_16x16x32_bf16 v[52:55], v[44:47], v[32:35], v[52:55]\n" \
  "s_waitcnt lgkmcnt(0)\n" \
  "v_mfma_f32_16x16x32_bf16 v[56:59], v[48:51], v[32:35], v[56:59]\n" \
  "v_mfma_f32_16x16x32_bf16 v[68:71], v[16:19], v[32:35], v[68:71]\n" \
  "ds_read_b128 v[36:39], v20 offset:8192\n" \
  "ds_read_b128 v[40:43], v20 offset:9216\n" \
  "ds_read_b64 v[12:13], v21 offset:21504\n" \
  "ds_read_b64 v[14:15], v21 offset:22016\n" \
  "ds_read_b128 v[28:31], v0 offset:25600\n" \
  "ds_read_b128 v[32:35], v0 offset:26624\n" \
  "v_cndmask_b32 v52, v52, v22, s[64:65]\n" \
  "ds_read_b128 v[72:75], v23 offset:0\n" \
  "v_cndmask_b32 v53, v53, v22, s[66:67]\n" \
  "ds_read_b128 v[76:79], v23 offset:4096\n" \
  "v_cndmask_b32 v54, v54, v22, s[68:69]\n" \
  "ds_read_b128 v[80:83], v23 offset:8192\n" \
  "v_cndmask_b32 v55, v55, v22, s[70:71]\n" \
  "ds_read_b128 v[84:87], v23 offset:12288\n" \
  "v_cndmask_b32 v56, v56, v22, s[72:73]\n" \
  "ds_read_b128 v[88:91], v23 offset:16384\n" \
  "v_cndmask_b32 v57, v57, v22, s[74:75]\n" \
  "ds_read_b128 v[92:95], v23 offset:20480\n" \
  "v_cndmask_b32 v58, v58, v22, s[76:77]\n" \
  "ds_read_b128 v[96:99], v23 offset:24576\n" \
  "v_cndmask_b32 v59, v59, v22, s[78:79]\n" \
  "ds_read_b128 v[100:103], v23 offset:28672\n" \
  "ds_read_b128 v[104:107], v23 offset:32768\n" \
  "ds_read_b128 v[108:111], v23 offset:36864\n" \
  "v_max3_f32 v10, v52, v53, v54\n" \
  "v_max3_f32 v10, v10, v55, v56\n" \
  "v_max3_f32 v10, v10, v57, v58\n" \
  "v_add_f32 v68, v68, v70\n" \
  "v_mul_f32 v68, s60, v68\n" \
  "v_add_f32 v69, v69, v71\n" \
  "v_mul_f32 v69, s60, v69\n" \
  "v_max3_f32 v10, v10, v59, v68\n" \
  "v_max_f32 v10, v10, v69\n" \
  "v_mov_b32 v11, v10\n" \
  "s_nop 1\n" \
  "v_permlane16_swap_b32 v10, v11\n" \
  "v_max_f32 v10, v10, v11\n" \
  "v_mov_b32 v11, v10\n" \
  "s_nop 1\n" \
  "v_permlane32_swap_b32 v10, v11\n" \
  "v_max_f32 v10, v10, v11\n" \
  "v_sub_f32 v52, v52, v10\n" \
  "v_sub_f32 v53, v53, v10\n" \
  "v_sub_f32 v54, v54, v10\n" \
  "v_sub_f32 v55, v55, v10\n" \
  "v_sub_f32 v56, v56, v10\n" \
  "v_sub_f32 v57, v57, v10\n" \
  "v_sub_f32 v58, v58, v10\n" \
  "v_sub_f32 v59, v59, v10\n" \
  "v_sub_f32 v68, v68, v10\n" \
  "v_sub_f32 v69, v69, v10\n" \
  "v_exp_f32 v52, v52\n" \
  "v_exp_f32 v53, v53\n" \
  "v_exp_f32 v54, v54\n" \
  "v_exp_f32 v55, v55\n" \
  "v_cvt_pk_bf16_f32 v44, v52, v53\n" \
  "v_exp_f32 v56, v56\n" \
  "v_exp_f32 v57, v57\n" \
  "v_cvt_pk_bf16_f32 v45, v54, v55\n" \
  "v_exp_f32 v58, v58\n" \
  "v_exp_f32 v59, v59\n" \
  "v_cvt_pk_bf16_f32 v46, v56, v57\n" \
  "v_exp_f32 v68, v68\n" \
  "v_exp_f32 v69, v69\n" \
  "v_cvt_pk_bf16_f32 v47, v58, v59\n" \
  "s_waitcnt lgkmcnt(11)\n" \
  "v_cvt_pk_bf16_f32 v28, v68, v69\n" \
  "s_waitcnt lgkmcnt(10)\n" \
  "v_mfma_f32_16x16x32_bf16 v[48:51], v[32:35], v[44:47], 0\n" \
  "v_mfma_f32_16x16x32_bf16 v[60:63], v[36:39], v[44:47], 0\n" \
  "v_mfma_f32_16x16x32_bf16 v[64:67], v[40:43], v[44:47], 0\n" \
  "v_mfma_f32_16x16x32_bf16 v[48:51], v[32:35], v[28:31], v[48:51]\n" \
  "v_mfma_f32_16x16x32_bf16 v[60:63], v[12:15], v[28:31], v[60:63]\n" \
  "v_mfma_f32_16x16x32_bf16 v[64:67], v[14:17], v[28:31], v[64:67]\n" \
  "v_mov_b64 v[52:53], 0\n" \
  "v_mov_b64 v[54:55], 0\n" \
  "v_mov_b64 v[56:57], 0\n" \
  "v_mov_b64 v[58:59], 0\n" \
  "v_mov_b64 v[68:69], 0\n" \
  "v_mov_b64 v[70:71], 0\n" \
  "v_rcp_f32 v10, v48\n" \
  "v_rcp_f32 v11, v48\n" \
  "s_nop 0\n" \
  "v_pk_mul_f32 v[60:61], v[60:61], v[10:11]\n" \
  "v_pk_mul_f32 v[62:63], v[62:63], v[10:11]\n" \
  "v_cvt_pk_bf16_f32 v24, v60, v61\n" \
  "v_cvt_pk_bf16_f32 v25, v62, v63\n" \
  "v_pk_mul_f32 v[64:65], v[64:65], v[10:11]\n" \
  "v_pk_mul_f32 v[66:67], v[66:67], v[10:11]\n" \
  "v_cvt_pk_bf16_f32 v26, v64, v65\n" \
  "v_cvt_pk_bf16_f32 v27, v66, v67\n" \
  "v_mov_b64 v[60:61], 0\n" \
  "v_mov_b64 v[62:63], 0\n" \
  "v_mov_b64 v[64:65], 0\n" \
  "v_mov_b64 v[66:67], 0\n" \
  "s_waitcnt vmcnt(5)\n" \
  "s_cmp_ge_u32 s55, 64\n" \
  "s_cbranch_scc1 RDY1_%=\n" \
  "s_waitcnt vmcnt(0)\n" \
  "ds_read_b128 v[44:47], v0 offset:28672\n" \
  "s_waitcnt lgkmcnt(0)\n" \
  "v_min_u32 v44, v44, v45\n" \
  "v_min3_u32 v44, v44, v46, v47\n" \
  "s_nop 0\n" \
  "v_readfirstlane_b32 s55, v44\n" \
  "s_add_u32 s56, s40, 3\n" \
  "s_min_u32 s56, s56, 64\n" \
  "s_cmp_ge_u32 s55, s56\n" \
  "s_cbranch_scc1 RDY1_%=\n" \
  "RDP1_%=:\n" \
  "buffer_load_dwordx4 v[44:47], off, s[36:39], s23 sc1\n" \
  "s_waitcnt vmcnt(0)\n" \
  "v_min_u32 v44, v44, v45\n" \
  "v_min3_u32 v44, v44, v46, v47\n" \
  "s_nop 0\n" \
  "v_readfirstlane_b32 s55, v44\n" \
  "s_cmp_ge_u32 s55, s56\n" \
  "s_cbranch_scc1 RDY1_%=\n" \
  "s_add_u32 s54, s54, 1\n" \
  "s_cmp_gt_u32 s54, 131072\n" \
  "s_cbranch_scc1 FAIL_%=\n" \
  "s_sleep 2\n" \
  "s_branch RDP1_%=\n" \
  "RDY1_%=:\n" \
  "v_mov_b64 v[28:29], s[32:33]\n" \
  "v_mov_b64 v[30:31], s[32:33]\n" \
  "s_add_u32 s56, s59, s14\n" \
  "s_add_u32 s57, s59, s13\n" \
  "s_cmp_lt_i32 s13, 0\n" \
  "s_barrier\n" \
  "buffer_store_dwordx4 v[28:31], v0, s[36:39], s56 offen offset:0 sc1\n" \
  "buffer_store_dwordx4 v[28:31], v0, s[36:39], s56 offen offset:1024 sc1\n" \
  "s_cbranch_scc1 XA1_%=\n" \
  "buffer_store_dwordx4 v[28:31], v0, s[36:39], s57 offen offset:0 sc1\n" \
  "buffer_store_dwordx4 v[28:31], v0, s[36:39], s57 offen offset:1024 sc1\n" \
  "XA1_%=:\n" \
  "s_nop 1\n" \
  "v_xor_b32 v5, 0x4000, v5\n" \
  "s_add_u32 s40, s40, 1\n" \
  "s_waitcnt lgkmcnt(0)\n" \
  "s_barrier\n" \
  "v_mov_b64 v[28:29], s[32:33]\n" \
  "v_mov_b64 v[30:31], s[32:33]\n" \
  "v_mov_b32 v32, s32\n" \
  "v_mov_b32 v33, 0\n" \
  "v_mov_b32 v34, s32\n" \
  "v_mov_b32 v35, 0\n" \
  "s_and_b32 s56, s25, 1\n" \
  "s_mul_i32 s57, s56, 4224\n" \
  "v_add_u32 v36, s57, v0\n" \
  "s_lshl_b32 s57, s56, 10\n" \
  "v_add_u32 v37, s57, v0\n" \
  "v_and_b32 v38, 256, v0\n" \
  "v_cmp_eq_u32 vcc, 0, v38\n" \
  "s_nop 1\n" \
  "v_cndmask_b32 v29, v29, v33, vcc\n" \
  "v_mov_b32 v31, v29\n" \
  "ds_write_b128 v36, v[28:31] offset:12288\n" \
  "ds_write_b128 v36, v[28:31] offset:13312\n" \
  "ds_write_b128 v36, v[28:31] offset:14400\n" \
  "ds_write_b128 v36, v[28:31] offset:15424\n" \
  "ds_write_b128 v37, v[32:35] offset:21504\n" \
  "s_add_u32 s45, s58, s18\n" \
  "s_add_u32 s46, s45, 131072\n" \
  "s_add_u32 s47, s58, s16\n" \
  "s_mov_b32 s56, s58\n" \
  "s_mov_b32 s58, s59\n" \
  "s_mov_b32 s59, s56\n" \
  "s_add_u32 s56, s40, 1\n" \
  "s_min_u32 s56, s56, 63\n" \
  "s_mul_i32 s56, s56, 24576\n" \
  "s_add_u32 s56, s56, s20\n" \
  "s_add_u32 s49, s56, 8192\n" \
  "s_add_u32 s50, s56, 12288\n" \
  "s_add_u32 s51, s56, s21\n" \
  "s_add_u32 s52, s56, s22\n" \
  "s_xor_b32 s28, s28, 16384\n" \
  "s_sleep 1\n" \
  "buffer_store_dwordx4 v[24:27], v0, s[36:39], s53 offen offset:0 sc1\n" \
  "s_add_u32 s53, s53, 8192\n" \
  "s_branch HX1_%=\n" \
  "HEADX_%=:\n" \
  "v_mov_b64 v[28:29], s[32:33]\n" \
  "v_mov_b64 v[30:31], s[32:33]\n" \
  "v_mov_b32 v32, s32\n" \
  "v_mov_b32 v33, 0\n" \
  "v_mov_b32 v34, s32\n" \
  "v_mov_b32 v35, 0\n" \
  "s_and_b32 s56, s25, 1\n" \
  "s_mul_i32 s57, s56, 4224\n" \
  "v_add_u32 v36, s57, v0\n" \
  "s_lshl_b32 s57, s56, 10\n" \
  "v_add_u32 v37, s57, v0\n" \
  "v_and_b32 v38, 256, v0\n" \
  "v_cmp_eq_u32 vcc, 0, v38\n" \
  "s_nop 1\n" \
  "v_cndmask_b32 v29, v29, v33, vcc\n" \
  "v_mov_b32 v31, v29\n" \
  "ds_write_b128 v36, v[28:31] offset:12288\n" \
  "ds_write_b128 v36, v[28:31] offset:13312\n" \
  "ds_write_b128 v36, v[28:31] offset:14400\n" \
  "ds_write_b128 v36, v[28:31] offset:15424\n" \
  "ds_write_b128 v37, v[32:35] offset:21504\n" \
  "s_add_u32 s45, s58, s18\n" \
  "s_add_u32 s46, s45, 131072\n" \
  "s_add_u32 s47, s58, s16\n" \
  "s_mov_b32 s56, s58\n" \
  "s_mov_b32 s58, s59\n" \
  "s_mov_b32 s59, s56\n" \
  "s_add_u32 s56, s40, 1\n" \
  "s_min_u32 s56, s56, 63\n" \
  "s_mul_i32 s56, s56, 24576\n" \
  "s_add_u32 s56, s56, s20\n" \
  "s_add_u32 s49, s56, 8192\n" \
  "s_add_u32 s50, s56, 12288\n" \
  "s_add_u32 s51, s56, s21\n" \
  "s_add_u32 s52, s56, s22\n" \
  "s_xor_b32 s28, s28, 16384\n" \
  "HX1_%=:\n" \
  "v_lshlrev_b32 v11, 3, v0\n" \
  "v_min_u32 v11, 2944, v11\n" \
  "s_sub_u32 s56, s49, 8192\n" \
  "s_add_u32 s56, s56, s80\n" \
  "s_add_u32 m0, 0, 29184\n" \
  "s_nop 0\n" \
  "buffer_load_dword v11, s[36:39], s56 offen sc1 lds\n" \
  "s_sub_u32 s56, s40, 1\n" \
  "s_max_i32 s56, s56, 0\n" \
  "v_mov_b32 v10, s56\n" \
  "buffer_store_dword v10, off, s[36:39], s24 offset:0 sc1\n" \
  "s_cmp_ge_u32 s25, 2\n" \
  "s_waitcnt lgkmcnt(0)\n" \
  "s_branch GW_%=\n" \
  "JOIN_%=:\n" \
  "v_xor_b32 v5, 0x4000, v5\n" \
  "s_add_u32 s40, s40, 1\n" \
  "s_waitcnt lgkmcnt(0)\n" \
  "s_barrier\n" \
  "s_branch LOOP_%=\n" \
  "GCHK_%=:\n" \
  "s_memrealtime s[82:83]\n" \
  "s_cmp_eq_u32 s11, s40\n" \
  "s_cbranch_scc1 GOK_%=\n" \
  "v_fma_f32 v10, v12, v13, v14\n" \
  "v_add_f32 v10, v10, v15\n" \
  "v_fma_f32 v10, v16, v17, v10\n" \
  "v_fma_f32 v10, v18, v19, v10\n" \
  "v_fma_f32 v10, v20, v21, v10\n" \
  "v_fma_f32 v10, v22, v23, v10\n" \
  "v_fma_f32 v10, v24, v25, v10\n" \
  "v_fma_f32 v10, v26, v27, v10\n" \
  "v_fma_f32 v10, v28, v29, v10\n" \
  "v_fma_f32 v10, v30, v31, v10\n" \
  "v_fma_f32 v10, v32, v33, v10\n" \
  "v_fma_f32 v10, v34, v35, v10\n" \
  "v_fma_f32 v10, v36, v37, v10\n" \
  "v_fma_f32 v10, v38, v39, v10\n" \
  "v_fma_f32 v10, v40, v41, v10\n" \
  "v_fma_f32 v10, v42, v43, v10\n" \
  "v_fma_f32 v10, v44, v45, v10\n" \
  "v_fma_f32 v10, v46, v47, v10\n" \
  "v_fma_f32 v10, v48, v49, v10\n" \
  "v_fma_f32 v10, v50, v51, v10\n" \
  "v_cmp_u_f32 vcc, v10, v10\n" \
  "s_waitcnt lgkmcnt(0)\n" \
  "s_sub_u32 s56, s82, s48\n" \
  "s_add_u32 s56, s56, 60\n" \
  "s_min_u32 s56, s56, 1200\n" \
  "s_mul_i32 s56, s56, 1093\n" \
  "s_lshr_b32 s56, s56, 16\n" \
  "s_mul_i32 s56, s56, 60\n" \
  "s_add_u32 s61, s56, s48\n" \
  "s_or_b32 s41, s41, 131072\n" \
  "s_add_u32 s42, s41, 4096\n" \
  "s_xor_b32 s43, s41, 16384\n" \
  "s_cbranch_vccz GOK_%=\n" \
  "WUG2_%=:\n" \
  "s_memrealtime s[82:83]\n" \
  "s_waitcnt lgkmcnt(0)\n" \
  "s_sub_u32 s56, s61, s82\n" \
  "s_sub_u32 s57, s56, 4\n" \
  "s_cmp_lt_u32 s57, 400\n" \
  "s_cbranch_scc1 WUG2_%=\n" \
  "s_cmp_eq_u32 s56, 3\n" \
  "s_cbranch_scc1 WP3G2_%=\n" \
  "s_cmp_eq_u32 s56, 2\n" \
  "s_cbranch_scc1 WP2G2_%=\n" \
  "s_cmp_eq_u32 s56, 1\n" \
  "s_cbranch_scc1 WP1G2_%=\n" \
  "s_branch WP0G2_%=\n" \
  "WP3G2_%=:\n" \
  "s_nop 7\n" \
  "WP2G2_%=:\n" \
  "s_nop 7\n" \
  "WP1G2_%=:\n" \
  "s_nop 6\n" \
  "WP0G2_%=:\n" \
  "s_barrier\n" \
  "s_cmp_ge_u32 s25, 2\n" \
  "s_cbranch_scc1 GC1_%=\n" \
  "s_add_u32 m0, s12, 0\n" \
  "s_nop 0\n" \
  "buffer_load_dwordx4 v2, s[36:39], s41 offen offset:0 sc1 lds\n" \
  "buffer_load_dwordx4 v2, s[36:39], s41 offen offset:1024 sc1 lds\n" \
  "s_add_u32 m0, s12, 4096\n" \
  "s_nop 0\n" \
  "buffer_load_dwordx4 v2, s[36:39], s42 offen offset:0 sc1 lds\n" \
  "buffer_load_dwordx4 v2, s[36:39], s42 offen offset:1024 sc1 lds\n" \
  "s_add_u32 m0, s27, 8192\n" \
  "s_nop 0\n" \
  "buffer_load_dwordx4 v1, s[36:39], s43 offen sc1 lds\n" \
  "v_bfe_u32 v11, v0, 2, 4\n" \
  "s_add_u32 s56, s43, 2048\n" \
  "s_add_u32 m0, 0, 10240\n" \
  "s_nop 0\n" \
  "buffer_load_dword v11, s[36:39], s56 offen sc1 lds\n" \
  "GC1_%=:\n" \
  "s_add_u32 s54, s54, 1\n" \
  "s_cmp_gt_u32 s54, 131072\n" \
  "s_cbranch_scc1 FAIL_%=\n" \
  "s_mov_b32 s81, 0\n" \
  "v_add_u32 v11, 65536, v1\n" \
  "ds_read_b128 v[72:75], v11 offset:0\n" \
  "v_mov_b64 v[52:53], 0\n" \
  "ds_read_b128 v[76:79], v11 offset:4096\n" \
  "v_mov_b64 v[54:55], 0\n" \
  "ds_read_b128 v[80:83], v11 offset:8192\n" \
  "v_mov_b64 v[56:57], 0\n" \
  "ds_read_b128 v[84:87], v11 offset:12288\n" \
  "v_mov_b64 v[58:59], 0\n" \
  "ds_read_b128 v[88:91], v11 offset:16384\n" \
  "v_mov_b64 v[60:61], 0\n" \
  "ds_read_b128 v[92:95], v11 offset:20480\n" \
  "v_mov_b64 v[62:63], 0\n" \
  "ds_read_b128 v[96:99], v11 offset:24576\n" \
  "v_mov_b64 v[64:65], 0\n" \
  "ds_read_b128 v[100:103], v11 offset:28672\n" \
  "v_mov_b64 v[66:67], 0\n" \
  "ds_read_b128 v[104:107], v11 offset:32768\n" \
  "v_mov_b64 v[68:69], 0\n" \
  "ds_read_b128 v[108:111], v11 offset:36864\n" \
  "v_mov_b64 v[70:71], 0\n" \
  "s_or_b32 s56, s40, 256\n" \
  "s_cmp_eq_u32 s11, s56\n" \
  "s_mov_b32 s11, s56\n" \
  "s_cbranch_scc0 GC2_%=\n" \
  "v_xor_b32 v10, s32, v12\n" \
  "v_xor_b32 v11, s32, v13\n" \
  "v_min_u32 v10, v10, v11\n" \
  "v_xor_b32 v11, s32, v14\n" \
  "v_min_u32 v10, v10, v11\n" \
  "v_xor_b32 v11, s32, v15\n" \
  "v_min_u32 v10, v10, v11\n" \
  "v_xor_b32 v11, s32, v16\n" \
  "v_min_u32 v10, v10, v11\n" \
  "v_xor_b32 v11, s32, v17\n" \
  "v_min_u32 v10, v10, v11\n" \
  "v_xor_b32 v11, s32, v18\n" \
  "v_min_u32 v10, v10, v11\n" \
  "v_xor_b32 v11, s32, v19\n" \
  "v_min_u32 v10, v10, v11\n" \
  "v_xor_b32 v11, s32, v20\n" \
  "v_min_u32 v10, v10, v11\n" \
  "v_xor_b32 v11, s32, v21\n" \
  "v_min_u32 v10, v10, v11\n" \
  "v_xor_b32 v11, s32, v22\n" \
  "v_min_u32 v10, v10, v11\n" \
  "v_xor_b32 v11, s32, v23\n" \
  "v_min_u32 v10, v10, v11\n" \
  "v_xor_b32 v11, s32, v24\n" \
  "v_min_u32 v10, v10, v11\n" \
  "v_xor_b32 v11, s32, v25\n" \
  "v_min_u32 v10, v10, v11\n" \
  "v_xor_b32 v11, s32, v26\n" \
  "v_min_u32 v10, v10, v11\n" \
  "v_xor_b32 v11, s32, v27\n" \
  "v_min_u32 v10, v10, v11\n" \
  "v_xor_b32 v11, s32, v28\n" \
  "v_min_u32 v10, v10, v11\n" \
  "v_xor_b32 v11, s32, v29\n" \
  "v_min_u32 v10, v10, v11\n" \
  "v_xor_b32 v11, s32, v30\n" \
  "v_min_u32 v10, v10, v11\n" \
  "v_xor_b32 v11, s32, v31\n" \
  "v_min_u32 v10, v10, v11\n" \
  "v_xor_b32 v11, s32, v32\n" \
  "v_min_u32 v10, v10, v11\n" \
  "v_xor_b32 v11, s32, v33\n" \
  "v_min_u32 v10, v10, v11\n" \
  "v_xor_b32 v11, s32, v34\n" \
  "v_min_u32 v10, v10, v11\n" \
  "v_xor_b32 v11, s32, v35\n" \
  "v_min_u32 v10, v10, v11\n" \
  "v_xor_b32 v11, s32, v36\n" \
  "v_min_u32 v10, v10, v11\n" \
  "v_xor_b32 v11, s32, v37\n" \
  "v_min_u32 v10, v10, v11\n" \
  "v_xor_b32 v11, s32, v38\n" \
  "v_min_u32 v10, v10, v11\n" \
  "v_xor_b32 v11, s32, v39\n" \
  "v_min_u32 v10, v10, v11\n" \
  "v_xor_b32 v11, s32, v40\n" \
  "v_min_u32 v10, v10, v11\n" \
  "v_xor_b32 v11, s32, v41\n" \
  "v_min_u32 v10, v10, v11\n" \
  "v_xor_b32 v11, s32, v42\n" \
  "v_min_u32 v10, v10, v11\n" \
  "v_xor_b32 v11, s32, v43\n" \
  "v_min_u32 v10, v10, v11\n" \
  "v_xor_b32 v11, s32, v44\n" \
  "v_min_u32 v10, v10, v11\n" \
  "v_xor_b32 v11, s32, v45\n" \
  "v_min_u32 v10, v10, v11\n" \
  "v_xor_b32 v11, s32, v46\n" \
  "v_min_u32 v10, v10, v11\n" \
  "v_xor_b32 v11, s32, v47\n" \
  "v_min_u32 v10, v10, v11\n" \
  "v_xor_b32 v11, s32, v48\n" \
  "v_min_u32 v10, v10, v11\n" \
  "v_xor_b32 v11, s32, v49\n" \
  "v_min_u32 v10, v10, v11\n" \
  "v_xor_b32 v11, s32, v50\n" \
  "v_min_u32 v10, v10, v11\n" \
  "v_xor_b32 v11, s32, v51\n" \
  "v_min_u32 v10, v10, v11\n" \
  "v_cmp_eq_u32 vcc, 0, v10\n" \
  "s_nop 3\n" \
  "s_cbranch_vccnz GC2_%=\n" \
  "s_mov_b32 s11, s40\n" \
  "GC2_%=:\n" \
  "s_cmp_ge_u32 s25, 2\n" \
  "s_cbranch_scc1 GC3_%=\n" \
  "s_waitcnt vmcnt(4) lgkmcnt(0)\n" \
  "s_branch GW_%=\n" \
  "GC3_%=:\n" \
  "v_mov_b64 v[28:29], s[32:33]\n" \
  "v_mov_b64 v[30:31], s[32:33]\n" \
  "v_mov_b32 v32, s32\n" \
  "v_mov_b32 v33, 0\n" \
  "v_mov_b32 v34, s32\n" \
  "v_mov_b32 v35, 0\n" \
  "s_and_b32 s56, s25, 1\n" \
  "s_mul_i32 s57, s56, 4224\n" \
  "v_add_u32 v36, s57, v0\n" \
  "s_lshl_b32 s57, s56, 10\n" \
  "v_add_u32 v37, s57, v0\n" \
  "v_and_b32 v38, 256, v0\n" \
  "v_cmp_eq_u32 vcc, 0, v38\n" \
  "s_nop 1\n" \
  "v_cndmask_b32 v29, v29, v33, vcc\n" \
  "v_mov_b32 v31, v29\n" \
  "ds_write_b128 v36, v[28:31] offset:12288\n" \
  "ds_write_b128 v36, v[28:31] offset:13312\n" \
  "ds_write_b128 v36, v[28:31] offset:14400\n" \
  "ds_write_b128 v36, v[28:31] offset:15424\n" \
  "ds_write_b128 v37, v[32:35] offset:21504\n" \
  "s_cmp_ge_u32 s25, 2\n" \
  "s_waitcnt lgkmcnt(0)\n" \
  "s_branch GW_%=\n" \
  "RCHK_%=:\n" \
  "s_memrealtime s[82:83]\n" \
  "v_xor_b32 v36, s32, v16\n" \
  "v_xor_b32 v37, s32, v17\n" \
  "v_min_u32 v36, v36, v37\n" \
  "v_xor_b32 v37, s32, v18\n" \
  "v_min_u32 v36, v36, v37\n" \
  "v_xor_b32 v37, s32, v19\n" \
  "v_min_u32 v36, v36, v37\n" \
  "v_xor_b32 v37, s32, v20\n" \
  "v_min_u32 v36, v36, v37\n" \
  "v_xor_b32 v37, s32, v21\n" \
  "v_min_u32 v36, v36, v37\n" \
  "v_xor_b32 v37, s32, v22\n" \
  "v_min_u32 v36, v36, v37\n" \
  "v_xor_b32 v37, s32, v23\n" \
  "v_min_u32 v36, v36, v37\n" \
  "v_xor_b32 v37, s32, v24\n" \
  "v_min_u32 v36, v36, v37\n" \
  "v_xor_b32 v37, s32, v25\n" \
  "v_min_u32 v36, v36, v37\n" \
  "v_xor_b32 v37, s32, v26\n" \
  "v_min_u32 v36, v36, v37\n" \
  "v_xor_b32 v37, s32, v27\n" \
  "v_min_u32 v36, v36, v37\n" \
  "v_xor_b32 v37, s32, v28\n" \
  "v_min_u32 v36, v36, v37\n" \
  "v_xor_b32 v37, s32, v29\n" \
  "v_min_u32 v36, v36, v37\n" \
  "v_xor_b32 v37, s32, v30\n" \
  "v_min_u32 v36, v36, v37\n" \
  "v_xor_b32 v37, s32, v31\n" \
  "v_min_u32 v36, v36, v37\n" \
  "v_cmp_eq_u32 vcc, 0, v36\n" \
  "s_waitcnt lgkmcnt(0)\n" \
  "s_cbranch_vccz RDONE_%=\n" \
  "s_add_u32 s54, s54, 1\n" \
  "s_cmp_gt_u32 s54, 131072\n" \
  "s_cbranch_scc1 FAIL_%=\n" \
  "s_mov_b32 s81, 0\n" \
  "s_sub_u32 s56, s82, s61\n" \
  "s_add_u32 s56, s56, 60\n" \
  "s_min_u32 s56, s56, 1200\n" \
  "s_mul_i32 s56, s56, 1093\n" \
  "s_lshr_b32 s56, s56, 16\n" \
  "s_mul_i32 s56, s56, 60\n" \
  "s_add_u32 s31, s56, s61\n" \
  "WUR2_%=:\n" \
  "s_memrealtime s[82:83]\n" \
  "s_waitcnt lgkmcnt(0)\n" \
  "s_sub_u32 s56, s31, s82\n" \
  "s_sub_u32 s57, s56, 4\n" \
  "s_cmp_lt_u32 s57, 400\n" \
  "s_cbranch_scc1 WUR2_%=\n" \
  "s_cmp_eq_u32 s56, 3\n" \
  "s_cbranch_scc1 WP3R2_%=\n" \
  "s_cmp_eq_u32 s56, 2\n" \
  "s_cbranch_scc1 WP2R2_%=\n" \
  "s_cmp_eq_u32 s56, 1\n" \
  "s_cbranch_scc1 WP1R2_%=\n" \
  "s_branch WP0R2_%=\n" \
  "WP3R2_%=:\n" \
  "s_nop 7\n" \
  "WP2R2_%=:\n" \
  "s_nop 7\n" \
  "WP1R2_%=:\n" \
  "s_nop 6\n" \
  "WP0R2_%=:\n" \
  "buffer_load_dwordx4 v[16:19], v0, s[36:39], s47 offen offset:0 sc1\n" \
  "buffer_load_dwordx4 v[20:23], v0, s[36:39], s47 offen offset:1024 sc1\n" \
  "buffer_load_dwordx4 v[24:27], v0, s[36:39], s47 offen offset:2048 sc1\n" \
  "buffer_load_dwordx4 v[28:31], v0, s[36:39], s47 offen offset:3072 sc1\n" \
  "v_mov_b32 v41, 0\n" \
  "ds_read_b64 v[50:51], v41 offset:29696\n" \
  "v_or_b32 v41, s96, v9\n" \
  "s_add_u32 s56, s99, 10\n" \
  "s_add_u32 s56, s56, s31\n" \
  "s_waitcnt lgkmcnt(0)\n" \
  "v_add_u32 v42, s56, v50\n" \
  "v_add_u32 v43, 9, v51\n" \
  "s_add_u32 s57, s44, 17920\n" \
  "buffer_store_dwordx2 v[42:43], v41, s[36:39], s57 offen offset:0 sc1\n" \
  "s_waitcnt vmcnt(4)\n" \
  "v_mfma_f32_16x16x32_bf16 v[32:35], v[12:15], v[16:19], 0\n" \
  "v_fma_f32 v36, v16, v17, v18\n" \
  "v_add_f32 v36, v36, v19\n" \
  "s_waitcnt vmcnt(3)\n" \
  "v_mfma_f32_16x16x32_bf16 v[32:35], v[12:15], v[20:23], v[32:35]\n" \
  "v_fma_f32 v36, v20, v21, v36\n" \
  "v_fma_f32 v36, v22, v23, v36\n" \
  "s_waitcnt vmcnt(2)\n" \
  "v_mfma_f32_16x16x32_bf16 v[32:35], v[12:15], v[24:27], v[32:35]\n" \
  "v_fma_f32 v36, v24, v25, v36\n" \
  "v_fma_f32 v36, v26, v27, v36\n" \
  "s_waitcnt vmcnt(1)\n" \
  "v_mfma_f32_16x16x32_bf16 v[32:35], v[12:15], v[28:31], v[32:35]\n" \
  "v_fma_f32 v36, v28, v29, v36\n" \
  "v_fma_f32 v36, v30, v31, v36\n" \
  "v_cmp_u_f32 vcc, v36, v36\n" \
  "s_cbranch_vccnz RCHK_%=\n" \
  "s_nop 3\n" \
  "s_branch RDONE_%=\n" \
  "LATE_%=:\n" \
  "s_cmp_lg_u32 s40, 0\n" \
  "s_cbranch_scc1 LATE1_%=\n" \
  "v_lshlrev_b32 v18, 2, v0\n" \
  "s_add_u32 s57, s26, 1081344\n" \
  "s_add_u32 s61, s57, 4096\n" \
  "ARP_%=:\n" \
  "buffer_load_dword v12, v18, s[36:39], s57 offen offset:0 sc1\n" \
  "buffer_load_dword v13, v18, s[36:39], s61 offen offset:0 sc1\n" \
  "s_waitcnt vmcnt(0)\n" \
  "v_min_u32 v14, v12, v13\n" \
  "v_cmp_eq_u32 vcc, 0, v14\n" \
  "s_nop 3\n" \
  "s_cbranch_vccz ARD_%=\n" \
  "s_add_u32 s54, s54, 1\n" \
  "s_cmp_gt_u32 s54, 131072\n" \
  "s_cbranch_scc1 FAIL_%=\n" \
  "s_sleep 3\n" \
  "s_branch ARP_%=\n" \
  "ARD_%=:\n" \
  "s_sub_u32 s56, s82, 100000\n" \
  "v_subrev_u32 v12, s56, v12\n" \
  "v_subrev_u32 v13, s56, v13\n" \
  "v_max_u32 v12, v12, v13\n" \
  "s_nop 1\n" \
  "v_max_u32_dpp v12, v12, v12 row_ror:1 row_mask:0xf bank_mask:0xf\n" \
  "s_nop 1\n" \
  "v_max_u32_dpp v12, v12, v12 row_ror:2 row_mask:0xf bank_mask:0xf\n" \
  "s_nop 1\n" \
  "v_max_u32_dpp v12, v12, v12 row_ror:4 row_mask:0xf bank_mask:0xf\n" \
  "s_nop 1\n" \
  "v_max_u32_dpp v12, v12, v12 row_ror:8 row_mask:0xf bank_mask:0xf\n" \
  "v_mov_b32 v14, v12\n" \
  "s_nop 1\n" \
  "v_permlane16_swap_b32 v12, v14\n" \
  "v_max_u32 v12, v12, v14\n" \
  "v_mov_b32 v14, v12\n" \
  "s_nop 1\n" \
  "v_permlane32_swap_b32 v12, v14\n" \
  "v_max_u32 v12, v12, v14\n" \
  "s_nop 0\n" \
  "v_readfirstlane_b32 s31, v12\n" \
  "s_nop 0\n" \
  "s_add_u32 s31, s31, s56\n" \
  "s_add_u32 s31, s31, 138\n" \
  "s_mov_b32 s61, s31\n" \
  "s_add_u32 s48, s31, s99\n" \
  "s_branch WQR_%=\n" \
  "LATE1_%=:\n" \
  "s_sub_u32 s56, s82, s61\n" \
  "s_add_u32 s56, s56, 71\n" \
  "s_min_u32 s56, s56, 1200\n" \
  "s_mul_i32 s56, s56, 1093\n" \
  "s_lshr_b32 s56, s56, 16\n" \
  "s_mul_i32 s56, s56, 60\n" \
  "s_add_u32 s31, s56, s61\n" \
  "s_branch WQR_%=\n" \
  "SLOWN_%=:\n" \
  "s_cmp_eq_u32 s10, s40\n" \
  "s_cbranch_scc1 YOK_%=\n" \
  "SLOW_%=:\n" \
  "s_nop 7\n" \
  "s_waitcnt lgkmcnt(0)\n" \
  "s_mov_b32 s10, s40\n" \
  "SLW_%=:\n" \
  "ds_read_b128 v[20:23], v0 offset:12288\n" \
  "ds_read_b128 v[24:27], v0 offset:13312\n" \
  "ds_read_b128 v[28:31], v0 offset:14400\n" \
  "ds_read_b128 v[32:35], v0 offset:15424\n" \
  "ds_read_b128 v[36:39], v0 offset:16512\n" \
  "ds_read_b128 v[40:43], v0 offset:17536\n" \
  "ds_read_b128 v[44:47], v0 offset:18624\n" \
  "ds_read_b128 v[48:51], v0 offset:19648\n" \
  "ds_read_b128 v[96:99], v0 offset:21504\n" \
  "ds_read_b128 v[100:103], v0 offset:22528\n" \
  "s_waitcnt lgkmcnt(9)\n" \
  "v_xor_b32 v10, s32, v20\n" \
  "v_xor_b32 v11, s32, v21\n" \
  "v_min_u32 v10, v10, v11\n" \
  "v_xor_b32 v11, s32, v22\n" \
  "v_min_u32 v10, v10, v11\n" \
  "v_xor_b32 v11, s32, v23\n" \
  "v_min_u32 v10, v10, v11\n" \
  "s_waitcnt lgkmcnt(8)\n" \
  "v_xor_b32 v11, s32, v24\n" \
  "v_min_u32 v10, v10, v11\n" \
  "v_xor_b32 v11, s32, v25\n" \
  "v_min_u32 v10, v10, v11\n" \
  "v_xor_b32 v11, s32, v26\n" \
  "v_min_u32 v10, v10, v11\n" \
  "v_xor_b32 v11, s32, v27\n" \
  "v_min_u32 v10, v10, v11\n" \
  "s_waitcnt lgkmcnt(7)\n" \
  "v_xor_b32 v11, s32, v28\n" \
  "v_min_u32 v10, v10, v11\n" \
  "v_xor_b32 v11, s32, v29\n" \
  "v_min_u32 v10, v10, v11\n" \
  "v_xor_b32 v11, s32, v30\n" \
  "v_min_u32 v10, v10, v11\n" \
  "v_xor_b32 v11, s32, v31\n" \
  "v_min_u32 v10, v10, v11\n" \
  "s_waitcnt lgkmcnt(6)\n" \
  "v_xor_b32 v11, s32, v32\n" \
  "v_min_u32 v10, v10, v11\n" \
  "v_xor_b32 v11, s32, v33\n" \
  "v_min_u32 v10, v10, v11\n" \
  "v_xor_b32 v11, s32, v34\n" \
  "v_min_u32 v10, v10, v11\n" \
  "v_xor_b32 v11, s32, v35\n" \
  "v_min_u32 v10, v10, v11\n" \
  "s_waitcnt lgkmcnt(5)\n" \
  "v_xor_b32 v11, s32, v36\n" \
  "v_min_u32 v10, v10, v11\n" \
  "v_xor_b32 v11, s32, v37\n" \
  "v_min_u32 v10, v10, v11\n" \
  "v_xor_b32 v11, s32, v38\n" \
  "v_min_u32 v10, v10, v11\n" \
  "v_xor_b32 v11, s32, v39\n" \
  "v_min_u32 v10, v10, v11\n" \
  "s_waitcnt lgkmcnt(4)\n" \
  "v_xor_b32 v11, s32, v40\n" \
  "v_min_u32 v10, v10, v11\n" \
  "v_xor_b32 v11, s32, v41\n" \
  "v_min_u32 v10, v10, v11\n" \
  "v_xor_b32 v11, s32, v42\n" \
  "v_min_u32 v10, v10, v11\n" \
  "v_xor_b32 v11, s32, v43\n" \
  "v_min_u32 v10, v10, v11\n" \
  "s_waitcnt lgkmcnt(3)\n" \
  "v_xor_b32 v11, s32, v44\n" \
  "v_min_u32 v10, v10, v11\n" \
  "v_xor_b32 v11, s32, v45\n" \
  "v_min_u32 v10, v10, v11\n" \
  "v_xor_b32 v11, s32, v46\n" \
  "v_min_u32 v10, v10, v11\n" \
  "v_xor_b32 v11, s32, v47\n" \
  "v_min_u32 v10, v10, v11\n" \
  "s_waitcnt lgkmcnt(2)\n" \
  "v_xor_b32 v11, s32, v48\n" \
  "v_min_u32 v10, v10, v11\n" \
  "v_xor_b32 v11, s32, v49\n" \
  "v_min_u32 v10, v10, v11\n" \
  "v_xor_b32 v11, s32, v50\n" \
  "v_min_u32 v10, v10, v11\n" \
  "v_xor_b32 v11, s32, v51\n" \
  "v_min_u32 v10, v10, v11\n" \
  "s_waitcnt lgkmcnt(1)\n" \
  "v_xor_b32 v11, s32, v96\n" \
  "v_min_u32 v10, v10, v11\n" \
  "v_xor_b32 v11, s32, v97\n" \
  "v_min_u32 v10, v10, v11\n" \
  "v_xor_b32 v11, s32, v98\n" \
  "v_min_u32 v10, v10, v11\n" \
  "v_xor_b32 v11, s32, v99\n" \
  "v_min_u32 v10, v10, v11\n" \
  "s_waitcnt lgkmcnt(0)\n" \
  "v_xor_b32 v11, s32, v100\n" \
  "v_min_u32 v10, v10, v11\n" \
  "v_xor_b32 v11, s32, v101\n" \
  "v_min_u32 v10, v10, v11\n" \
  "v_xor_b32 v11, s32, v102\n" \
  "v_min_u32 v10, v10, v11\n" \
  "v_xor_b32 v11, s32, v103\n" \
  "v_min_u32 v10, v10, v11\n" \
  "v_cmp_eq_u32 vcc, 0, v10\n" \
  "s_nop 3\n" \
  "s_cbranch_vccz SLG_%=\n" \
  "s_add_u32 s54, s54, 1\n" \
  "s_cmp_gt_u32 s54, 131072\n" \
  "s_cbranch_scc1 FAIL_%=\n" \
  "s_branch SLW_%=\n" \
  "SLG_%=:\n" \
  "ds_read_b64_tr_b16 v[72:73], v4 offset:12288\n" \
  "ds_read_b64_tr_b16 v[60:61], v4 offset:12544\n" \
  "ds_read_b64_tr_b16 v[74:75], v4 offset:12416\n" \
  "ds_read_b64_tr_b16 v[62:63], v4 offset:12672\n" \
  "ds_read_b64_tr_b16 v[76:77], v4 offset:12800\n" \
  "ds_read_b64_tr_b16 v[64:65], v4 offset:13056\n" \
  "ds_read_b64_tr_b16 v[78:79], v4 offset:12928\n" \
  "ds_read_b64_tr_b16 v[66:67], v4 offset:13184\n" \
  "ds_read_b64_tr_b16 v[80:81], v4 offset:13312\n" \
  "ds_read_b64_tr_b16 v[12:13], v4 offset:13568\n" \
  "ds_read_b64_tr_b16 v[82:83], v4 offset:13440\n" \
  "ds_read_b64_tr_b16 v[14:15], v4 offset:13696\n" \
  "ds_read_b64_tr_b16 v[84:85], v4 offset:13824\n" \
  "ds_read_b64_tr_b16 v[16:17], v4 offset:14080\n" \
  "ds_read_b64_tr_b16 v[86:87], v4 offset:13952\n" \
  "ds_read_b64_tr_b16 v[18:19], v4 offset:14208\n" \
  "v_lshrrev_b32 v45, 1, v0\n" \
  "ds_read_b64 v[52:53], v45 offset:21504\n" \
  "ds_read_b64 v[54:55], v45 offset:22016\n" \
  "ds_read_b64 v[56:57], v45 offset:22528\n" \
  "ds_read_b64 v[58:59], v45 offset:23040\n" \
  "s_waitcnt lgkmcnt(0)\n" \
  "ds_read_b128 v[28:31], v5 offset:0\n" \
  "ds_read_b128 v[32:35], v5 offset:4096\n" \
  "ds_read_b128 v[36:39], v5 offset:1024\n" \
  "ds_read_b128 v[40:43], v5 offset:5120\n" \
  "ds_read_b128 v[96:99], v5 offset:2048\n" \
  "ds_read_b128 v[100:103], v5 offset:6144\n" \
  "ds_read_b128 v[104:107], v5 offset:3072\n" \
  "ds_read_b128 v[108:111], v5 offset:7168\n" \
  "s_waitcnt lgkmcnt(7)\n" \
  "v_mfma_f32_16x16x32_bf16 v[44:47], v[28:31], v[72:75], 0\n" \
  "s_waitcnt lgkmcnt(6)\n" \
  "v_mfma_f32_16x16x32_bf16 v[48:51], v[32:35], v[72:75], 0\n" \
  "s_waitcnt lgkmcnt(5)\n" \
  "v_mfma_f32_16x16x32_bf16 v[44:47], v[36:39], v[76:79], v[44:47]\n" \
  "s_waitcnt lgkmcnt(4)\n" \
  "v_mfma_f32_16x16x32_bf16 v[48:51], v[40:43], v[76:79], v[48:51]\n" \
  "s_waitcnt lgkmcnt(3)\n" \
  "v_mfma_f32_16x16x32_bf16 v[44:47], v[96:99], v[80:83], v[44:47]\n" \
  "s_waitcnt lgkmcnt(2)\n" \
  "v_mfma_f32_16x16x32_bf16 v[48:51], v[100:103], v[80:83], v[48:51]\n" \
  "s_waitcnt lgkmcnt(1)\n" \
  "v_mfma_f32_16x16x32_bf16 v[44:47], v[104:107], v[84:87], v[44:47]\n" \
  "s_waitcnt lgkmcnt(0)\n" \
  "v_mfma_f32_16x16x32_bf16 v[48:51], v[108:111], v[84:87], v[48:51]\n" \
  "v_mfma_f32_16x16x32_bf16 v[24:27], v[60:63], v[72:75], 0\n" \
  "v_mfma_f32_16x16x32_bf16 v[24:27], v[64:67], v[76:79], v[24:27]\n" \
  "v_mfma_f32_16x16x32_bf16 v[24:27], v[12:15], v[80:83], v[24:27]\n" \
  "v_mfma_f32_16x16x32_bf16 v[24:27], v[16:19], v[84:87], v[24:27]\n" \
  "ds_read_b128 v[96:99], v5 offset:8192\n" \
  "ds_read_b128 v[100:103], v5 offset:9216\n" \
  "ds_read_b128 v[104:107], v5 offset:10240\n" \
  "ds_read_b128 v[108:111], v5 offset:11264\n" \
  "v_max3_f32 v10, v44, v45, v46\n" \
  "v_max3_f32 v10, v10, v47, v48\n" \
  "v_max3_f32 v10, v10, v49, v50\n" \
  "v_max_f32 v10, v10, v51\n" \
  "v_mov_b32 v11, v10\n" \
  "s_nop 1\n" \
  "v_permlane16_swap_b32 v10, v11\n" \
  "v_max_f32 v10, v10, v11\n" \
  "v_mov_b32 v11, v10\n" \
  "s_nop 1\n" \
  "v_permlane32_swap_b32 v10, v11\n" \
  "v_max_f32 v10, v10, v11\n" \
  "v_add_f32 v68, v24, v26\n" \
  "v_fma_f32 v68, v68, s60, -v10\n" \
  "v_exp_f32 v68, v68\n" \
  "v_add_f32 v69, v25, v27\n" \
  "v_fma_f32 v69, v69, s60, -v10\n" \
  "v_exp_f32 v69, v69\n" \
  "s_nop 0\n" \
  "v_max_f32 v11, v68, v69\n" \
  "v_cmp_lt_f32 vcc, s35, v11\n" \
  "v_cvt_pk_bf16_f32 v88, v68, v69\n" \
  "s_waitcnt lgkmcnt(0)\n" \
  "s_nop 3\n" \
  "s_cbranch_vccnz SLX_%=\n" \
  "v_sub_f32 v44, v44, v10\n" \
  "v_sub_f32 v45, v45, v10\n" \
  "v_sub_f32 v46, v46, v10\n" \
  "v_sub_f32 v47, v47, v10\n" \
  "v_sub_f32 v48, v48, v10\n" \
  "v_sub_f32 v49, v49, v10\n" \
  "v_sub_f32 v50, v50, v10\n" \
  "v_sub_f32 v51, v51, v10\n" \
  "v_exp_f32 v44, v44\n" \
  "v_exp_f32 v45, v45\n" \
  "v_exp_f32 v46, v46\n" \
  "v_exp_f32 v47, v47\n" \
  "v_exp_f32 v48, v48\n" \
  "v_exp_f32 v49, v49\n" \
  "v_exp_f32 v50, v50\n" \
  "v_exp_f32 v51, v51\n" \
  "v_cvt_pk_bf16_f32 v20, v44, v45\n" \
  "v_cvt_pk_bf16_f32 v21, v46, v47\n" \
  "v_cvt_pk_bf16_f32 v22, v48, v49\n" \
  "v_cvt_pk_bf16_f32 v23, v50, v51\n" \
  "s_branch SLY_%=\n" \
  "SLX_%=:\n" \
  "v_max3_f32 v10, v44, v45, v46\n" \
  "v_max3_f32 v10, v10, v47, v48\n" \
  "v_max3_f32 v10, v10, v49, v50\n" \
  "v_add_f32 v24, v24, v26\n" \
  "v_mul_f32 v24, s60, v24\n" \
  "v_add_f32 v25, v25, v27\n" \
  "v_mul_f32 v25, s60, v25\n" \
  "v_max3_f32 v10, v10, v51, v24\n" \
  "v_max_f32 v10, v10, v25\n" \
  "v_mov_b32 v11, v10\n" \
  "s_nop 1\n" \
  "v_permlane16_swap_b32 v10, v11\n" \
  "v_max_f32 v10, v10, v11\n" \
  "v_mov_b32 v11, v10\n" \
  "s_nop 1\n" \
  "v_permlane32_swap_b32 v10, v11\n" \
  "v_max_f32 v10, v10, v11\n" \
  "v_sub_f32 v44, v44, v10\n" \
  "v_sub_f32 v45, v45, v10\n" \
  "v_sub_f32 v46, v46, v10\n" \
  "v_sub_f32 v47, v47, v10\n" \
  "v_sub_f32 v48, v48, v10\n" \
  "v_sub_f32 v49, v49, v10\n" \
  "v_sub_f32 v50, v50, v10\n" \
  "v_sub_f32 v51, v51, v10\n" \
  "v_sub_f32 v24, v24, v10\n" \
  "v_sub_f32 v25, v25, v10\n" \
  "v_exp_f32 v44, v44\n" \
  "v_exp_f32 v45, v45\n" \
  "v_exp_f32 v46, v46\n" \
  "v_exp_f32 v47, v47\n" \
  "v_cvt_pk_bf16_f32 v20, v44, v45\n" \
  "v_exp_f32 v48, v48\n" \
  "v_exp_f32 v49, v49\n" \
  "v_cvt_pk_bf16_f32 v21, v46, v47\n" \
  "v_exp_f32 v50, v50\n" \
  "v_exp_f32 v51, v51\n" \
  "v_cvt_pk_bf16_f32 v22, v48, v49\n" \
  "v_exp_f32 v24, v24\n" \
  "v_exp_f32 v25, v25\n" \
  "v_cvt_pk_bf16_f32 v23, v50, v51\n" \
  "v_cvt_pk_bf16_f32 v88, v24, v25\n" \
  "SLY_%=:\n" \
  "s_nop 1\n" \
  "v_mfma_f32_16x16x32_bf16 v[28:31], v[96:99], v[20:23], 0\n" \
  "v_mfma_f32_16x16x32_bf16 v[32:35], v[100:103], v[20:23], 0\n" \
  "v_mfma_f32_16x16x32_bf16 v[36:39], v[104:107], v[20:23], 0\n" \
  "v_mfma_f32_16x16x32_bf16 v[40:43], v[108:111], v[20:23], 0\n" \
  "v_mfma_f32_16x16x32_bf16 v[68:71], v[20:23], v[92:95], 0\n" \
  "v_mfma_f32_16x16x32_bf16 v[28:31], v[52:55], v[88:91], v[28:31]\n" \
  "v_mfma_f32_16x16x32_bf16 v[32:35], v[54:57], v[88:91], v[32:35]\n" \
  "v_mfma_f32_16x16x32_bf16 v[36:39], v[56:59], v[88:91], v[36:39]\n" \
  "v_mfma_f32_16x16x32_bf16 v[40:43], v[58:61], v[88:91], v[40:43]\n" \
  "v_mfma_f32_16x16x32_bf16 v[68:71], v[88:91], v[92:95], v[68:71]\n" \
  "s_nop 3\n" \
  "v_cvt_pk_bf16_f32 v44, v28, v29\n" \
  "v_cvt_pk_bf16_f32 v45, v30, v31\n" \
  "v_cvt_pk_bf16_f32 v46, v32, v33\n" \
  "v_cvt_pk_bf16_f32 v47, v34, v35\n" \
  "s_nop 1\n" \
  "s_branch WOJ_%=\n" \
  "TAIL_%=:\n" \
  "s_waitcnt lgkmcnt(0)\n" \
  "v_smfmac_f32_16x16x64_bf16 v[68:71], v[28:31], v[208:215], v6\n" \
  "v_smfmac_f32_16x16x64_bf16 v[68:71], v[32:35], v[216:223], v6\n" \
  "v_smfmac_f32_16x16x64_bf16 v[68:71], v[36:39], v[224:231], v6\n" \
  "v_smfmac_f32_16x16x64_bf16 v[68:71], v[40:43], v[232:239], v6\n" \
  "v_smfmac_f32_16x16x64_bf16 v[68:71], v[44:47], v[240:247], v6\n" \
  "v_smfmac_f32_16x16x64_bf16 v[68:71], v[48:51], v[248:255], v6\n" \
  "v_lshrrev_b32 v44, 1, v1\n" \
  "s_nop 6\n" \
  "v_add_f32 v68, v68, v70\n" \
  "v_add_f32 v69, v69, v71\n" \
  "v_cvt_pk_bf16_f32 v68, v68, v69\n" \
  "ds_write_b32 v44, v68 offset:21504\n" \
  "s_waitcnt vmcnt(0)\n" \
  "s_waitcnt lgkmcnt(0)\n" \
  "s_barrier\n" \
  "s_cmp_lg_u32 s25, 0\n" \
  "s_cbranch_scc1 T62_%=\n" \
  "v_mov_b32 v92, 63\n" \
  "buffer_store_dword v92, off, s[36:39], s24 sc1\n" \
  "T62_%=:\n" \
  "s_cmp_lt_u32 s25, 2\n" \
  "s_cbranch_scc1 TJOIN_%=\n" \
  "ds_read_b128 v[28:31], v5 offset:12288\n" \
  "ds_read_b128 v[36:39], v5 offset:0\n" \
  "ds_read_b128 v[40:43], v5 offset:4096\n" \
  "ds_read_b128 v[32:35], v5 offset:13312\n" \
  "ds_read_b128 v[44:47], v5 offset:1024\n" \
  "ds_read_b128 v[48:51], v5 offset:5120\n" \
  "ds_read_b64_tr_b16 v[60:61], v4 offset:12544\n" \
  "ds_read_b64_tr_b16 v[62:63], v4 offset:12672\n" \
  "ds_read_b64_tr_b16 v[64:65], v4 offset:13056\n" \
  "ds_read_b64_tr_b16 v[66:67], v4 offset:13184\n" \
  "ds_read_b64_tr_b16 v[12:13], v4 offset:13568\n" \
  "ds_read_b64_tr_b16 v[14:15], v4 offset:13696\n" \
  "ds_read_b64_tr_b16 v[16:17], v4 offset:14080\n" \
  "ds_read_b64_tr_b16 v[18:19], v4 offset:14208\n" \
  "v_add_u32 v20, s12, v5\n" \
  "v_lshrrev_b32 v21, 1, v2\n" \
  "v_mov_b32 v22, 0xf149f2ca\n" \
  "s_waitcnt lgkmcnt(12)\n" \
  "v_mfma_f32_16x16x32_bf16 v[52:55], v[36:39], v[28:31], 0\n" \
  "s_waitcnt lgkmcnt(11)\n" \
  "v_mfma_f32_16x16x32_bf16 v[56:59], v[40:43], v[28:31], 0\n" \
  "s_waitcnt lgkmcnt(6)\n" \
  "v_mfma_f32_16x16x32_bf16 v[68:71], v[60:63], v[28:31], 0\n" \
  "ds_read_b128 v[28:31], v5 offset:14336\n" \
  "ds_read_b128 v[36:39], v5 offset:2048\n" \
  "ds_read_b128 v[40:43], v5 offset:6144\n" \
  "v_mfma_f32_16x16x32_bf16 v[52:55], v[44:47], v[32:35], v[52:55]\n" \
  "v_mfma_f32_16x16x32_bf16 v[56:59], v[48:51], v[32:35], v[56:59]\n" \
  "s_waitcnt lgkmcnt(7)\n" \
  "v_mfma_f32_16x16x32_bf16 v[68:71], v[64:67], v[32:35], v[68:71]\n" \
  "ds_read_b128 v[32:35], v5 offset:15360\n" \
  "ds_read_b128 v[44:47], v5 offset:3072\n" \
  "ds_read_b128 v[48:51], v5 offset:7168\n" \
  "s_waitcnt lgkmcnt(4)\n" \
  "v_mfma_f32_16x16x32_bf16 v[52:55], v[36:39], v[28:31], v[52:55]\n" \
  "s_waitcnt lgkmcnt(3)\n" \
  "v_mfma_f32_16x16x32_bf16 v[56:59], v[40:43], v[28:31], v[56:59]\n" \
  "v_mfma_f32_16x16x32_bf16 v[68:71], v[12:15], v[28:31], v[68:71]\n" \
  "s_waitcnt lgkmcnt(1)\n" \
  "v_mfma_f32_16x16x32_bf16 v[52:55], v[44:47], v[32:35], v[52:55]\n" \
  "s_waitcnt lgkmcnt(0)\n" \
  "v_mfma_f32_16x16x32_bf16 v[56:59], v[48:51], v[32:35], v[56:59]\n" \
  "v_mfma_f32_16x16x32_bf16 v[68:71], v[16:19], v[32:35], v[68:71]\n" \
  "ds_read_b128 v[36:39], v20 offset:8192\n" \
  "ds_read_b128 v[40:43], v20 offset:9216\n" \
  "ds_read_b64 v[12:13], v21 offset:21504\n" \
  "ds_read_b64 v[14:15], v21 offset:22016\n" \
  "ds_read_b128 v[28:31], v0 offset:25600\n" \
  "ds_read_b128 v[32:35], v0 offset:26624\n" \
  "v_cndmask_b32 v52, v52, v22, s[64:65]\n" \
  "v_cndmask_b32 v53, v53, v22, s[66:67]\n" \
  "v_cndmask_b32 v54, v54, v22, s[68:69]\n" \
  "v_cndmask_b32 v55, v55, v22, s[70:71]\n" \
  "v_cndmask_b32 v56, v56, v22, s[72:73]\n" \
  "v_cndmask_b32 v57, v57, v22, s[74:75]\n" \
  "v_cndmask_b32 v58, v58, v22, s[76:77]\n" \
  "v_cndmask_b32 v59, v59, v22, s[78:79]\n" \
  "v_max3_f32 v10, v52, v53, v54\n" \
  "v_max3_f32 v10, v10, v55, v56\n" \
  "v_max3_f32 v10, v10, v57, v58\n" \
  "v_add_f32 v68, v68, v70\n" \
  "v_mul_f32 v68, s60, v68\n" \
  "v_add_f32 v69, v69, v71\n" \
  "v_mul_f32 v69, s60, v69\n" \
  "v_max3_f32 v10, v10, v59, v68\n" \
  "v_max_f32 v10, v10, v69\n" \
  "v_mov_b32 v11, v10\n" \
  "s_nop 1\n" \
  "v_permlane16_swap_b32 v10, v11\n" \
  "v_max_f32 v10, v10, v11\n" \
  "v_mov_b32 v11, v10\n" \
  "s_nop 1\n" \
  "v_permlane32_swap_b32 v10, v11\n" \
  "v_max_f32 v10, v10, v11\n" \
  "v_sub_f32 v52, v52, v10\n" \
  "v_sub_f32 v53, v53, v10\n" \
  "v_sub_f32 v54, v54, v10\n" \
  "v_sub_f32 v55, v55, v10\n" \
  "v_sub_f32 v56, v56, v10\n" \
  "v_sub_f32 v57, v57, v10\n" \
  "v_sub_f32 v58, v58, v10\n" \
  "v_sub_f32 v59, v59, v10\n" \
  "v_sub_f32 v68, v68, v10\n" \
  "v_sub_f32 v69, v69, v10\n" \
  "v_exp_f32 v52, v52\n" \
  "v_exp_f32 v53, v53\n" \
  "v_exp_f32 v54, v54\n" \
  "v_exp_f32 v55, v55\n" \
  "v_cvt_pk_bf16_f32 v44, v52, v53\n" \
  "v_exp_f32 v56, v56\n" \
  "v_exp_f32 v57, v57\n" \
  "v_cvt_pk_bf16_f32 v45, v54, v55\n" \
  "v_exp_f32 v58, v58\n" \
  "v_exp_f32 v59, v59\n" \
  "v_cvt_pk_bf16_f32 v46, v56, v57\n" \
  "v_exp_f32 v68, v68\n" \
  "v_exp_f32 v69, v69\n" \
  "v_cvt_pk_bf16_f32 v47, v58, v59\n" \
  "s_waitcnt lgkmcnt(1)\n" \
  "v_cvt_pk_bf16_f32 v28, v68, v69\n" \
  "s_waitcnt lgkmcnt(0)\n" \
  "v_mfma_f32_16x16x32_bf16 v[48:51], v[32:35], v[44:47], 0\n" \
  "v_mfma_f32_16x16x32_bf16 v[60:63], v[36:39], v[44:47], 0\n" \
  "v_mfma_f32_16x16x32_bf16 v[64:67], v[40:43], v[44:47], 0\n" \
  "v_mfma_f32_16x16x32_bf16 v[48:51], v[32:35], v[28:31], v[48:51]\n" \
  "v_mfma_f32_16x16x32_bf16 v[60:63], v[12:15], v[28:31], v[60:63]\n" \
  "v_mfma_f32_16x16x32_bf16 v[64:67], v[14:17], v[28:31], v[64:67]\n" \
  "s_nop 5\n" \
  "v_rcp_f32 v10, v48\n" \
  "v_rcp_f32 v11, v48\n" \
  "s_nop 0\n" \
  "v_pk_mul_f32 v[60:61], v[60:61], v[10:11]\n" \
  "v_pk_mul_f32 v[62:63], v[62:63], v[10:11]\n" \
  "v_cvt_pk_bf16_f32 v24, v60, v61\n" \
  "v_cvt_pk_bf16_f32 v25, v62, v63\n" \
  "v_pk_mul_f32 v[64:65], v[64:65], v[10:11]\n" \
  "v_pk_mul_f32 v[66:67], v[66:67], v[10:11]\n" \
  "v_cvt_pk_bf16_f32 v26, v64, v65\n" \
  "v_cvt_pk_bf16_f32 v27, v66, v67\n" \
  "buffer_store_dwordx4 v[24:27], v0, s[36:39], s53 offen offset:0 sc1\n" \
  "TJOIN_%=:\n" \
  "s_waitcnt vmcnt(0) lgkmcnt(0)\n" \
  "s_barrier\n" \
  "s_cmp_lg_u32 s25, 0\n" \
  "s_cbranch_scc1 BYE_%=\n" \
  "v_mov_b32 v92, 64\n" \
  "buffer_store_dword v92, off, s[36:39], s24 sc1\n" \
  "s_branch BYE_%=\n" \
  "FAIL_%=:\n" \
  "v_mov_b32 v92, 1\n" \
  "s_mov_b32 s56, 12582916\n" \
  "buffer_store_dword v92, off, s[36:39], s56 sc1\n" \
  "BYE_%=:\n" \
  "v_mov_b32 v40, 0\n" \
  "v_mov_b32 v41, 0\n" \
  "s_lshl_b32 s56, %[b], 6\n" \
  "s_lshl_b32 s57, s25, 3\n" \
  "s_add_u32 s56, s56, s57\n" \
  "s_add_u32 s56, s56, 12615680\n" \
  "buffer_store_dwordx2 v[40:41], off, s[36:39], s56 sc1\n" \
  "END_%=:\n" \
  "s_waitcnt vmcnt(0) lgkmcnt(0)\n"

#define WORK_ASM_TEXT \
  "s_load_dwordx16 s[16:31], %[ka], 0\n" \
  "s_load_dwordx4 s[32:35], %[ka], 64\n" \
  "s_and_b32 s49, s93, 0xff\n" \
  "s_xor_b32 s49, s49, 0\n" \
  "s_lshr_b32 s48, s93, 8\n" \
  "s_and_b32 s50, s48, 1\n" \
  "s_lshr_b32 s51, s48, 1\n" \
  "s_mov_b32 s62, 0\n" \
  "s_mov_b32 s77, 0x7f817f81\n" \
  "v_mbcnt_lo_u32_b32 v0, -1, 0\n" \
  "v_mbcnt_hi_u32_b32 v0, -1, v0\n" \
  "v_lshlrev_b32 v1, 4, v0\n" \
  "v_and_b32 v2, 15, v0\n" \
  "v_lshrrev_b32 v3, 4, v0\n" \
  "v_lshrrev_b32 v4, 3, v0\n" \
  "v_mul_u32_u24 v11, 8192, v4\n" \
  "v_and_b32 v4, 2, v4\n" \
  "v_and_b32 v5, 7, v0\n" \
  "v_xor_b32 v4, v4, v5\n" \
  "v_lshl_add_u32 v11, v4, 4, v11\n" \
  "v_lshrrev_b32 v4, 3, v0\n" \
  "v_mul_u32_u24 v12, 8192, v4\n" \
  "v_and_b32 v4, 2, v4\n" \
  "v_or_b32 v4, 4, v4\n" \
  "v_and_b32 v5, 7, v0\n" \
  "v_xor_b32 v4, v4, v5\n" \
  "v_lshl_add_u32 v12, v4, 4, v12\n" \
  "v_lshrrev_b32 v4, 2, v0\n" \
  "v_lshlrev_b32 v13, 13, v4\n" \
  "v_lshrrev_b32 v4, 4, v0\n" \
  "v_and_b32 v4, 2, v4\n" \
  "v_and_b32 v5, 3, v0\n" \
  "v_xor_b32 v4, v4, v5\n" \
  "v_lshl_add_u32 v13, v4, 4, v13\n" \
  "s_waitcnt lgkmcnt(0)\n" \
  "s_mov_b32 s36, s32\n" \
  "s_and_b32 s37, s33, 0xffff\n" \
  "s_mov_b32 s38, 134217728\n" \
  "s_mov_b32 s39, 0x20000\n" \
  "s_mov_b32 s40, s34\n" \
  "s_and_b32 s41, s35, 0xffff\n" \
  "s_mov_b32 s42, 2621440\n" \
  "s_mov_b32 s43, 0x20000\n" \
  "s_mov_b32 s44, s16\n" \
  "s_and_b32 s45, s17, 0xffff\n" \
  "s_mov_b32 s46, 2621440\n" \
  "s_mov_b32 s47, 0x20000\n" \
  "s_lshr_b32 s53, s49, 2\n" \
  "s_lshl_b32 s53, s53, 8\n" \
  "s_mul_i32 s52, s48, 1310720\n" \
  "s_add_u32 s53, s53, s52\n" \
  "s_and_b32 s52, s49, 1\n" \
  "s_lshl_b32 s52, s52, 7\n" \
  "s_add_u32 s52, s52, s53\n" \
  "s_bitcmp1_b32 s49, 1\n" \
  "s_cselect_b64 s[82:83], s[20:21], s[18:19]\n" \
  "s_cselect_b32 s69, 0x3e0293ee, 1.0\n" \
  "s_mul_i32 s72, s48, 20480\n" \
  "s_mul_i32 s73, s48, 10240\n" \
  "s_add_u32 s57, s72, 14680064\n" \
  "s_mov_b32 s58, s73\n" \
  "s_add_u32 s76, s57, 2539520\n" \
  "s_and_b32 s54, s49, 3\n" \
  "s_lshl_b32 s54, s54, 6\n" \
  "s_add_u32 s54, s54, s53\n" \
  "s_add_u32 s80, s22, s54\n" \
  "s_addc_u32 s81, s23, 0\n" \
  "s_add_u32 m0, s73, 122880\n" \
  "s_nop 0\n" \
  "global_load_lds_dwordx4 v13, s[80:81]\n" \
  "s_add_u32 s80, s80, 131072\n" \
  "s_addc_u32 s81, s81, 0\n" \
  "s_add_u32 m0, s73, 123904\n" \
  "s_nop 0\n" \
  "global_load_lds_dwordx4 v13, s[80:81]\n" \
  "s_add_u32 s80, s80, 131072\n" \
  "s_addc_u32 s81, s81, 0\n" \
  "s_add_u32 m0, s73, 124928\n" \
  "s_nop 0\n" \
  "global_load_lds_dwordx4 v13, s[80:81]\n" \
  "s_add_u32 s80, s80, 131072\n" \
  "s_addc_u32 s81, s81, 0\n" \
  "s_add_u32 m0, s73, 125952\n" \
  "s_nop 0\n" \
  "global_load_lds_dwordx4 v13, s[80:81]\n" \
  "s_add_u32 s80, s80, 131072\n" \
  "s_addc_u32 s81, s81, 0\n" \
  "s_add_u32 m0, s73, 126976\n" \
  "s_nop 0\n" \
  "global_load_lds_dwordx4 v13, s[80:81]\n" \
  "s_add_u32 s80, s80, 131072\n" \
  "s_addc_u32 s81, s81, 0\n" \
  "s_add_u32 m0, s73, 128000\n" \
  "s_nop 0\n" \
  "global_load_lds_dwordx4 v13, s[80:81]\n" \
  "s_add_u32 s80, s80, 131072\n" \
  "s_addc_u32 s81, s81, 0\n" \
  "s_add_u32 m0, s73, 129024\n" \
  "s_nop 0\n" \
  "global_load_lds_dwordx4 v13, s[80:81]\n" \
  "s_add_u32 s80, s80, 131072\n" \
  "s_addc_u32 s81, s81, 0\n" \
  "s_add_u32 m0, s73, 130048\n" \
  "s_nop 0\n" \
  "global_load_lds_dwordx4 v13, s[80:81]\n" \
  "s_add_u32 s80, s80, 131072\n" \
  "s_addc_u32 s81, s81, 0\n" \
  "s_add_u32 m0, s73, 131072\n" \
  "s_nop 0\n" \
  "global_load_lds_dwordx4 v13, s[80:81]\n" \
  "s_add_u32 s80, s80, 131072\n" \
  "s_addc_u32 s81, s81, 0\n" \
  "s_add_u32 m0, s73, 132096\n" \
  "s_nop 0\n" \
  "global_load_lds_dwordx4 v13, s[80:81]\n" \
  "s_getreg_b32 s53, hwreg(20, 0, 3)\n" \
  "s_or_b32 s53, s53, 0x3c96e1a8\n" \
  "v_mov_b32 v40, 0x5a17c0de\n" \
  "v_mov_b32 v41, s53\n" \
  "s_lshl_b32 s53, %[b], 6\n" \
  "s_lshl_b32 s54, s48, 3\n" \
  "s_add_u32 s53, s53, s54\n" \
  "s_add_u32 s53, s53, 12615680\n" \
  "s_waitcnt vmcnt(10)\n" \
  "buffer_store_dwordx2 v[40:41], off, s[36:39], s53 sc1\n" \
  "s_add_u32 s80, s82, s52\n" \
  "s_addc_u32 s81, s83, 0\n" \
  "s_add_u32 m0, s72, 40960\n" \
  "s_nop 0\n" \
  "global_load_lds_dwordx4 v11, s[80:81]\n" \
  "s_add_u32 s80, s80, 65536\n" \
  "s_addc_u32 s81, s81, 0\n" \
  "s_add_u32 m0, s72, 41984\n" \
  "s_nop 0\n" \
  "global_load_lds_dwordx4 v12, s[80:81]\n" \
  "s_add_u32 s80, s80, 65536\n" \
  "s_addc_u32 s81, s81, 0\n" \
  "s_add_u32 m0, s72, 43008\n" \
  "s_nop 0\n" \
  "global_load_lds_dwordx4 v11, s[80:81]\n" \
  "s_add_u32 s80, s80, 65536\n" \
  "s_addc_u32 s81, s81, 0\n" \
  "s_add_u32 m0, s72, 44032\n" \
  "s_nop 0\n" \
  "global_load_lds_dwordx4 v12, s[80:81]\n" \
  "s_add_u32 s80, s80, 65536\n" \
  "s_addc_u32 s81, s81, 0\n" \
  "s_add_u32 m0, s72, 45056\n" \
  "s_nop 0\n" \
  "global_load_lds_dwordx4 v11, s[80:81]\n" \
  "s_add_u32 s80, s80, 65536\n" \
  "s_addc_u32 s81, s81, 0\n" \
  "s_add_u32 m0, s72, 46080\n" \
  "s_nop 0\n" \
  "global_load_lds_dwordx4 v12, s[80:81]\n" \
  "s_add_u32 s80, s80, 65536\n" \
  "s_addc_u32 s81, s81, 0\n" \
  "s_add_u32 m0, s72, 47104\n" \
  "s_nop 0\n" \
  "global_load_lds_dwordx4 v11, s[80:81]\n" \
  "s_add_u32 s80, s80, 65536\n" \
  "s_addc_u32 s81, s81, 0\n" \
  "s_add_u32 m0, s72, 48128\n" \
  "s_nop 0\n" \
  "global_load_lds_dwordx4 v12, s[80:81]\n" \
  "s_add_u32 s80, s80, 65536\n" \
  "s_addc_u32 s81, s81, 0\n" \
  "s_add_u32 m0, s72, 49152\n" \
  "s_nop 0\n" \
  "global_load_lds_dwordx4 v11, s[80:81]\n" \
  "s_add_u32 s80, s80, 65536\n" \
  "s_addc_u32 s81, s81, 0\n" \
  "s_add_u32 m0, s72, 50176\n" \
  "s_nop 0\n" \
  "global_load_lds_dwordx4 v12, s[80:81]\n" \
  "s_add_u32 s80, s80, 65536\n" \
  "s_addc_u32 s81, s81, 0\n" \
  "s_add_u32 m0, s72, 51200\n" \
  "s_nop 0\n" \
  "global_load_lds_dwordx4 v11, s[80:81]\n" \
  "s_add_u32 s80, s80, 65536\n" \
  "s_addc_u32 s81, s81, 0\n" \
  "s_add_u32 m0, s72, 52224\n" \
  "s_nop 0\n" \
  "global_load_lds_dwordx4 v12, s[80:81]\n" \
  "s_add_u32 s80, s80, 65536\n" \
  "s_addc_u32 s81, s81, 0\n" \
  "s_add_u32 m0, s72, 53248\n" \
  "s_nop 0\n" \
  "global_load_lds_dwordx4 v11, s[80:81]\n" \
  "s_add_u32 s80, s80, 65536\n" \
  "s_addc_u32 s81, s81, 0\n" \
  "s_add_u32 m0, s72, 54272\n" \
  "s_nop 0\n" \
  "global_load_lds_dwordx4 v12, s[80:81]\n" \
  "s_add_u32 s80, s80, 65536\n" \
  "s_addc_u32 s81, s81, 0\n" \
  "s_add_u32 m0, s72, 55296\n" \
  "s_nop 0\n" \
  "global_load_lds_dwordx4 v11, s[80:81]\n" \
  "s_add_u32 s80, s80, 65536\n" \
  "s_addc_u32 s81, s81, 0\n" \
  "s_add_u32 m0, s72, 56320\n" \
  "s_nop 0\n" \
  "global_load_lds_dwordx4 v12, s[80:81]\n" \
  "s_add_u32 s80, s80, 65536\n" \
  "s_addc_u32 s81, s81, 0\n" \
  "s_add_u32 m0, s72, 57344\n" \
  "s_nop 0\n" \
  "global_load_lds_dwordx4 v11, s[80:81]\n" \
  "s_add_u32 s80, s80, 65536\n" \
  "s_addc_u32 s81, s81, 0\n" \
  "s_add_u32 m0, s72, 58368\n" \
  "s_nop 0\n" \
  "global_load_lds_dwordx4 v12, s[80:81]\n" \
  "s_add_u32 s80, s80, 65536\n" \
  "s_addc_u32 s81, s81, 0\n" \
  "s_add_u32 m0, s72, 59392\n" \
  "s_nop 0\n" \
  "global_load_lds_dwordx4 v11, s[80:81]\n" \
  "s_add_u32 s80, s80, 65536\n" \
  "s_addc_u32 s81, s81, 0\n" \
  "s_add_u32 m0, s72, 60416\n" \
  "s_nop 0\n" \
  "global_load_lds_dwordx4 v12, s[80:81]\n" \
  "v_mov_b32 v32, s77\n" \
  "v_mov_b32 v33, s77\n" \
  "v_mov_b32 v34, s77\n" \
  "v_mov_b32 v35, s77\n" \
  "s_mul_i32 s71, s49, 49152\n" \
  "s_lshl_b32 s52, s48, 10\n" \
  "s_add_u32 s71, s71, s52\n" \
  "s_add_u32 s71, s71, 18874368\n" \
  "s_xor_b32 s53, s49, 1\n" \
  "s_mul_i32 s70, s53, 49152\n" \
  "s_add_u32 s70, s70, s52\n" \
  "s_add_u32 s70, s70, 18874368\n" \
  "s_lshl_b32 s52, s50, 1\n" \
  "v_bfe_u32 v4, v0, 2, 2\n" \
  "v_lshl_add_u32 v11, v3, 3, v4\n" \
  "v_lshrrev_b32 v4, 1, v4\n" \
  "v_and_b32 v5, 1, v3\n" \
  "v_lshl_add_u32 v4, v5, 1, v4\n" \
  "v_xor_b32 v4, s52, v4\n" \
  "v_lshlrev_b32 v11, 7, v11\n" \
  "v_lshl_add_u32 v11, v4, 5, v11\n" \
  "v_and_b32 v4, 3, v0\n" \
  "v_lshl_add_u32 v11, v4, 3, v11\n" \
  "v_add_u32 v11, 40960, v11\n" \
  "s_add_u32 s52, s52, 1\n" \
  "v_bfe_u32 v4, v0, 2, 2\n" \
  "v_lshl_add_u32 v12, v3, 3, v4\n" \
  "v_lshrrev_b32 v4, 1, v4\n" \
  "v_and_b32 v5, 1, v3\n" \
  "v_lshl_add_u32 v4, v5, 1, v4\n" \
  "v_xor_b32 v4, s52, v4\n" \
  "v_lshlrev_b32 v12, 7, v12\n" \
  "v_lshl_add_u32 v12, v4, 5, v12\n" \
  "v_and_b32 v4, 3, v0\n" \
  "v_lshl_add_u32 v12, v4, 3, v12\n" \
  "v_add_u32 v12, 40960, v12\n" \
  "v_add_u32 v15, 40960, v11\n" \
  "v_add_u32 v14, 40960, v12\n" \
  "v_bfe_u32 v4, v0, 2, 2\n" \
  "v_lshl_add_u32 v13, v3, 3, v4\n" \
  "v_lshlrev_b32 v13, 6, v13\n" \
  "v_and_b32 v4, 1, v3\n" \
  "v_xor_b32 v4, s50, v4\n" \
  "v_lshl_add_u32 v13, v4, 5, v13\n" \
  "v_and_b32 v4, 3, v0\n" \
  "v_lshl_add_u32 v13, v4, 3, v13\n" \
  "v_add_u32 v13, 122880, v13\n" \
  "v_lshlrev_b32 v46, 2, v1\n" \
  "s_lshl_b32 s52, s48, 12\n" \
  "s_add_u32 s52, s52, 12615680\n" \
  "s_mov_b32 s53, 12615680\n" \
  "buffer_load_dwordx4 v[52:55], v46, s[36:39], s52 offen sc1\n" \
  "buffer_load_dwordx4 v[56:59], v46, s[36:39], s52 offen offset:16 sc1\n" \
  "buffer_load_dwordx2 v[44:45], off, s[36:39], s53 sc1\n" \
  "s_waitcnt vmcnt(3)\n" \
  "s_barrier\n" \
  "ds_read_b64_tr_b16 a[0:1], v11 offset:0\n" \
  "ds_read_b64_tr_b16 a[2:3], v11 offset:512\n" \
  "ds_read_b64_tr_b16 a[80:81], v12 offset:0\n" \
  "ds_read_b64_tr_b16 a[82:83], v12 offset:512\n" \
  "ds_read_b64_tr_b16 a[4:5], v11 offset:4096\n" \
  "ds_read_b64_tr_b16 a[6:7], v11 offset:4608\n" \
  "ds_read_b64_tr_b16 a[84:85], v12 offset:4096\n" \
  "ds_read_b64_tr_b16 a[86:87], v12 offset:4608\n" \
  "ds_read_b64_tr_b16 a[8:9], v11 offset:8192\n" \
  "ds_read_b64_tr_b16 a[10:11], v11 offset:8704\n" \
  "ds_read_b64_tr_b16 a[88:89], v12 offset:8192\n" \
  "ds_read_b64_tr_b16 a[90:91], v12 offset:8704\n" \
  "ds_read_b64_tr_b16 a[12:13], v11 offset:12288\n" \
  "ds_read_b64_tr_b16 a[14:15], v11 offset:12800\n" \
  "ds_read_b64_tr_b16 a[92:93], v12 offset:12288\n" \
  "ds_read_b64_tr_b16 a[94:95], v12 offset:12800\n" \
  "ds_read_b64_tr_b16 a[16:17], v11 offset:16384\n" \
  "ds_read_b64_tr_b16 a[18:19], v11 offset:16896\n" \
  "ds_read_b64_tr_b16 a[96:97], v12 offset:16384\n" \
  "ds_read_b64_tr_b16 a[98:99], v12 offset:16896\n" \
  "ds_read_b64_tr_b16 a[20:21], v11 offset:20480\n" \
  "ds_read_b64_tr_b16 a[22:23], v11 offset:20992\n" \
  "ds_read_b64_tr_b16 a[100:101], v12 offset:20480\n" \
  "ds_read_b64_tr_b16 a[102:103], v12 offset:20992\n" \
  "ds_read_b64_tr_b16 a[24:25], v11 offset:24576\n" \
  "ds_read_b64_tr_b16 a[26:27], v11 offset:25088\n" \
  "ds_read_b64_tr_b16 a[104:105], v12 offset:24576\n" \
  "ds_read_b64_tr_b16 a[106:107], v12 offset:25088\n" \
  "ds_read_b64_tr_b16 a[28:29], v11 offset:28672\n" \
  "ds_read_b64_tr_b16 a[30:31], v11 offset:29184\n" \
  "ds_read_b64_tr_b16 a[108:109], v12 offset:28672\n" \
  "ds_read_b64_tr_b16 a[110:111], v12 offset:29184\n" \
  "ds_read_b64_tr_b16 a[32:33], v11 offset:32768\n" \
  "ds_read_b64_tr_b16 a[34:35], v11 offset:33280\n" \
  "ds_read_b64_tr_b16 a[112:113], v12 offset:32768\n" \
  "ds_read_b64_tr_b16 a[114:115], v12 offset:33280\n" \
  "ds_read_b64_tr_b16 a[36:37], v11 offset:36864\n" \
  "ds_read_b64_tr_b16 a[38:39], v11 offset:37376\n" \
  "ds_read_b64_tr_b16 a[116:117], v12 offset:36864\n" \
  "ds_read_b64_tr_b16 a[118:119], v12 offset:37376\n" \
  "WARA_%=:\n" \
  "s_waitcnt vmcnt(0)\n" \
  "v_xor_b32 v42, 0x5a17c0de, v44\n" \
  "v_xor_b32 v52, 0x5a17c0de, v52\n" \
  "v_xor_b32 v53, 0x3c96e1a8, v53\n" \
  "v_and_b32 v53, -8, v53\n" \
  "v_or3_b32 v42, v42, v52, v53\n" \
  "v_xor_b32 v54, 0x5a17c0de, v54\n" \
  "v_xor_b32 v55, 0x3c96e1a8, v55\n" \
  "v_and_b32 v55, -8, v55\n" \
  "v_or3_b32 v42, v42, v54, v55\n" \
  "v_xor_b32 v56, 0x5a17c0de, v56\n" \
  "v_xor_b32 v57, 0x3c96e1a8, v57\n" \
  "v_and_b32 v57, -8, v57\n" \
  "v_or3_b32 v42, v42, v56, v57\n" \
  "v_xor_b32 v58, 0x5a17c0de, v58\n" \
  "v_xor_b32 v59, 0x3c96e1a8, v59\n" \
  "v_and_b32 v59, -8, v59\n" \
  "v_or3_b32 v42, v42, v58, v59\n" \
  "v_cmp_ne_u32 vcc, 0, v42\n" \
  "v_readfirstlane_b32 s94, v45\n" \
  "s_nop 3\n" \
  "s_cbranch_vccz WARB_%=\n" \
  "s_sleep 8\n" \
  "s_add_u32 s62, s62, 1\n" \
  "s_cmp_gt_u32 s62, 131072\n" \
  "s_cbranch_scc1 WFAIL_%=\n" \
  "v_lshlrev_b32 v46, 2, v1\n" \
  "s_lshl_b32 s52, s48, 12\n" \
  "s_add_u32 s52, s52, 12615680\n" \
  "s_mov_b32 s53, 12615680\n" \
  "buffer_load_dwordx4 v[52:55], v46, s[36:39], s52 offen sc1\n" \
  "buffer_load_dwordx4 v[56:59], v46, s[36:39], s52 offen offset:16 sc1\n" \
  "buffer_load_dwordx2 v[44:45], off, s[36:39], s53 sc1\n" \
  "s_branch WARA_%=\n" \
  "WARB_%=:\n" \
  "s_add_u32 s94, s94, %[b]\n" \
  "s_add_u32 s94, s94, 0\n" \
  "s_and_b32 s94, s94, 7\n" \
  "s_cmp_lg_u32 s94, s92\n" \
  "s_cbranch_scc1 REDO_%=\n" \
  "s_waitcnt lgkmcnt(0)\n" \
  "s_barrier\n" \
  "s_add_u32 m0, s58, 0\n" \
  "s_add_u32 s54, s57, 0\n" \
  "buffer_load_dwordx4 v1, s[36:39], s54 offen offset:0 sc1 lds\n" \
  "buffer_load_dwordx4 v1, s[36:39], s54 offen offset:1024 sc1 lds\n" \
  "buffer_load_dwordx4 v1, s[36:39], s54 offen offset:2048 sc1 lds\n" \
  "buffer_load_dwordx4 v1, s[36:39], s54 offen offset:3072 sc1 lds\n" \
  "s_add_u32 m0, s58, 4096\n" \
  "s_add_u32 s54, s57, 4096\n" \
  "buffer_load_dwordx4 v1, s[36:39], s54 offen offset:0 sc1 lds\n" \
  "buffer_load_dwordx4 v1, s[36:39], s54 offen offset:1024 sc1 lds\n" \
  "buffer_load_dwordx4 v1, s[36:39], s54 offen offset:2048 sc1 lds\n" \
  "buffer_load_dwordx4 v1, s[36:39], s54 offen offset:3072 sc1 lds\n" \
  "s_add_u32 m0, s58, 8192\n" \
  "s_add_u32 s54, s57, 8192\n" \
  "buffer_load_dwordx4 v1, s[36:39], s54 offen offset:0 sc1 lds\n" \
  "buffer_load_dwordx4 v1, s[36:39], s54 offen offset:1024 sc1 lds\n" \
  "s_add_u32 s57, s57, 10240\n" \
  "s_add_u32 s58, s58, 40960\n" \
  "s_sub_u32 s54, s58, 122880\n" \
  "s_cmp_ge_u32 s58, 122880\n" \
  "s_cselect_b32 s58, s54, s58\n" \
  "s_add_u32 m0, s58, 0\n" \
  "s_add_u32 s54, s57, 0\n" \
  "buffer_load_dwordx4 v1, s[36:39], s54 offen offset:0 sc1 lds\n" \
  "buffer_load_dwordx4 v1, s[36:39], s54 offen offset:1024 sc1 lds\n" \
  "buffer_load_dwordx4 v1, s[36:39], s54 offen offset:2048 sc1 lds\n" \
  "buffer_load_dwordx4 v1, s[36:39], s54 offen offset:3072 sc1 lds\n" \
  "s_add_u32 m0, s58, 4096\n" \
  "s_add_u32 s54, s57, 4096\n" \
  "buffer_load_dwordx4 v1, s[36:39], s54 offen offset:0 sc1 lds\n" \
  "buffer_load_dwordx4 v1, s[36:39], s54 offen offset:1024 sc1 lds\n" \
  "buffer_load_dwordx4 v1, s[36:39], s54 offen offset:2048 sc1 lds\n" \
  "buffer_load_dwordx4 v1, s[36:39], s54 offen offset:3072 sc1 lds\n" \
  "s_add_u32 m0, s58, 8192\n" \
  "s_add_u32 s54, s57, 8192\n" \
  "buffer_load_dwordx4 v1, s[36:39], s54 offen offset:0 sc1 lds\n" \
  "buffer_load_dwordx4 v1, s[36:39], s54 offen offset:1024 sc1 lds\n" \
  "s_add_u32 s57, s57, 71680\n" \
  "s_min_u32 s57, s57, s76\n" \
  "s_add_u32 s58, s58, 40960\n" \
  "s_sub_u32 s54, s58, 122880\n" \
  "s_cmp_ge_u32 s58, 122880\n" \
  "s_cselect_b32 s58, s54, s58\n" \
  "ds_read_b64_tr_b16 a[40:41], v15 offset:0\n" \
  "ds_read_b64_tr_b16 a[42:43], v15 offset:512\n" \
  "ds_read_b64_tr_b16 a[120:121], v14 offset:0\n" \
  "ds_read_b64_tr_b16 a[122:123], v14 offset:512\n" \
  "ds_read_b64_tr_b16 a[44:45], v15 offset:4096\n" \
  "ds_read_b64_tr_b16 a[46:47], v15 offset:4608\n" \
  "ds_read_b64_tr_b16 a[124:125], v14 offset:4096\n" \
  "ds_read_b64_tr_b16 a[126:127], v14 offset:4608\n" \
  "ds_read_b64_tr_b16 a[48:49], v15 offset:8192\n" \
  "ds_read_b64_tr_b16 a[50:51], v15 offset:8704\n" \
  "ds_read_b64_tr_b16 a[128:129], v14 offset:8192\n" \
  "ds_read_b64_tr_b16 a[130:131], v14 offset:8704\n" \
  "ds_read_b64_tr_b16 a[52:53], v15 offset:12288\n" \
  "ds_read_b64_tr_b16 a[54:55], v15 offset:12800\n" \
  "ds_read_b64_tr_b16 a[132:133], v14 offset:12288\n" \
  "ds_read_b64_tr_b16 a[134:135], v14 offset:12800\n" \
  "ds_read_b64_tr_b16 a[56:57], v15 offset:16384\n" \
  "ds_read_b64_tr_b16 a[58:59], v15 offset:16896\n" \
  "ds_read_b64_tr_b16 a[136:137], v14 offset:16384\n" \
  "ds_read_b64_tr_b16 a[138:139], v14 offset:16896\n" \
  "ds_read_b64_tr_b16 a[60:61], v15 offset:20480\n" \
  "ds_read_b64_tr_b16 a[62:63], v15 offset:20992\n" \
  "ds_read_b64_tr_b16 a[140:141], v14 offset:20480\n" \
  "ds_read_b64_tr_b16 a[142:143], v14 offset:20992\n" \
  "ds_read_b64_tr_b16 a[64:65], v15 offset:24576\n" \
  "ds_read_b64_tr_b16 a[66:67], v15 offset:25088\n" \
  "ds_read_b64_tr_b16 a[144:145], v14 offset:24576\n" \
  "ds_read_b64_tr_b16 a[146:147], v14 offset:25088\n" \
  "ds_read_b64_tr_b16 a[68:69], v15 offset:28672\n" \
  "ds_read_b64_tr_b16 a[70:71], v15 offset:29184\n" \
  "ds_read_b64_tr_b16 a[148:149], v14 offset:28672\n" \
  "ds_read_b64_tr_b16 a[150:151], v14 offset:29184\n" \
  "ds_read_b64_tr_b16 a[72:73], v15 offset:32768\n" \
  "ds_read_b64_tr_b16 a[74:75], v15 offset:33280\n" \
  "ds_read_b64_tr_b16 a[152:153], v14 offset:32768\n" \
  "ds_read_b64_tr_b16 a[154:155], v14 offset:33280\n" \
  "ds_read_b64_tr_b16 a[76:77], v15 offset:36864\n" \
  "ds_read_b64_tr_b16 a[78:79], v15 offset:37376\n" \
  "ds_read_b64_tr_b16 a[156:157], v14 offset:36864\n" \
  "ds_read_b64_tr_b16 a[158:159], v14 offset:37376\n" \
  "ds_read_b64_tr_b16 a[160:161], v13 offset:0\n" \
  "ds_read_b64_tr_b16 a[162:163], v13 offset:256\n" \
  "ds_read_b64_tr_b16 a[164:165], v13 offset:2048\n" \
  "ds_read_b64_tr_b16 a[166:167], v13 offset:2304\n" \
  "ds_read_b64_tr_b16 a[168:169], v13 offset:4096\n" \
  "ds_read_b64_tr_b16 a[170:171], v13 offset:4352\n" \
  "ds_read_b64_tr_b16 a[172:173], v13 offset:6144\n" \
  "ds_read_b64_tr_b16 a[174:175], v13 offset:6400\n" \
  "ds_read_b64_tr_b16 a[176:177], v13 offset:8192\n" \
  "ds_read_b64_tr_b16 a[178:179], v13 offset:8448\n" \
  "ds_read_b64_tr_b16 a[180:181], v13 offset:10240\n" \
  "ds_read_b64_tr_b16 a[182:183], v13 offset:10496\n" \
  "ds_read_b64_tr_b16 a[184:185], v13 offset:12288\n" \
  "ds_read_b64_tr_b16 a[186:187], v13 offset:12544\n" \
  "ds_read_b64_tr_b16 a[188:189], v13 offset:14336\n" \
  "ds_read_b64_tr_b16 a[190:191], v13 offset:14592\n" \
  "ds_read_b64_tr_b16 a[192:193], v13 offset:16384\n" \
  "ds_read_b64_tr_b16 a[194:195], v13 offset:16640\n" \
  "ds_read_b64_tr_b16 a[196:197], v13 offset:18432\n" \
  "ds_read_b64_tr_b16 a[198:199], v13 offset:18688\n" \
  "ds_read_b64_tr_b16 a[200:201], v13 offset:20480\n" \
  "ds_read_b64_tr_b16 a[202:203], v13 offset:20736\n" \
  "ds_read_b64_tr_b16 a[204:205], v13 offset:22528\n" \
  "ds_read_b64_tr_b16 a[206:207], v13 offset:22784\n" \
  "ds_read_b64_tr_b16 a[208:209], v13 offset:24576\n" \
  "ds_read_b64_tr_b16 a[210:211], v13 offset:24832\n" \
  "ds_read_b64_tr_b16 a[212:213], v13 offset:26624\n" \
  "ds_read_b64_tr_b16 a[214:215], v13 offset:26880\n" \
  "ds_read_b64_tr_b16 a[216:217], v13 offset:28672\n" \
  "ds_read_b64_tr_b16 a[218:219], v13 offset:28928\n" \
  "ds_read_b64_tr_b16 a[220:221], v13 offset:30720\n" \
  "ds_read_b64_tr_b16 a[222:223], v13 offset:30976\n" \
  "ds_read_b64_tr_b16 a[224:225], v13 offset:32768\n" \
  "ds_read_b64_tr_b16 a[226:227], v13 offset:33024\n" \
  "ds_read_b64_tr_b16 a[228:229], v13 offset:34816\n" \
  "ds_read_b64_tr_b16 a[230:231], v13 offset:35072\n" \
  "ds_read_b64_tr_b16 a[232:233], v13 offset:36864\n" \
  "ds_read_b64_tr_b16 a[234:235], v13 offset:37120\n" \
  "ds_read_b64_tr_b16 a[236:237], v13 offset:38912\n" \
  "ds_read_b64_tr_b16 a[238:239], v13 offset:39168\n" \
  "s_waitcnt vmcnt(10)\n" \
  "s_waitcnt lgkmcnt(0)\n" \
  "s_barrier\n" \
  "s_lshr_b32 s52, s49, 2\n" \
  "s_lshl_b32 s61, s52, 6\n" \
  "s_and_b32 s53, s49, 3\n" \
  "s_lshl_b32 s54, s53, 2\n" \
  "s_add_u32 s61, s61, s54\n" \
  "s_add_u32 s61, s61, 12587008\n" \
  "s_mul_i32 s60, s52, 2113536\n" \
  "s_mul_i32 s54, s51, 24576\n" \
  "s_add_u32 s60, s60, s54\n" \
  "s_add_u32 s60, s60, 50282496\n" \
  "s_lshl_b32 s54, s53, 1\n" \
  "s_add_u32 s54, s54, s50\n" \
  "s_lshl_b32 s73, s54, 10\n" \
  "s_add_u32 s73, s73, 16384\n" \
  "s_and_b32 s72, s54, 3\n" \
  "s_lshl_b32 s72, s72, 10\n" \
  "s_lshr_b32 s54, s53, 1\n" \
  "s_lshl_b32 s54, s54, 13\n" \
  "s_add_u32 s72, s72, s54\n" \
  "s_mov_b32 s56, 32\n" \
  "v_min_u32 v4, 8, v0\n" \
  "v_mul_u32_u24 v5, 43, v4\n" \
  "v_lshrrev_b32 v5, 7, v5\n" \
  "v_mul_u32_u24 v6, 3, v5\n" \
  "v_sub_u32 v102, v4, v6\n" \
  "s_lshl_b32 s52, s49, 2\n" \
  "s_add_u32 s52, s52, s48\n" \
  "s_mul_i32 s52, s52, 3\n" \
  "s_sub_u32 s52, s52, 3072\n" \
  "v_add_u32 v102, s52, v102\n" \
  "v_mov_b32 v100, s26\n" \
  "v_mov_b32 v6, s28\n" \
  "v_cmp_eq_u32 vcc, 1, v5\n" \
  "s_nop 1\n" \
  "v_cndmask_b32 v100, v100, v6, vcc\n" \
  "v_mov_b32 v6, s30\n" \
  "v_cmp_eq_u32 vcc, 2, v5\n" \
  "s_nop 1\n" \
  "v_cndmask_b32 v100, v100, v6, vcc\n" \
  "v_mov_b32 v101, s27\n" \
  "v_mov_b32 v6, s29\n" \
  "v_cmp_eq_u32 vcc, 1, v5\n" \
  "s_nop 1\n" \
  "v_cndmask_b32 v101, v101, v6, vcc\n" \
  "v_mov_b32 v6, s31\n" \
  "v_cmp_eq_u32 vcc, 2, v5\n" \
  "s_nop 1\n" \
  "v_cndmask_b32 v101, v101, v6, vcc\n" \
  "s_mul_i32 s52, s51, 20480\n" \
  "v_add_u32 v8, s52, v1\n" \
  "v_mov_b32 v9, v8\n" \
  "s_mov_b32 s59, 40960\n" \
  "s_mov_b32 s55, 0\n" \
  "ds_read_b128 v[16:19], v9 offset:0\n" \
  "ds_read_b128 v[20:23], v9 offset:10240\n" \
  "ds_read_b128 v[24:27], v9 offset:1024\n" \
  "v_mov_b32 v7, 0xf0000000\n" \
  "WALP_%=:\n" \
  "v_add_u32 v10, s59, v8\n" \
  "s_add_u32 s59, s59, 40960\n" \
  "s_cmp_ge_u32 s59, 122880\n" \
  "s_cselect_b32 s59, 0, s59\n" \
  "s_waitcnt lgkmcnt(2)\n" \
  "v_mfma_f32_16x16x32_bf16 v[40:43], a[0:3], v[16:19], 0\n" \
  "ds_read_b128 v[28:31], v9 offset:11264\n" \
  "v_mfma_f32_16x16x32_bf16 v[44:47], a[80:83], v[16:19], 0\n" \
  "v_mfma_f32_16x16x32_bf16 v[48:51], v[16:19], a[160:163], 0\n" \
  "s_waitcnt lgkmcnt(2)\n" \
  "v_mfma_f32_16x16x32_bf16 v[52:55], a[0:3], v[20:23], 0\n" \
  "ds_read_b128 v[16:19], v9 offset:2048\n" \
  "v_mfma_f32_16x16x32_bf16 v[56:59], a[80:83], v[20:23], 0\n" \
  "v_mfma_f32_16x16x32_bf16 v[60:63], v[20:23], a[160:163], 0\n" \
  "s_waitcnt lgkmcnt(2)\n" \
  "v_mfma_f32_16x16x32_bf16 v[40:43], a[4:7], v[24:27], v[40:43]\n" \
  "ds_read_b128 v[20:23], v9 offset:12288\n" \
  "v_mul_f32 v64, s69, v64\n" \
  "v_mul_f32 v65, s69, v65\n" \
  "v_mfma_f32_16x16x32_bf16 v[44:47], a[84:87], v[24:27], v[44:47]\n" \
  "v_mul_f32 v66, s69, v66\n" \
  "v_mul_f32 v67, s69, v67\n" \
  "v_mfma_f32_16x16x32_bf16 v[48:51], v[24:27], a[164:167], v[48:51]\n" \
  "v_cvt_pk_bf16_f32 v88, v64, v65\n" \
  "v_cvt_pk_bf16_f32 v89, v66, v67\n" \
  "s_waitcnt lgkmcnt(2)\n" \
  "v_mfma_f32_16x16x32_bf16 v[52:55], a[4:7], v[28:31], v[52:55]\n" \
  "ds_read_b128 v[24:27], v9 offset:3072\n" \
  "v_mul_f32 v68, s69, v68\n" \
  "v_mul_f32 v69, s69, v69\n" \
  "v_mfma_f32_16x16x32_bf16 v[56:59], a[84:87], v[28:31], v[56:59]\n" \
  "v_mul_f32 v70, s69, v70\n" \
  "v_mul_f32 v71, s69, v71\n" \
  "v_mfma_f32_16x16x32_bf16 v[60:63], v[28:31], a[164:167], v[60:63]\n" \
  "v_cvt_pk_bf16_f32 v90, v68, v69\n" \
  "v_cvt_pk_bf16_f32 v91, v70, v71\n" \
  "s_waitcnt lgkmcnt(2)\n" \
  "v_mfma_f32_16x16x32_bf16 v[40:43], a[8:11], v[16:19], v[40:43]\n" \
  "ds_read_b128 v[28:31], v9 offset:13312\n" \
  "s_add_u32 s52, s60, s72\n" \
  "s_add_u32 s52, s52, 0\n" \
  "buffer_store_dwordx4 v[88:91], v7, s[36:39], s52 offen offset:0 sc1\n" \
  "v_cvt_pk_bf16_f32 v96, v72, v73\n" \
  "v_mfma_f32_16x16x32_bf16 v[44:47], a[88:91], v[16:19], v[44:47]\n" \
  "v_cvt_pk_bf16_f32 v97, v74, v75\n" \
  "v_mul_f32 v76, s69, v76\n" \
  "v_mfma_f32_16x16x32_bf16 v[48:51], v[16:19], a[168:171], v[48:51]\n" \
  "v_mul_f32 v77, s69, v77\n" \
  "v_mul_f32 v78, s69, v78\n" \
  "s_waitcnt lgkmcnt(2)\n" \
  "v_mfma_f32_16x16x32_bf16 v[52:55], a[8:11], v[20:23], v[52:55]\n" \
  "ds_read_b128 v[16:19], v9 offset:4096\n" \
  "v_mul_f32 v79, s69, v79\n" \
  "v_cvt_pk_bf16_f32 v92, v76, v77\n" \
  "v_mfma_f32_16x16x32_bf16 v[56:59], a[88:91], v[20:23], v[56:59]\n" \
  "v_cvt_pk_bf16_f32 v93, v78, v79\n" \
  "v_mul_f32 v80, s69, v80\n" \
  "v_mfma_f32_16x16x32_bf16 v[60:63], v[20:23], a[168:171], v[60:63]\n" \
  "v_mul_f32 v81, s69, v81\n" \
  "v_mul_f32 v82, s69, v82\n" \
  "s_waitcnt lgkmcnt(2)\n" \
  "v_mfma_f32_16x16x32_bf16 v[40:43], a[12:15], v[24:27], v[40:43]\n" \
  "ds_read_b128 v[20:23], v9 offset:14336\n" \
  "v_mul_f32 v83, s69, v83\n" \
  "v_cvt_pk_bf16_f32 v94, v80, v81\n" \
  "v_mfma_f32_16x16x32_bf16 v[44:47], a[92:95], v[24:27], v[44:47]\n" \
  "v_cvt_pk_bf16_f32 v95, v82, v83\n" \
  "s_add_u32 s52, s60, s72\n" \
  "s_add_u32 s52, s52, 4096\n" \
  "buffer_store_dwordx4 v[92:95], v7, s[36:39], s52 offen offset:0 sc1\n" \
  "v_mfma_f32_16x16x32_bf16 v[48:51], v[24:27], a[172:175], v[48:51]\n" \
  "v_cvt_pk_bf16_f32 v98, v84, v85\n" \
  "v_cvt_pk_bf16_f32 v99, v86, v87\n" \
  "s_waitcnt lgkmcnt(2)\n" \
  "v_mfma_f32_16x16x32_bf16 v[52:55], a[12:15], v[28:31], v[52:55]\n" \
  "ds_read_b128 v[24:27], v9 offset:5120\n" \
  "s_add_u32 s52, s60, s73\n" \
  "buffer_store_dwordx4 v[96:99], v7, s[36:39], s52 offen offset:0 sc1\n" \
  "s_add_u32 s60, s60, 49152\n" \
  "v_mfma_f32_16x16x32_bf16 v[56:59], a[92:95], v[28:31], v[56:59]\n" \
  "s_add_u32 m0, s58, 0\n" \
  "s_add_u32 s54, s57, 0\n" \
  "buffer_load_dwordx4 v1, s[36:39], s54 offen offset:0 sc1 lds\n" \
  "v_mfma_f32_16x16x32_bf16 v[60:63], v[28:31], a[172:175], v[60:63]\n" \
  "s_waitcnt lgkmcnt(2)\n" \
  "v_mfma_f32_16x16x32_bf16 v[40:43], a[16:19], v[16:19], v[40:43]\n" \
  "ds_read_b128 v[28:31], v9 offset:15360\n" \
  "v_mfma_f32_16x16x32_bf16 v[44:47], a[96:99], v[16:19], v[44:47]\n" \
  "buffer_load_dwordx4 v1, s[36:39], s54 offen offset:1024 sc1 lds\n" \
  "v_mfma_f32_16x16x32_bf16 v[48:51], v[16:19], a[176:179], v[48:51]\n" \
  "s_waitcnt lgkmcnt(2)\n" \
  "v_mfma_f32_16x16x32_bf16 v[52:55], a[16:19], v[20:23], v[52:55]\n" \
  "ds_read_b128 v[16:19], v9 offset:6144\n" \
  "v_mfma_f32_16x16x32_bf16 v[56:59], a[96:99], v[20:23], v[56:59]\n" \
  "buffer_load_dwordx4 v1, s[36:39], s54 offen offset:2048 sc1 lds\n" \
  "v_mfma_f32_16x16x32_bf16 v[60:63], v[20:23], a[176:179], v[60:63]\n" \
  "s_waitcnt lgkmcnt(2)\n" \
  "v_mfma_f32_16x16x32_bf16 v[40:43], a[20:23], v[24:27], v[40:43]\n" \
  "ds_read_b128 v[20:23], v9 offset:16384\n" \
  "v_mfma_f32_16x16x32_bf16 v[44:47], a[100:103], v[24:27], v[44:47]\n" \
  "buffer_load_dwordx4 v1, s[36:39], s54 offen offset:3072 sc1 lds\n" \
  "v_mfma_f32_16x16x32_bf16 v[48:51], v[24:27], a[180:183], v[48:51]\n" \
  "s_waitcnt lgkmcnt(2)\n" \
  "v_mfma_f32_16x16x32_bf16 v[52:55], a[20:23], v[28:31], v[52:55]\n" \
  "ds_read_b128 v[24:27], v9 offset:7168\n" \
  "v_mfma_f32_16x16x32_bf16 v[56:59], a[100:103], v[28:31], v[56:59]\n" \
  "s_add_u32 m0, s58, 4096\n" \
  "s_add_u32 s54, s57, 4096\n" \
  "buffer_load_dwordx4 v1, s[36:39], s54 offen offset:0 sc1 lds\n" \
  "v_mfma_f32_16x16x32_bf16 v[60:63], v[28:31], a[180:183], v[60:63]\n" \
  "s_waitcnt lgkmcnt(2)\n" \
  "v_mfma_f32_16x16x32_bf16 v[40:43], a[24:27], v[16:19], v[40:43]\n" \
  "ds_read_b128 v[28:31], v9 offset:17408\n" \
  "v_mfma_f32_16x16x32_bf16 v[44:47], a[104:107], v[16:19], v[44:47]\n" \
  "buffer_load_dwordx4 v1, s[36:39], s54 offen offset:1024 sc1 lds\n" \
  "v_mfma_f32_16x16x32_bf16 v[48:51], v[16:19], a[184:187], v[48:51]\n" \
  "s_waitcnt lgkmcnt(2)\n" \
  "v_mfma_f32_16x16x32_bf16 v[52:55], a[24:27], v[20:23], v[52:55]\n" \
  "ds_read_b128 v[16:19], v9 offset:8192\n" \
  "v_mfma_f32_16x16x32_bf16 v[56:59], a[104:107], v[20:23], v[56:59]\n" \
  "buffer_load_dwordx4 v1, s[36:39], s54 offen offset:2048 sc1 lds\n" \
  "v_mfma_f32_16x16x32_bf16 v[60:63], v[20:23], a[184:187], v[60:63]\n" \
  "s_waitcnt lgkmcnt(2)\n" \
  "v_mfma_f32_16x16x32_bf16 v[40:43], a[28:31], v[24:27], v[40:43]\n" \
  "ds_read_b128 v[20:23], v9 offset:18432\n" \
  "v_mfma_f32_16x16x32_bf16 v[44:47], a[108:111], v[24:27], v[44:47]\n" \
  "buffer_load_dwordx4 v1, s[36:39], s54 offen offset:3072 sc1 lds\n" \
  "v_mfma_f32_16x16x32_bf16 v[48:51], v[24:27], a[188:191], v[48:51]\n" \
  "s_waitcnt lgkmcnt(2)\n" \
  "v_mfma_f32_16x16x32_bf16 v[52:55], a[28:31], v[28:31], v[52:55]\n" \
  "ds_read_b128 v[24:27], v9 offset:9216\n" \
  "v_mfma_f32_16x16x32_bf16 v[56:59], a[108:111], v[28:31], v[56:59]\n" \
  "s_add_u32 m0, s58, 8192\n" \
  "s_add_u32 s54, s57, 8192\n" \
  "buffer_load_dwordx4 v1, s[36:39], s54 offen offset:0 sc1 lds\n" \
  "v_mfma_f32_16x16x32_bf16 v[60:63], v[28:31], a[188:191], v[60:63]\n" \
  "s_waitcnt lgkmcnt(2)\n" \
  "v_mfma_f32_16x16x32_bf16 v[40:43], a[32:35], v[16:19], v[40:43]\n" \
  "ds_read_b128 v[28:31], v9 offset:19456\n" \
  "v_mfma_f32_16x16x32_bf16 v[44:47], a[112:115], v[16:19], v[44:47]\n" \
  "buffer_load_dwordx4 v1, s[36:39], s54 offen offset:1024 sc1 lds\n" \
  "v_mfma_f32_16x16x32_bf16 v[48:51], v[16:19], a[192:195], v[48:51]\n" \
  "s_waitcnt lgkmcnt(0)\n" \
  "s_waitcnt vmcnt(10)\n" \
  "s_barrier\n" \
  "s_cmp_lg_u32 s48, 0\n" \
  "s_cbranch_scc1 WSG1_%=\n" \
  "s_lshl_b32 s52, s55, 1\n" \
  "v_mov_b32 v4, s52\n" \
  "buffer_store_dword v4, off, s[36:39], s61 sc1\n" \
  "WSG1_%=:\n" \
  "v_mfma_f32_16x16x32_bf16 v[52:55], a[32:35], v[20:23], v[52:55]\n" \
  "ds_read_b128 v[16:19], v10 offset:0\n" \
  "v_mfma_f32_16x16x32_bf16 v[56:59], a[112:115], v[20:23], v[56:59]\n" \
  "v_mfma_f32_16x16x32_bf16 v[60:63], v[20:23], a[192:195], v[60:63]\n" \
  "v_mfma_f32_16x16x32_bf16 v[40:43], a[36:39], v[24:27], v[40:43]\n" \
  "ds_read_b128 v[20:23], v10 offset:10240\n" \
  "v_mfma_f32_16x16x32_bf16 v[44:47], a[116:119], v[24:27], v[44:47]\n" \
  "v_mfma_f32_16x16x32_bf16 v[48:51], v[24:27], a[196:199], v[48:51]\n" \
  "v_mfma_f32_16x16x32_bf16 v[52:55], a[36:39], v[28:31], v[52:55]\n" \
  "ds_read_b128 v[24:27], v10 offset:1024\n" \
  "v_mfma_f32_16x16x32_bf16 v[56:59], a[116:119], v[28:31], v[56:59]\n" \
  "v_mfma_f32_16x16x32_bf16 v[60:63], v[28:31], a[196:199], v[60:63]\n" \
  "s_add_u32 s57, s57, 10240\n" \
  "s_add_u32 s58, s58, 40960\n" \
  "s_sub_u32 s54, s58, 122880\n" \
  "s_cmp_ge_u32 s58, 122880\n" \
  "s_cselect_b32 s58, s54, s58\n" \
  "v_mov_b32 v9, v10\n" \
  "v_add_u32 v10, s59, v8\n" \
  "s_add_u32 s59, s59, 40960\n" \
  "s_cmp_ge_u32 s59, 122880\n" \
  "s_cselect_b32 s59, 0, s59\n" \
  "s_waitcnt lgkmcnt(2)\n" \
  "v_mfma_f32_16x16x32_bf16 v[40:43], a[40:43], v[16:19], v[40:43]\n" \
  "ds_read_b128 v[28:31], v9 offset:11264\n" \
  "v_mfma_f32_16x16x32_bf16 v[44:47], a[120:123], v[16:19], v[44:47]\n" \
  "v_mfma_f32_16x16x32_bf16 v[48:51], v[16:19], a[200:203], v[48:51]\n" \
  "s_waitcnt lgkmcnt(2)\n" \
  "v_mfma_f32_16x16x32_bf16 v[52:55], a[40:43], v[20:23], v[52:55]\n" \
  "ds_read_b128 v[16:19], v9 offset:2048\n" \
  "v_mfma_f32_16x16x32_bf16 v[56:59], a[120:123], v[20:23], v[56:59]\n" \
  "s_add_u32 m0, s58, 0\n" \
  "s_add_u32 s54, s57, 0\n" \
  "buffer_load_dwordx4 v1, s[36:39], s54 offen offset:0 sc1 lds\n" \
  "v_mfma_f32_16x16x32_bf16 v[60:63], v[20:23], a[200:203], v[60:63]\n" \
  "s_waitcnt lgkmcnt(2)\n" \
  "v_mfma_f32_16x16x32_bf16 v[40:43], a[44:47], v[24:27], v[40:43]\n" \
  "ds_read_b128 v[20:23], v9 offset:12288\n" \
  "v_mfma_f32_16x16x32_bf16 v[44:47], a[124:127], v[24:27], v[44:47]\n" \
  "buffer_load_dwordx4 v1, s[36:39], s54 offen offset:1024 sc1 lds\n" \
  "v_mfma_f32_16x16x32_bf16 v[48:51], v[24:27], a[204:207], v[48:51]\n" \
  "s_waitcnt lgkmcnt(2)\n" \
  "v_mfma_f32_16x16x32_bf16 v[52:55], a[44:47], v[28:31], v[52:55]\n" \
  "ds_read_b128 v[24:27], v9 offset:3072\n" \
  "v_mfma_f32_16x16x32_bf16 v[56:59], a[124:127], v[28:31], v[56:59]\n" \
  "v_mfma_f32_16x16x32_bf16 v[60:63], v[28:31], a[204:207], v[60:63]\n" \
  "s_waitcnt lgkmcnt(2)\n" \
  "v_mfma_f32_16x16x32_bf16 v[40:43], a[48:51], v[16:19], v[40:43]\n" \
  "ds_read_b128 v[28:31], v9 offset:13312\n" \
  "v_mfma_f32_16x16x32_bf16 v[44:47], a[128:131], v[16:19], v[44:47]\n" \
  "buffer_load_dwordx4 v1, s[36:39], s54 offen offset:2048 sc1 lds\n" \
  "v_mfma_f32_16x16x32_bf16 v[48:51], v[16:19], a[208:211], v[48:51]\n" \
  "s_waitcnt lgkmcnt(2)\n" \
  "v_mfma_f32_16x16x32_bf16 v[52:55], a[48:51], v[20:23], v[52:55]\n" \
  "ds_read_b128 v[16:19], v9 offset:4096\n" \
  "v_mfma_f32_16x16x32_bf16 v[56:59], a[128:131], v[20:23], v[56:59]\n" \
  "buffer_load_dwordx4 v1, s[36:39], s54 offen offset:3072 sc1 lds\n" \
  "v_mfma_f32_16x16x32_bf16 v[60:63], v[20:23], a[208:211], v[60:63]\n" \
  "s_waitcnt lgkmcnt(2)\n" \
  "v_mfma_f32_16x16x32_bf16 v[40:43], a[52:55], v[24:27], v[40:43]\n" \
  "ds_read_b128 v[20:23], v9 offset:14336\n" \
  "v_mfma_f32_16x16x32_bf16 v[44:47], a[132:135], v[24:27], v[44:47]\n" \
  "v_mfma_f32_16x16x32_bf16 v[48:51], v[24:27], a[212:215], v[48:51]\n" \
  "s_waitcnt lgkmcnt(2)\n" \
  "v_mfma_f32_16x16x32_bf16 v[52:55], a[52:55], v[28:31], v[52:55]\n" \
  "ds_read_b128 v[24:27], v9 offset:5120\n" \
  "v_mfma_f32_16x16x32_bf16 v[56:59], a[132:135], v[28:31], v[56:59]\n" \
  "s_add_u32 m0, s58, 4096\n" \
  "s_add_u32 s54, s57, 4096\n" \
  "buffer_load_dwordx4 v1, s[36:39], s54 offen offset:0 sc1 lds\n" \
  "v_mfma_f32_16x16x32_bf16 v[60:63], v[28:31], a[212:215], v[60:63]\n" \
  "s_waitcnt lgkmcnt(2)\n" \
  "v_mfma_f32_16x16x32_bf16 v[40:43], a[56:59], v[16:19], v[40:43]\n" \
  "ds_read_b128 v[28:31], v9 offset:15360\n" \
  "v_mfma_f32_16x16x32_bf16 v[44:47], a[136:139], v[16:19], v[44:47]\n" \
  "buffer_load_dwordx4 v1, s[36:39], s54 offen offset:1024 sc1 lds\n" \
  "v_mfma_f32_16x16x32_bf16 v[48:51], v[16:19], a[216:219], v[48:51]\n" \
  "s_waitcnt lgkmcnt(2)\n" \
  "v_mfma_f32_16x16x32_bf16 v[52:55], a[56:59], v[20:23], v[52:55]\n" \
  "ds_read_b128 v[16:19], v9 offset:6144\n" \
  "v_mfma_f32_16x16x32_bf16 v[56:59], a[136:139], v[20:23], v[56:59]\n" \
  "v_mfma_f32_16x16x32_bf16 v[60:63], v[20:23], a[216:219], v[60:63]\n" \
  "s_waitcnt lgkmcnt(2)\n" \
  "v_mfma_f32_16x16x32_bf16 v[40:43], a[60:63], v[24:27], v[40:43]\n" \
  "ds_read_b128 v[20:23], v9 offset:16384\n" \
  "v_mfma_f32_16x16x32_bf16 v[44:47], a[140:143], v[24:27], v[44:47]\n" \
  "buffer_load_dwordx4 v1, s[36:39], s54 offen offset:2048 sc1 lds\n" \
  "v_mfma_f32_16x16x32_bf16 v[48:51], v[24:27], a[220:223], v[48:51]\n" \
  "s_waitcnt lgkmcnt(2)\n" \
  "v_mfma_f32_16x16x32_bf16 v[52:55], a[60:63], v[28:31], v[52:55]\n" \
  "ds_read_b128 v[24:27], v9 offset:7168\n" \
  "v_mfma_f32_16x16x32_bf16 v[56:59], a[140:143], v[28:31], v[56:59]\n" \
  "buffer_load_dwordx4 v1, s[36:39], s54 offen offset:3072 sc1 lds\n" \
  "v_mfma_f32_16x16x32_bf16 v[60:63], v[28:31], a[220:223], v[60:63]\n" \
  "s_waitcnt lgkmcnt(2)\n" \
  "v_mfma_f32_16x16x32_bf16 v[40:43], a[64:67], v[16:19], v[40:43]\n" \
  "ds_read_b128 v[28:31], v9 offset:17408\n" \
  "v_mfma_f32_16x16x32_bf16 v[44:47], a[144:147], v[16:19], v[44:47]\n" \
  "v_mfma_f32_16x16x32_bf16 v[48:51], v[16:19], a[224:227], v[48:51]\n" \
  "s_waitcnt lgkmcnt(2)\n" \
  "v_mfma_f32_16x16x32_bf16 v[52:55], a[64:67], v[20:23], v[52:55]\n" \
  "ds_read_b128 v[16:19], v9 offset:8192\n" \
  "v_mfma_f32_16x16x32_bf16 v[56:59], a[144:147], v[20:23], v[56:59]\n" \
  "s_add_u32 m0, s58, 8192\n" \
  "s_add_u32 s54, s57, 8192\n" \
  "buffer_load_dwordx4 v1, s[36:39], s54 offen offset:0 sc1 lds\n" \
  "v_mfma_f32_16x16x32_bf16 v[60:63], v[20:23], a[224:227], v[60:63]\n" \
  "s_waitcnt lgkmcnt(2)\n" \
  "v_mfma_f32_16x16x32_bf16 v[40:43], a[68:71], v[24:27], v[40:43]\n" \
  "ds_read_b128 v[20:23], v9 offset:18432\n" \
  "v_mfma_f32_16x16x32_bf16 v[44:47], a[148:151], v[24:27], v[44:47]\n" \
  "buffer_load_dwordx4 v1, s[36:39], s54 offen offset:1024 sc1 lds\n" \
  "v_mfma_f32_16x16x32_bf16 v[48:51], v[24:27], a[228:231], v[48:51]\n" \
  "s_waitcnt lgkmcnt(2)\n" \
  "v_mfma_f32_16x16x32_bf16 v[52:55], a[68:71], v[28:31], v[52:55]\n" \
  "ds_read_b128 v[24:27], v9 offset:9216\n" \
  "v_mfma_f32_16x16x32_bf16 v[56:59], a[148:151], v[28:31], v[56:59]\n" \
  "v_mfma_f32_16x16x32_bf16 v[60:63], v[28:31], a[228:231], v[60:63]\n" \
  "s_waitcnt lgkmcnt(2)\n" \
  "v_mfma_f32_16x16x32_bf16 v[40:43], a[72:75], v[16:19], v[40:43]\n" \
  "ds_read_b128 v[28:31], v9 offset:19456\n" \
  "v_mfma_f32_16x16x32_bf16 v[44:47], a[152:155], v[16:19], v[44:47]\n" \
  "v_mfma_f32_16x16x32_bf16 v[48:51], v[16:19], a[232:235], v[48:51]\n" \
  "s_waitcnt lgkmcnt(0)\n" \
  "s_waitcnt vmcnt(10)\n" \
  "s_barrier\n" \
  "v_mfma_f32_16x16x32_bf16 v[52:55], a[72:75], v[20:23], v[52:55]\n" \
  "ds_read_b128 v[16:19], v10 offset:0\n" \
  "v_mfma_f32_16x16x32_bf16 v[56:59], a[152:155], v[20:23], v[56:59]\n" \
  "v_mfma_f32_16x16x32_bf16 v[60:63], v[20:23], a[232:235], v[60:63]\n" \
  "v_mfma_f32_16x16x32_bf16 v[40:43], a[76:79], v[24:27], v[40:43]\n" \
  "ds_read_b128 v[20:23], v10 offset:10240\n" \
  "v_mfma_f32_16x16x32_bf16 v[44:47], a[156:159], v[24:27], v[44:47]\n" \
  "v_mfma_f32_16x16x32_bf16 v[48:51], v[24:27], a[236:239], v[48:51]\n" \
  "v_mfma_f32_16x16x32_bf16 v[52:55], a[76:79], v[28:31], v[52:55]\n" \
  "ds_read_b128 v[24:27], v10 offset:1024\n" \
  "v_mfma_f32_16x16x32_bf16 v[56:59], a[156:159], v[28:31], v[56:59]\n" \
  "v_mfma_f32_16x16x32_bf16 v[60:63], v[28:31], a[236:239], v[60:63]\n" \
  "s_add_u32 s57, s57, 71680\n" \
  "s_min_u32 s57, s57, s76\n" \
  "s_add_u32 s58, s58, 40960\n" \
  "s_sub_u32 s54, s58, 122880\n" \
  "s_cmp_ge_u32 s58, 122880\n" \
  "s_cselect_b32 s58, s54, s58\n" \
  "v_mov_b32 v9, v10\n" \
  "v_mov_b32 v7, v1\n" \
  "s_add_u32 s55, s55, 1\n" \
  "WAP1_%=:\n" \
  "v_add_u32 v10, s59, v8\n" \
  "s_add_u32 s59, s59, 40960\n" \
  "s_cmp_ge_u32 s59, 122880\n" \
  "s_cselect_b32 s59, 0, s59\n" \
  "s_waitcnt lgkmcnt(2)\n" \
  "v_mfma_f32_16x16x32_bf16 v[64:67], a[0:3], v[16:19], 0\n" \
  "ds_read_b128 v[28:31], v9 offset:11264\n" \
  "v_mfma_f32_16x16x32_bf16 v[68:71], a[80:83], v[16:19], 0\n" \
  "v_mfma_f32_16x16x32_bf16 v[72:75], v[16:19], a[160:163], 0\n" \
  "s_waitcnt lgkmcnt(2)\n" \
  "v_mfma_f32_16x16x32_bf16 v[76:79], a[0:3], v[20:23], 0\n" \
  "ds_read_b128 v[16:19], v9 offset:2048\n" \
  "v_mfma_f32_16x16x32_bf16 v[80:83], a[80:83], v[20:23], 0\n" \
  "v_mfma_f32_16x16x32_bf16 v[84:87], v[20:23], a[160:163], 0\n" \
  "s_waitcnt lgkmcnt(2)\n" \
  "v_mfma_f32_16x16x32_bf16 v[64:67], a[4:7], v[24:27], v[64:67]\n" \
  "ds_read_b128 v[20:23], v9 offset:12288\n" \
  "v_mul_f32 v40, s69, v40\n" \
  "v_mul_f32 v41, s69, v41\n" \
  "v_mfma_f32_16x16x32_bf16 v[68:71], a[84:87], v[24:27], v[68:71]\n" \
  "v_mul_f32 v42, s69, v42\n" \
  "v_mul_f32 v43, s69, v43\n" \
  "v_mfma_f32_16x16x32_bf16 v[72:75], v[24:27], a[164:167], v[72:75]\n" \
  "v_cvt_pk_bf16_f32 v88, v40, v41\n" \
  "v_cvt_pk_bf16_f32 v89, v42, v43\n" \
  "s_waitcnt lgkmcnt(2)\n" \
  "v_mfma_f32_16x16x32_bf16 v[76:79], a[4:7], v[28:31], v[76:79]\n" \
  "ds_read_b128 v[24:27], v9 offset:3072\n" \
  "v_mul_f32 v44, s69, v44\n" \
  "v_mul_f32 v45, s69, v45\n" \
  "v_mfma_f32_16x16x32_bf16 v[80:83], a[84:87], v[28:31], v[80:83]\n" \
  "v_mul_f32 v46, s69, v46\n" \
  "v_mul_f32 v47, s69, v47\n" \
  "v_mfma_f32_16x16x32_bf16 v[84:87], v[28:31], a[164:167], v[84:87]\n" \
  "v_cvt_pk_bf16_f32 v90, v44, v45\n" \
  "v_cvt_pk_bf16_f32 v91, v46, v47\n" \
  "s_waitcnt lgkmcnt(2)\n" \
  "v_mfma_f32_16x16x32_bf16 v[64:67], a[8:11], v[16:19], v[64:67]\n" \
  "ds_read_b128 v[28:31], v9 offset:13312\n" \
  "s_add_u32 s52, s60, s72\n" \
  "s_add_u32 s52, s52, 0\n" \
  "buffer_store_dwordx4 v[88:91], v7, s[36:39], s52 offen offset:0 sc1\n" \
  "v_cvt_pk_bf16_f32 v96, v48, v49\n" \
  "v_mfma_f32_16x16x32_bf16 v[68:71], a[88:91], v[16:19], v[68:71]\n" \
  "v_cvt_pk_bf16_f32 v97, v50, v51\n" \
  "v_mul_f32 v52, s69, v52\n" \
  "v_mfma_f32_16x16x32_bf16 v[72:75], v[16:19], a[168:171], v[72:75]\n" \
  "v_mul_f32 v53, s69, v53\n" \
  "v_mul_f32 v54, s69, v54\n" \
  "s_waitcnt lgkmcnt(2)\n" \
  "v_mfma_f32_16x16x32_bf16 v[76:79], a[8:11], v[20:23], v[76:79]\n" \
  "ds_read_b128 v[16:19], v9 offset:4096\n" \
  "v_mul_f32 v55, s69, v55\n" \
  "v_cvt_pk_bf16_f32 v92, v52, v53\n" \
  "v_mfma_f32_16x16x32_bf16 v[80:83], a[88:91], v[20:23], v[80:83]\n" \
  "v_cvt_pk_bf16_f32 v93, v54, v55\n" \
  "v_mul_f32 v56, s69, v56\n" \
  "v_mfma_f32_16x16x32_bf16 v[84:87], v[20:23], a[168:171], v[84:87]\n" \
  "v_mul_f32 v57, s69, v57\n" \
  "v_mul_f32 v58, s69, v58\n" \
  "s_waitcnt lgkmcnt(2)\n" \
  "v_mfma_f32_16x16x32_bf16 v[64:67], a[12:15], v[24:27], v[64:67]\n" \
  "ds_read_b128 v[20:23], v9 offset:14336\n" \
  "v_mul_f32 v59, s69, v59\n" \
  "v_cvt_pk_bf16_f32 v94, v56, v57\n" \
  "v_mfma_f32_16x16x32_bf16 v[68:71], a[92:95], v[24:27], v[68:71]\n" \
  "v_cvt_pk_bf16_f32 v95, v58, v59\n" \
  "s_add_u32 s52, s60, s72\n" \
  "s_add_u32 s52, s52, 4096\n" \
  "buffer_store_dwordx4 v[92:95], v7, s[36:39], s52 offen offset:0 sc1\n" \
  "v_mfma_f32_16x16x32_bf16 v[72:75], v[24:27], a[172:175], v[72:75]\n" \
  "v_cvt_pk_bf16_f32 v98, v60, v61\n" \
  "v_cvt_pk_bf16_f32 v99, v62, v63\n" \
  "s_waitcnt lgkmcnt(2)\n" \
  "v_mfma_f32_16x16x32_bf16 v[76:79], a[12:15], v[28:31], v[76:79]\n" \
  "ds_read_b128 v[24:27], v9 offset:5120\n" \
  "s_lshr_b32 s52, s55, 1\n" \
  "s_sub_u32 s52, s52, 3\n" \
  "s_min_u32 s52, s52, 10\n" \
  "s_lshl_b32 s52, s52, 12\n" \
  "s_add_u32 s52, s52, s71\n" \
  "buffer_store_dwordx4 v[32:35], v1, s[36:39], s52 offen offset:0 sc1\n" \
  "s_add_u32 s52, s60, s73\n" \
  "buffer_store_dwordx4 v[96:99], v7, s[36:39], s52 offen offset:0 sc1\n" \
  "s_add_u32 s60, s60, 49152\n" \
  "v_mfma_f32_16x16x32_bf16 v[80:83], a[92:95], v[28:31], v[80:83]\n" \
  "s_add_u32 m0, s58, 0\n" \
  "s_add_u32 s54, s57, 0\n" \
  "buffer_load_dwordx4 v1, s[36:39], s54 offen offset:0 sc1 lds\n" \
  "v_mfma_f32_16x16x32_bf16 v[84:87], v[28:31], a[172:175], v[84:87]\n" \
  "s_waitcnt lgkmcnt(2)\n" \
  "v_mfma_f32_16x16x32_bf16 v[64:67], a[16:19], v[16:19], v[64:67]\n" \
  "ds_read_b128 v[28:31], v9 offset:15360\n" \
  "v_mfma_f32_16x16x32_bf16 v[68:71], a[96:99], v[16:19], v[68:71]\n" \
  "buffer_load_dwordx4 v1, s[36:39], s54 offen offset:1024 sc1 lds\n" \
  "v_mfma_f32_16x16x32_bf16 v[72:75], v[16:19], a[176:179], v[72:75]\n" \
  "s_waitcnt lgkmcnt(2)\n" \
  "v_mfma_f32_16x16x32_bf16 v[76:79], a[16:19], v[20:23], v[76:79]\n" \
  "ds_read_b128 v[16:19], v9 offset:6144\n" \
  "v_mfma_f32_16x16x32_bf16 v[80:83], a[96:99], v[20:23], v[80:83]\n" \
  "buffer_load_dwordx4 v1, s[36:39], s54 offen offset:2048 sc1 lds\n" \
  "v_mfma_f32_16x16x32_bf16 v[84:87], v[20:23], a[176:179], v[84:87]\n" \
  "s_waitcnt lgkmcnt(2)\n" \
  "v_mfma_f32_16x16x32_bf16 v[64:67], a[20:23], v[24:27], v[64:67]\n" \
  "ds_read_b128 v[20:23], v9 offset:16384\n" \
  "v_mfma_f32_16x16x32_bf16 v[68:71], a[100:103], v[24:27], v[68:71]\n" \
  "buffer_load_dwordx4 v1, s[36:39], s54 offen offset:3072 sc1 lds\n" \
  "v_mfma_f32_16x16x32_bf16 v[72:75], v[24:27], a[180:183], v[72:75]\n" \
  "s_waitcnt lgkmcnt(2)\n" \
  "v_mfma_f32_16x16x32_bf16 v[76:79], a[20:23], v[28:31], v[76:79]\n" \
  "ds_read_b128 v[24:27], v9 offset:7168\n" \
  "v_mfma_f32_16x16x32_bf16 v[80:83], a[100:103], v[28:31], v[80:83]\n" \
  "s_add_u32 m0, s58, 4096\n" \
  "s_add_u32 s54, s57, 4096\n" \
  "buffer_load_dwordx4 v1, s[36:39], s54 offen offset:0 sc1 lds\n" \
  "v_mfma_f32_16x16x32_bf16 v[84:87], v[28:31], a[180:183], v[84:87]\n" \
  "s_waitcnt lgkmcnt(2)\n" \
  "v_mfma_f32_16x16x32_bf16 v[64:67], a[24:27], v[16:19], v[64:67]\n" \
  "ds_read_b128 v[28:31], v9 offset:17408\n" \
  "v_mfma_f32_16x16x32_bf16 v[68:71], a[104:107], v[16:19], v[68:71]\n" \
  "buffer_load_dwordx4 v1, s[36:39], s54 offen offset:1024 sc1 lds\n" \
  "v_mfma_f32_16x16x32_bf16 v[72:75], v[16:19], a[184:187], v[72:75]\n" \
  "s_waitcnt lgkmcnt(2)\n" \
  "v_mfma_f32_16x16x32_bf16 v[76:79], a[24:27], v[20:23], v[76:79]\n" \
  "ds_read_b128 v[16:19], v9 offset:8192\n" \
  "v_mfma_f32_16x16x32_bf16 v[80:83], a[104:107], v[20:23], v[80:83]\n" \
  "buffer_load_dwordx4 v1, s[36:39], s54 offen offset:2048 sc1 lds\n" \
  "v_mfma_f32_16x16x32_bf16 v[84:87], v[20:23], a[184:187], v[84:87]\n" \
  "s_waitcnt lgkmcnt(2)\n" \
  "v_mfma_f32_16x16x32_bf16 v[64:67], a[28:31], v[24:27], v[64:67]\n" \
  "ds_read_b128 v[20:23], v9 offset:18432\n" \
  "v_mfma_f32_16x16x32_bf16 v[68:71], a[108:111], v[24:27], v[68:71]\n" \
  "buffer_load_dwordx4 v1, s[36:39], s54 offen offset:3072 sc1 lds\n" \
  "v_mfma_f32_16x16x32_bf16 v[72:75], v[24:27], a[188:191], v[72:75]\n" \
  "s_waitcnt lgkmcnt(2)\n" \
  "v_mfma_f32_16x16x32_bf16 v[76:79], a[28:31], v[28:31], v[76:79]\n" \
  "ds_read_b128 v[24:27], v9 offset:9216\n" \
  "v_mfma_f32_16x16x32_bf16 v[80:83], a[108:111], v[28:31], v[80:83]\n" \
  "s_add_u32 m0, s58, 8192\n" \
  "s_add_u32 s54, s57, 8192\n" \
  "buffer_load_dwordx4 v1, s[36:39], s54 offen offset:0 sc1 lds\n" \
  "v_mfma_f32_16x16x32_bf16 v[84:87], v[28:31], a[188:191], v[84:87]\n" \
  "s_waitcnt lgkmcnt(2)\n" \
  "v_mfma_f32_16x16x32_bf16 v[64:67], a[32:35], v[16:19], v[64:67]\n" \
  "ds_read_b128 v[28:31], v9 offset:19456\n" \
  "v_mfma_f32_16x16x32_bf16 v[68:71], a[112:115], v[16:19], v[68:71]\n" \
  "buffer_load_dwordx4 v1, s[36:39], s54 offen offset:1024 sc1 lds\n" \
  "v_mfma_f32_16x16x32_bf16 v[72:75], v[16:19], a[192:195], v[72:75]\n" \
  "s_waitcnt lgkmcnt(0)\n" \
  "s_waitcnt vmcnt(10)\n" \
  "s_barrier\n" \
  "s_cmp_lg_u32 s48, 0\n" \
  "s_cbranch_scc1 WSG2_%=\n" \
  "s_lshl_b32 s52, s55, 1\n" \
  "v_mov_b32 v4, s52\n" \
  "buffer_store_dword v4, off, s[36:39], s61 sc1\n" \
  "WSG2_%=:\n" \
  "v_mfma_f32_16x16x32_bf16 v[76:79], a[32:35], v[20:23], v[76:79]\n" \
  "ds_read_b128 v[16:19], v10 offset:0\n" \
  "v_mfma_f32_16x16x32_bf16 v[80:83], a[112:115], v[20:23], v[80:83]\n" \
  "v_mfma_f32_16x16x32_bf16 v[84:87], v[20:23], a[192:195], v[84:87]\n" \
  "v_mfma_f32_16x16x32_bf16 v[64:67], a[36:39], v[24:27], v[64:67]\n" \
  "ds_read_b128 v[20:23], v10 offset:10240\n" \
  "v_mfma_f32_16x16x32_bf16 v[68:71], a[116:119], v[24:27], v[68:71]\n" \
  "v_mfma_f32_16x16x32_bf16 v[72:75], v[24:27], a[196:199], v[72:75]\n" \
  "v_mfma_f32_16x16x32_bf16 v[76:79], a[36:39], v[28:31], v[76:79]\n" \
  "ds_read_b128 v[24:27], v10 offset:1024\n" \
  "v_mfma_f32_16x16x32_bf16 v[80:83], a[116:119], v[28:31], v[80:83]\n" \
  "v_mfma_f32_16x16x32_bf16 v[84:87], v[28:31], a[196:199], v[84:87]\n" \
  "s_add_u32 s57, s57, 10240\n" \
  "s_add_u32 s58, s58, 40960\n" \
  "s_sub_u32 s54, s58, 122880\n" \
  "s_cmp_ge_u32 s58, 122880\n" \
  "s_cselect_b32 s58, s54, s58\n" \
  "v_mov_b32 v9, v10\n" \
  "v_add_u32 v10, s59, v8\n" \
  "s_add_u32 s59, s59, 40960\n" \
  "s_cmp_ge_u32 s59, 122880\n" \
  "s_cselect_b32 s59, 0, s59\n" \
  "s_waitcnt lgkmcnt(2)\n" \
  "v_mfma_f32_16x16x32_bf16 v[64:67], a[40:43], v[16:19], v[64:67]\n" \
  "ds_read_b128 v[28:31], v9 offset:11264\n" \
  "v_mfma_f32_16x16x32_bf16 v[68:71], a[120:123], v[16:19], v[68:71]\n" \
  "v_mfma_f32_16x16x32_bf16 v[72:75], v[16:19], a[200:203], v[72:75]\n" \
  "s_waitcnt lgkmcnt(2)\n" \
  "v_mfma_f32_16x16x32_bf16 v[76:79], a[40:43], v[20:23], v[76:79]\n" \
  "ds_read_b128 v[16:19], v9 offset:2048\n" \
  "v_mfma_f32_16x16x32_bf16 v[80:83], a[120:123], v[20:23], v[80:83]\n" \
  "s_add_u32 m0, s58, 0\n" \
  "s_add_u32 s54, s57, 0\n" \
  "buffer_load_dwordx4 v1, s[36:39], s54 offen offset:0 sc1 lds\n" \
  "v_mfma_f32_16x16x32_bf16 v[84:87], v[20:23], a[200:203], v[84:87]\n" \
  "s_waitcnt lgkmcnt(2)\n" \
  "v_mfma_f32_16x16x32_bf16 v[64:67], a[44:47], v[24:27], v[64:67]\n" \
  "ds_read_b128 v[20:23], v9 offset:12288\n" \
  "v_mfma_f32_16x16x32_bf16 v[68:71], a[124:127], v[24:27], v[68:71]\n" \
  "buffer_load_dwordx4 v1, s[36:39], s54 offen offset:1024 sc1 lds\n" \
  "v_mfma_f32_16x16x32_bf16 v[72:75], v[24:27], a[204:207], v[72:75]\n" \
  "s_waitcnt lgkmcnt(2)\n" \
  "v_mfma_f32_16x16x32_bf16 v[76:79], a[44:47], v[28:31], v[76:79]\n" \
  "ds_read_b128 v[24:27], v9 offset:3072\n" \
  "v_mfma_f32_16x16x32_bf16 v[80:83], a[124:127], v[28:31], v[80:83]\n" \
  "v_mfma_f32_16x16x32_bf16 v[84:87], v[28:31], a[204:207], v[84:87]\n" \
  "s_waitcnt lgkmcnt(2)\n" \
  "v_mfma_f32_16x16x32_bf16 v[64:67], a[48:51], v[16:19], v[64:67]\n" \
  "ds_read_b128 v[28:31], v9 offset:13312\n" \
  "v_mfma_f32_16x16x32_bf16 v[68:71], a[128:131], v[16:19], v[68:71]\n" \
  "buffer_load_dwordx4 v1, s[36:39], s54 offen offset:2048 sc1 lds\n" \
  "v_mfma_f32_16x16x32_bf16 v[72:75], v[16:19], a[208:211], v[72:75]\n" \
  "s_waitcnt lgkmcnt(2)\n" \
  "v_mfma_f32_16x16x32_bf16 v[76:79], a[48:51], v[20:23], v[76:79]\n" \
  "ds_read_b128 v[16:19], v9 offset:4096\n" \
  "v_mfma_f32_16x16x32_bf16 v[80:83], a[128:131], v[20:23], v[80:83]\n" \
  "buffer_load_dwordx4 v1, s[36:39], s54 offen offset:3072 sc1 lds\n" \
  "v_mfma_f32_16x16x32_bf16 v[84:87], v[20:23], a[208:211], v[84:87]\n" \
  "s_waitcnt lgkmcnt(2)\n" \
  "v_mfma_f32_16x16x32_bf16 v[64:67], a[52:55], v[24:27], v[64:67]\n" \
  "ds_read_b128 v[20:23], v9 offset:14336\n" \
  "v_mfma_f32_16x16x32_bf16 v[68:71], a[132:135], v[24:27], v[68:71]\n" \
  "v_mfma_f32_16x16x32_bf16 v[72:75], v[24:27], a[212:215], v[72:75]\n" \
  "s_waitcnt lgkmcnt(2)\n" \
  "v_mfma_f32_16x16x32_bf16 v[76:79], a[52:55], v[28:31], v[76:79]\n" \
  "ds_read_b128 v[24:27], v9 offset:5120\n" \
  "v_mfma_f32_16x16x32_bf16 v[80:83], a[132:135], v[28:31], v[80:83]\n" \
  "s_add_u32 m0, s58, 4096\n" \
  "s_add_u32 s54, s57, 4096\n" \
  "buffer_load_dwordx4 v1, s[36:39], s54 offen offset:0 sc1 lds\n" \
  "v_mfma_f32_16x16x32_bf16 v[84:87], v[28:31], a[212:215], v[84:87]\n" \
  "s_waitcnt lgkmcnt(2)\n" \
  "v_mfma_f32_16x16x32_bf16 v[64:67], a[56:59], v[16:19], v[64:67]\n" \
  "ds_read_b128 v[28:31], v9 offset:15360\n" \
  "v_mfma_f32_16x16x32_bf16 v[68:71], a[136:139], v[16:19], v[68:71]\n" \
  "buffer_load_dwordx4 v1, s[36:39], s54 offen offset:1024 sc1 lds\n" \
  "v_mfma_f32_16x16x32_bf16 v[72:75], v[16:19], a[216:219], v[72:75]\n" \
  "s_waitcnt lgkmcnt(2)\n" \
  "v_mfma_f32_16x16x32_bf16 v[76:79], a[56:59], v[20:23], v[76:79]\n" \
  "ds_read_b128 v[16:19], v9 offset:6144\n" \
  "v_mfma_f32_16x16x32_bf16 v[80:83], a[136:139], v[20:23], v[80:83]\n" \
  "v_mfma_f32_16x16x32_bf16 v[84:87], v[20:23], a[216:219], v[84:87]\n" \
  "s_waitcnt lgkmcnt(2)\n" \
  "v_mfma_f32_16x16x32_bf16 v[64:67], a[60:63], v[24:27], v[64:67]\n" \
  "ds_read_b128 v[20:23], v9 offset:16384\n" \
  "v_mfma_f32_16x16x32_bf16 v[68:71], a[140:143], v[24:27], v[68:71]\n" \
  "buffer_load_dwordx4 v1, s[36:39], s54 offen offset:2048 sc1 lds\n" \
  "v_mfma_f32_16x16x32_bf16 v[72:75], v[24:27], a[220:223], v[72:75]\n" \
  "s_waitcnt lgkmcnt(2)\n" \
  "v_mfma_f32_16x16x32_bf16 v[76:79], a[60:63], v[28:31], v[76:79]\n" \
  "ds_read_b128 v[24:27], v9 offset:7168\n" \
  "v_mfma_f32_16x16x32_bf16 v[80:83], a[140:143], v[28:31], v[80:83]\n" \
  "buffer_load_dwordx4 v1, s[36:39], s54 offen offset:3072 sc1 lds\n" \
  "v_mfma_f32_16x16x32_bf16 v[84:87], v[28:31], a[220:223], v[84:87]\n" \
  "s_waitcnt lgkmcnt(2)\n" \
  "v_mfma_f32_16x16x32_bf16 v[64:67], a[64:67], v[16:19], v[64:67]\n" \
  "ds_read_b128 v[28:31], v9 offset:17408\n" \
  "v_mfma_f32_16x16x32_bf16 v[68:71], a[144:147], v[16:19], v[68:71]\n" \
  "v_mfma_f32_16x16x32_bf16 v[72:75], v[16:19], a[224:227], v[72:75]\n" \
  "s_waitcnt lgkmcnt(2)\n" \
  "v_mfma_f32_16x16x32_bf16 v[76:79], a[64:67], v[20:23], v[76:79]\n" \
  "ds_read_b128 v[16:19], v9 offset:8192\n" \
  "v_mfma_f32_16x16x32_bf16 v[80:83], a[144:147], v[20:23], v[80:83]\n" \
  "s_add_u32 m0, s58, 8192\n" \
  "s_add_u32 s54, s57, 8192\n" \
  "buffer_load_dwordx4 v1, s[36:39], s54 offen offset:0 sc1 lds\n" \
  "v_mfma_f32_16x16x32_bf16 v[84:87], v[20:23], a[224:227], v[84:87]\n" \
  "s_waitcnt lgkmcnt(2)\n" \
  "v_mfma_f32_16x16x32_bf16 v[64:67], a[68:71], v[24:27], v[64:67]\n" \
  "ds_read_b128 v[20:23], v9 offset:18432\n" \
  "v_mfma_f32_16x16x32_bf16 v[68:71], a[148:151], v[24:27], v[68:71]\n" \
  "buffer_load_dwordx4 v1, s[36:39], s54 offen offset:1024 sc1 lds\n" \
  "v_mfma_f32_16x16x32_bf16 v[72:75], v[24:27], a[228:231], v[72:75]\n" \
  "s_waitcnt lgkmcnt(2)\n" \
  "v_mfma_f32_16x16x32_bf16 v[76:79], a[68:71], v[28:31], v[76:79]\n" \
  "ds_read_b128 v[24:27], v9 offset:9216\n" \
  "v_mfma_f32_16x16x32_bf16 v[80:83], a[148:151], v[28:31], v[80:83]\n" \
  "v_mfma_f32_16x16x32_bf16 v[84:87], v[28:31], a[228:231], v[84:87]\n" \
  "s_waitcnt lgkmcnt(2)\n" \
  "v_mfma_f32_16x16x32_bf16 v[64:67], a[72:75], v[16:19], v[64:67]\n" \
  "ds_read_b128 v[28:31], v9 offset:19456\n" \
  "v_mfma_f32_16x16x32_bf16 v[68:71], a[152:155], v[16:19], v[68:71]\n" \
  "v_mfma_f32_16x16x32_bf16 v[72:75], v[16:19], a[232:235], v[72:75]\n" \
  "s_waitcnt lgkmcnt(0)\n" \
  "s_waitcnt vmcnt(10)\n" \
  "s_barrier\n" \
  "v_mfma_f32_16x16x32_bf16 v[76:79], a[72:75], v[20:23], v[76:79]\n" \
  "ds_read_b128 v[16:19], v10 offset:0\n" \
  "v_mfma_f32_16x16x32_bf16 v[80:83], a[152:155], v[20:23], v[80:83]\n" \
  "v_mfma_f32_16x16x32_bf16 v[84:87], v[20:23], a[232:235], v[84:87]\n" \
  "v_mfma_f32_16x16x32_bf16 v[64:67], a[76:79], v[24:27], v[64:67]\n" \
  "ds_read_b128 v[20:23], v10 offset:10240\n" \
  "v_mfma_f32_16x16x32_bf16 v[68:71], a[156:159], v[24:27], v[68:71]\n" \
  "v_mfma_f32_16x16x32_bf16 v[72:75], v[24:27], a[236:239], v[72:75]\n" \
  "v_mfma_f32_16x16x32_bf16 v[76:79], a[76:79], v[28:31], v[76:79]\n" \
  "ds_read_b128 v[24:27], v10 offset:1024\n" \
  "v_mfma_f32_16x16x32_bf16 v[80:83], a[156:159], v[28:31], v[80:83]\n" \
  "v_mfma_f32_16x16x32_bf16 v[84:87], v[28:31], a[236:239], v[84:87]\n" \
  "s_add_u32 s57, s57, 71680\n" \
  "s_min_u32 s57, s57, s76\n" \
  "s_add_u32 s58, s58, 40960\n" \
  "s_sub_u32 s54, s58, 122880\n" \
  "s_cmp_ge_u32 s58, 122880\n" \
  "s_cselect_b32 s58, s54, s58\n" \
  "v_mov_b32 v9, v10\n" \
  "s_add_u32 s55, s55, 1\n" \
  "s_cmp_lt_u32 s55, s56\n" \
  "s_cbranch_scc1 WALP_%=\n" \
  "v_mul_f32 v64, s69, v64\n" \
  "v_mul_f32 v65, s69, v65\n" \
  "v_mul_f32 v66, s69, v66\n" \
  "v_mul_f32 v67, s69, v67\n" \
  "v_cvt_pk_bf16_f32 v88, v64, v65\n" \
  "v_cvt_pk_bf16_f32 v89, v66, v67\n" \
  "v_mul_f32 v68, s69, v68\n" \
  "v_mul_f32 v69, s69, v69\n" \
  "v_mul_f32 v70, s69, v70\n" \
  "v_mul_f32 v71, s69, v71\n" \
  "v_cvt_pk_bf16_f32 v90, v68, v69\n" \
  "v_cvt_pk_bf16_f32 v91, v70, v71\n" \
  "s_add_u32 s52, s60, s72\n" \
  "s_add_u32 s52, s52, 0\n" \
  "buffer_store_dwordx4 v[88:91], v7, s[36:39], s52 offen offset:0 sc1\n" \
  "v_cvt_pk_bf16_f32 v96, v72, v73\n" \
  "v_cvt_pk_bf16_f32 v97, v74, v75\n" \
  "v_mul_f32 v76, s69, v76\n" \
  "v_mul_f32 v77, s69, v77\n" \
  "v_mul_f32 v78, s69, v78\n" \
  "v_mul_f32 v79, s69, v79\n" \
  "v_cvt_pk_bf16_f32 v92, v76, v77\n" \
  "v_cvt_pk_bf16_f32 v93, v78, v79\n" \
  "v_mul_f32 v80, s69, v80\n" \
  "v_mul_f32 v81, s69, v81\n" \
  "v_mul_f32 v82, s69, v82\n" \
  "v_mul_f32 v83, s69, v83\n" \
  "v_cvt_pk_bf16_f32 v94, v80, v81\n" \
  "v_cvt_pk_bf16_f32 v95, v82, v83\n" \
  "s_add_u32 s52, s60, s72\n" \
  "s_add_u32 s52, s52, 4096\n" \
  "buffer_store_dwordx4 v[92:95], v7, s[36:39], s52 offen offset:0 sc1\n" \
  "v_cvt_pk_bf16_f32 v98, v84, v85\n" \
  "v_cvt_pk_bf16_f32 v99, v86, v87\n" \
  "s_add_u32 s52, s60, s73\n" \
  "buffer_store_dwordx4 v[96:99], v7, s[36:39], s52 offen offset:0 sc1\n" \
  "s_add_u32 s60, s60, 49152\n" \
  "s_waitcnt vmcnt(0) lgkmcnt(0)\n" \
  "s_barrier\n" \
  "s_cmp_lg_u32 s48, 0\n" \
  "s_cbranch_scc1 WSG3_%=\n" \
  "s_lshl_b32 s52, s55, 1\n" \
  "v_mov_b32 v4, s52\n" \
  "buffer_store_dword v4, off, s[36:39], s61 sc1\n" \
  "WSG3_%=:\n" \
  "s_cmp_ge_u32 s49, 108\n" \
  "s_cbranch_scc1 WHSKIP_%=\n" \
  "v_lshrrev_b32 v4, 3, v0\n" \
  "v_mul_u32_u24 v11, 3456, v4\n" \
  "v_and_b32 v4, 2, v4\n" \
  "v_and_b32 v5, 7, v0\n" \
  "v_xor_b32 v4, v4, v5\n" \
  "v_lshl_add_u32 v11, v4, 4, v11\n" \
  "v_lshrrev_b32 v4, 3, v0\n" \
  "v_mul_u32_u24 v12, 3456, v4\n" \
  "v_and_b32 v4, 2, v4\n" \
  "v_or_b32 v4, 4, v4\n" \
  "v_and_b32 v5, 7, v0\n" \
  "v_xor_b32 v4, v4, v5\n" \
  "v_lshl_add_u32 v12, v4, 4, v12\n" \
  "s_lshr_b32 s53, s49, 2\n" \
  "s_lshl_b32 s52, s53, 7\n" \
  "s_mul_i32 s54, s48, 552960\n" \
  "s_add_u32 s52, s52, s54\n" \
  "s_mul_i32 s72, s48, 20480\n" \
  "s_add_u32 s80, s26, s52\n" \
  "s_addc_u32 s81, s27, 0\n" \
  "s_add_u32 m0, s72, 0\n" \
  "s_nop 0\n" \
  "global_load_lds_dwordx4 v11, s[80:81]\n" \
  "s_waitcnt vmcnt(4)\n" \
  "s_add_u32 s80, s80, 27648\n" \
  "s_addc_u32 s81, s81, 0\n" \
  "s_add_u32 m0, s72, 1024\n" \
  "s_nop 0\n" \
  "global_load_lds_dwordx4 v12, s[80:81]\n" \
  "s_waitcnt vmcnt(4)\n" \
  "s_add_u32 s80, s80, 27648\n" \
  "s_addc_u32 s81, s81, 0\n" \
  "s_add_u32 m0, s72, 2048\n" \
  "s_nop 0\n" \
  "global_load_lds_dwordx4 v11, s[80:81]\n" \
  "s_waitcnt vmcnt(4)\n" \
  "s_add_u32 s80, s80, 27648\n" \
  "s_addc_u32 s81, s81, 0\n" \
  "s_add_u32 m0, s72, 3072\n" \
  "s_nop 0\n" \
  "global_load_lds_dwordx4 v12, s[80:81]\n" \
  "s_waitcnt vmcnt(4)\n" \
  "s_add_u32 s80, s80, 27648\n" \
  "s_addc_u32 s81, s81, 0\n" \
  "s_add_u32 m0, s72, 4096\n" \
  "s_nop 0\n" \
  "global_load_lds_dwordx4 v11, s[80:81]\n" \
  "s_waitcnt vmcnt(4)\n" \
  "s_add_u32 s80, s80, 27648\n" \
  "s_addc_u32 s81, s81, 0\n" \
  "s_add_u32 m0, s72, 5120\n" \
  "s_nop 0\n" \
  "global_load_lds_dwordx4 v12, s[80:81]\n" \
  "s_waitcnt vmcnt(4)\n" \
  "s_add_u32 s80, s80, 27648\n" \
  "s_addc_u32 s81, s81, 0\n" \
  "s_add_u32 m0, s72, 6144\n" \
  "s_nop 0\n" \
  "global_load_lds_dwordx4 v11, s[80:81]\n" \
  "s_waitcnt vmcnt(4)\n" \
  "s_add_u32 s80, s80, 27648\n" \
  "s_addc_u32 s81, s81, 0\n" \
  "s_add_u32 m0, s72, 7168\n" \
  "s_nop 0\n" \
  "global_load_lds_dwordx4 v12, s[80:81]\n" \
  "s_waitcnt vmcnt(4)\n" \
  "s_add_u32 s80, s80, 27648\n" \
  "s_addc_u32 s81, s81, 0\n" \
  "s_add_u32 m0, s72, 8192\n" \
  "s_nop 0\n" \
  "global_load_lds_dwordx4 v11, s[80:81]\n" \
  "s_waitcnt vmcnt(4)\n" \
  "s_add_u32 s80, s80, 27648\n" \
  "s_addc_u32 s81, s81, 0\n" \
  "s_add_u32 m0, s72, 9216\n" \
  "s_nop 0\n" \
  "global_load_lds_dwordx4 v12, s[80:81]\n" \
  "s_waitcnt vmcnt(4)\n" \
  "s_add_u32 s80, s80, 27648\n" \
  "s_addc_u32 s81, s81, 0\n" \
  "s_add_u32 m0, s72, 10240\n" \
  "s_nop 0\n" \
  "global_load_lds_dwordx4 v11, s[80:81]\n" \
  "s_waitcnt vmcnt(4)\n" \
  "s_add_u32 s80, s80, 27648\n" \
  "s_addc_u32 s81, s81, 0\n" \
  "s_add_u32 m0, s72, 11264\n" \
  "s_nop 0\n" \
  "global_load_lds_dwordx4 v12, s[80:81]\n" \
  "s_waitcnt vmcnt(4)\n" \
  "s_add_u32 s80, s80, 27648\n" \
  "s_addc_u32 s81, s81, 0\n" \
  "s_add_u32 m0, s72, 12288\n" \
  "s_nop 0\n" \
  "global_load_lds_dwordx4 v11, s[80:81]\n" \
  "s_waitcnt vmcnt(4)\n" \
  "s_add_u32 s80, s80, 27648\n" \
  "s_addc_u32 s81, s81, 0\n" \
  "s_add_u32 m0, s72, 13312\n" \
  "s_nop 0\n" \
  "global_load_lds_dwordx4 v12, s[80:81]\n" \
  "s_waitcnt vmcnt(4)\n" \
  "s_add_u32 s80, s80, 27648\n" \
  "s_addc_u32 s81, s81, 0\n" \
  "s_add_u32 m0, s72, 14336\n" \
  "s_nop 0\n" \
  "global_load_lds_dwordx4 v11, s[80:81]\n" \
  "s_waitcnt vmcnt(4)\n" \
  "s_add_u32 s80, s80, 27648\n" \
  "s_addc_u32 s81, s81, 0\n" \
  "s_add_u32 m0, s72, 15360\n" \
  "s_nop 0\n" \
  "global_load_lds_dwordx4 v12, s[80:81]\n" \
  "s_waitcnt vmcnt(4)\n" \
  "s_add_u32 s80, s80, 27648\n" \
  "s_addc_u32 s81, s81, 0\n" \
  "s_add_u32 m0, s72, 16384\n" \
  "s_nop 0\n" \
  "global_load_lds_dwordx4 v11, s[80:81]\n" \
  "s_waitcnt vmcnt(4)\n" \
  "s_add_u32 s80, s80, 27648\n" \
  "s_addc_u32 s81, s81, 0\n" \
  "s_add_u32 m0, s72, 17408\n" \
  "s_nop 0\n" \
  "global_load_lds_dwordx4 v12, s[80:81]\n" \
  "s_waitcnt vmcnt(4)\n" \
  "s_add_u32 s80, s80, 27648\n" \
  "s_addc_u32 s81, s81, 0\n" \
  "s_add_u32 m0, s72, 18432\n" \
  "s_nop 0\n" \
  "global_load_lds_dwordx4 v11, s[80:81]\n" \
  "s_waitcnt vmcnt(4)\n" \
  "s_add_u32 s80, s80, 27648\n" \
  "s_addc_u32 s81, s81, 0\n" \
  "s_add_u32 m0, s72, 19456\n" \
  "s_nop 0\n" \
  "global_load_lds_dwordx4 v12, s[80:81]\n" \
  "s_waitcnt vmcnt(4)\n" \
  "s_add_u32 s80, s28, s52\n" \
  "s_addc_u32 s81, s29, 0\n" \
  "s_add_u32 m0, s72, 81920\n" \
  "s_nop 0\n" \
  "global_load_lds_dwordx4 v11, s[80:81]\n" \
  "s_waitcnt vmcnt(4)\n" \
  "s_add_u32 s80, s80, 27648\n" \
  "s_addc_u32 s81, s81, 0\n" \
  "s_add_u32 m0, s72, 82944\n" \
  "s_nop 0\n" \
  "global_load_lds_dwordx4 v12, s[80:81]\n" \
  "s_waitcnt vmcnt(4)\n" \
  "s_add_u32 s80, s80, 27648\n" \
  "s_addc_u32 s81, s81, 0\n" \
  "s_add_u32 m0, s72, 83968\n" \
  "s_nop 0\n" \
  "global_load_lds_dwordx4 v11, s[80:81]\n" \
  "s_waitcnt vmcnt(4)\n" \
  "s_add_u32 s80, s80, 27648\n" \
  "s_addc_u32 s81, s81, 0\n" \
  "s_add_u32 m0, s72, 84992\n" \
  "s_nop 0\n" \
  "global_load_lds_dwordx4 v12, s[80:81]\n" \
  "s_waitcnt vmcnt(4)\n" \
  "s_add_u32 s80, s80, 27648\n" \
  "s_addc_u32 s81, s81, 0\n" \
  "s_add_u32 m0, s72, 86016\n" \
  "s_nop 0\n" \
  "global_load_lds_dwordx4 v11, s[80:81]\n" \
  "s_waitcnt vmcnt(4)\n" \
  "s_add_u32 s80, s80, 27648\n" \
  "s_addc_u32 s81, s81, 0\n" \
  "s_add_u32 m0, s72, 87040\n" \
  "s_nop 0\n" \
  "global_load_lds_dwordx4 v12, s[80:81]\n" \
  "s_waitcnt vmcnt(4)\n" \
  "s_add_u32 s80, s80, 27648\n" \
  "s_addc_u32 s81, s81, 0\n" \
  "s_add_u32 m0, s72, 88064\n" \
  "s_nop 0\n" \
  "global_load_lds_dwordx4 v11, s[80:81]\n" \
  "s_waitcnt vmcnt(4)\n" \
  "s_add_u32 s80, s80, 27648\n" \
  "s_addc_u32 s81, s81, 0\n" \
  "s_add_u32 m0, s72, 89088\n" \
  "s_nop 0\n" \
  "global_load_lds_dwordx4 v12, s[80:81]\n" \
  "s_waitcnt vmcnt(4)\n" \
  "s_add_u32 s80, s80, 27648\n" \
  "s_addc_u32 s81, s81, 0\n" \
  "s_add_u32 m0, s72, 90112\n" \
  "s_nop 0\n" \
  "global_load_lds_dwordx4 v11, s[80:81]\n" \
  "s_waitcnt vmcnt(4)\n" \
  "s_add_u32 s80, s80, 27648\n" \
  "s_addc_u32 s81, s81, 0\n" \
  "s_add_u32 m0, s72, 91136\n" \
  "s_nop 0\n" \
  "global_load_lds_dwordx4 v12, s[80:81]\n" \
  "s_waitcnt vmcnt(4)\n" \
  "s_add_u32 s80, s80, 27648\n" \
  "s_addc_u32 s81, s81, 0\n" \
  "s_add_u32 m0, s72, 92160\n" \
  "s_nop 0\n" \
  "global_load_lds_dwordx4 v11, s[80:81]\n" \
  "s_waitcnt vmcnt(4)\n" \
  "s_add_u32 s80, s80, 27648\n" \
  "s_addc_u32 s81, s81, 0\n" \
  "s_add_u32 m0, s72, 93184\n" \
  "s_nop 0\n" \
  "global_load_lds_dwordx4 v12, s[80:81]\n" \
  "s_waitcnt vmcnt(4)\n" \
  "s_add_u32 s80, s80, 27648\n" \
  "s_addc_u32 s81, s81, 0\n" \
  "s_add_u32 m0, s72, 94208\n" \
  "s_nop 0\n" \
  "global_load_lds_dwordx4 v11, s[80:81]\n" \
  "s_waitcnt vmcnt(4)\n" \
  "s_add_u32 s80, s80, 27648\n" \
  "s_addc_u32 s81, s81, 0\n" \
  "s_add_u32 m0, s72, 95232\n" \
  "s_nop 0\n" \
  "global_load_lds_dwordx4 v12, s[80:81]\n" \
  "s_waitcnt vmcnt(4)\n" \
  "s_add_u32 s80, s80, 27648\n" \
  "s_addc_u32 s81, s81, 0\n" \
  "s_add_u32 m0, s72, 96256\n" \
  "s_nop 0\n" \
  "global_load_lds_dwordx4 v11, s[80:81]\n" \
  "s_waitcnt vmcnt(4)\n" \
  "s_add_u32 s80, s80, 27648\n" \
  "s_addc_u32 s81, s81, 0\n" \
  "s_add_u32 m0, s72, 97280\n" \
  "s_nop 0\n" \
  "global_load_lds_dwordx4 v12, s[80:81]\n" \
  "s_waitcnt vmcnt(4)\n" \
  "s_add_u32 s80, s80, 27648\n" \
  "s_addc_u32 s81, s81, 0\n" \
  "s_add_u32 m0, s72, 98304\n" \
  "s_nop 0\n" \
  "global_load_lds_dwordx4 v11, s[80:81]\n" \
  "s_waitcnt vmcnt(4)\n" \
  "s_add_u32 s80, s80, 27648\n" \
  "s_addc_u32 s81, s81, 0\n" \
  "s_add_u32 m0, s72, 99328\n" \
  "s_nop 0\n" \
  "global_load_lds_dwordx4 v12, s[80:81]\n" \
  "s_waitcnt vmcnt(4)\n" \
  "s_add_u32 s80, s80, 27648\n" \
  "s_addc_u32 s81, s81, 0\n" \
  "s_add_u32 m0, s72, 100352\n" \
  "s_nop 0\n" \
  "global_load_lds_dwordx4 v11, s[80:81]\n" \
  "s_waitcnt vmcnt(4)\n" \
  "s_add_u32 s80, s80, 27648\n" \
  "s_addc_u32 s81, s81, 0\n" \
  "s_add_u32 m0, s72, 101376\n" \
  "s_nop 0\n" \
  "global_load_lds_dwordx4 v12, s[80:81]\n" \
  "s_waitcnt vmcnt(4)\n" \
  "s_lshl_b32 s52, s50, 1\n" \
  "v_bfe_u32 v4, v0, 2, 2\n" \
  "v_lshl_add_u32 v11, v3, 3, v4\n" \
  "v_lshrrev_b32 v4, 1, v4\n" \
  "v_and_b32 v5, 1, v3\n" \
  "v_lshl_add_u32 v4, v5, 1, v4\n" \
  "v_xor_b32 v4, s52, v4\n" \
  "v_lshlrev_b32 v11, 7, v11\n" \
  "v_lshl_add_u32 v11, v4, 5, v11\n" \
  "v_and_b32 v4, 3, v0\n" \
  "v_lshl_add_u32 v11, v4, 3, v11\n" \
  "s_add_u32 s52, s52, 1\n" \
  "v_bfe_u32 v4, v0, 2, 2\n" \
  "v_lshl_add_u32 v12, v3, 3, v4\n" \
  "v_lshrrev_b32 v4, 1, v4\n" \
  "v_and_b32 v5, 1, v3\n" \
  "v_lshl_add_u32 v4, v5, 1, v4\n" \
  "v_xor_b32 v4, s52, v4\n" \
  "v_lshlrev_b32 v12, 7, v12\n" \
  "v_lshl_add_u32 v12, v4, 5, v12\n" \
  "v_and_b32 v4, 3, v0\n" \
  "v_lshl_add_u32 v12, v4, 3, v12\n" \
  "s_and_b32 s54, s49, 3\n" \
  "s_lshl_b32 s52, s54, 4\n" \
  "s_add_u32 s52, s52, s51\n" \
  "s_mul_i32 s60, s52, 108\n" \
  "s_lshl_b32 s53, s53, 1\n" \
  "s_add_u32 s60, s60, s53\n" \
  "s_add_u32 s60, s60, s50\n" \
  "s_lshl_b32 s60, s60, 10\n" \
  "s_add_u32 s60, s60, 24944640\n" \
  "s_mul_i32 s57, s54, 655360\n" \
  "s_add_u32 s57, s57, s72\n" \
  "s_add_u32 s57, s57, 14680064\n" \
  "s_add_u32 s76, s57, 573440\n" \
  "s_mul_i32 s58, s48, 10240\n" \
  "s_mov_b32 s56, 8\n" \
  "s_waitcnt vmcnt(0) lgkmcnt(0)\n" \
  "s_barrier\n" \
  "ds_read_b64_tr_b16 a[0:1], v11 offset:0\n" \
  "ds_read_b64_tr_b16 a[2:3], v11 offset:512\n" \
  "ds_read_b64_tr_b16 a[80:81], v12 offset:0\n" \
  "ds_read_b64_tr_b16 a[82:83], v12 offset:512\n" \
  "ds_read_b64_tr_b16 a[4:5], v11 offset:4096\n" \
  "ds_read_b64_tr_b16 a[6:7], v11 offset:4608\n" \
  "ds_read_b64_tr_b16 a[84:85], v12 offset:4096\n" \
  "ds_read_b64_tr_b16 a[86:87], v12 offset:4608\n" \
  "ds_read_b64_tr_b16 a[8:9], v11 offset:8192\n" \
  "ds_read_b64_tr_b16 a[10:11], v11 offset:8704\n" \
  "ds_read_b64_tr_b16 a[88:89], v12 offset:8192\n" \
  "ds_read_b64_tr_b16 a[90:91], v12 offset:8704\n" \
  "ds_read_b64_tr_b16 a[12:13], v11 offset:12288\n" \
  "ds_read_b64_tr_b16 a[14:15], v11 offset:12800\n" \
  "ds_read_b64_tr_b16 a[92:93], v12 offset:12288\n" \
  "ds_read_b64_tr_b16 a[94:95], v12 offset:12800\n" \
  "ds_read_b64_tr_b16 a[16:17], v11 offset:16384\n" \
  "ds_read_b64_tr_b16 a[18:19], v11 offset:16896\n" \
  "ds_read_b64_tr_b16 a[96:97], v12 offset:16384\n" \
  "ds_read_b64_tr_b16 a[98:99], v12 offset:16896\n" \
  "ds_read_b64_tr_b16 a[20:21], v11 offset:20480\n" \
  "ds_read_b64_tr_b16 a[22:23], v11 offset:20992\n" \
  "ds_read_b64_tr_b16 a[100:101], v12 offset:20480\n" \
  "ds_read_b64_tr_b16 a[102:103], v12 offset:20992\n" \
  "ds_read_b64_tr_b16 a[24:25], v11 offset:24576\n" \
  "ds_read_b64_tr_b16 a[26:27], v11 offset:25088\n" \
  "ds_read_b64_tr_b16 a[104:105], v12 offset:24576\n" \
  "ds_read_b64_tr_b16 a[106:107], v12 offset:25088\n" \
  "ds_read_b64_tr_b16 a[28:29], v11 offset:28672\n" \
  "ds_read_b64_tr_b16 a[30:31], v11 offset:29184\n" \
  "ds_read_b64_tr_b16 a[108:109], v12 offset:28672\n" \
  "ds_read_b64_tr_b16 a[110:111], v12 offset:29184\n" \
  "ds_read_b64_tr_b16 a[32:33], v11 offset:32768\n" \
  "ds_read_b64_tr_b16 a[34:35], v11 offset:33280\n" \
  "ds_read_b64_tr_b16 a[112:113], v12 offset:32768\n" \
  "ds_read_b64_tr_b16 a[114:115], v12 offset:33280\n" \
  "ds_read_b64_tr_b16 a[36:37], v11 offset:36864\n" \
  "ds_read_b64_tr_b16 a[38:39], v11 offset:37376\n" \
  "ds_read_b64_tr_b16 a[116:117], v12 offset:36864\n" \
  "ds_read_b64_tr_b16 a[118:119], v12 offset:37376\n" \
  "s_waitcnt lgkmcnt(0)\n" \
  "s_barrier\n" \
  "s_add_u32 m0, s58, 0\n" \
  "s_add_u32 s54, s57, 0\n" \
  "buffer_load_dwordx4 v1, s[36:39], s54 offen offset:0 sc1 lds\n" \
  "buffer_load_dwordx4 v1, s[36:39], s54 offen offset:1024 sc1 lds\n" \
  "buffer_load_dwordx4 v1, s[36:39], s54 offen offset:2048 sc1 lds\n" \
  "buffer_load_dwordx4 v1, s[36:39], s54 offen offset:3072 sc1 lds\n" \
  "s_add_u32 m0, s58, 4096\n" \
  "s_add_u32 s54, s57, 4096\n" \
  "buffer_load_dwordx4 v1, s[36:39], s54 offen offset:0 sc1 lds\n" \
  "buffer_load_dwordx4 v1, s[36:39], s54 offen offset:1024 sc1 lds\n" \
  "buffer_load_dwordx4 v1, s[36:39], s54 offen offset:2048 sc1 lds\n" \
  "buffer_load_dwordx4 v1, s[36:39], s54 offen offset:3072 sc1 lds\n" \
  "s_add_u32 m0, s58, 8192\n" \
  "s_add_u32 s54, s57, 8192\n" \
  "buffer_load_dwordx4 v1, s[36:39], s54 offen offset:0 sc1 lds\n" \
  "buffer_load_dwordx4 v1, s[36:39], s54 offen offset:1024 sc1 lds\n" \
  "s_add_u32 s57, s57, 10240\n" \
  "s_add_u32 s58, s58, 40960\n" \
  "s_sub_u32 s54, s58, 122880\n" \
  "s_cmp_ge_u32 s58, 122880\n" \
  "s_cselect_b32 s58, s54, s58\n" \
  "v_add_u32 v11, 40960, v11\n" \
  "v_add_u32 v12, 40960, v12\n" \
  "ds_read_b64_tr_b16 a[40:41], v11 offset:0\n" \
  "ds_read_b64_tr_b16 a[42:43], v11 offset:512\n" \
  "ds_read_b64_tr_b16 a[120:121], v12 offset:0\n" \
  "ds_read_b64_tr_b16 a[122:123], v12 offset:512\n" \
  "ds_read_b64_tr_b16 a[44:45], v11 offset:4096\n" \
  "ds_read_b64_tr_b16 a[46:47], v11 offset:4608\n" \
  "ds_read_b64_tr_b16 a[124:125], v12 offset:4096\n" \
  "ds_read_b64_tr_b16 a[126:127], v12 offset:4608\n" \
  "ds_read_b64_tr_b16 a[48:49], v11 offset:8192\n" \
  "ds_read_b64_tr_b16 a[50:51], v11 offset:8704\n" \
  "ds_read_b64_tr_b16 a[128:129], v12 offset:8192\n" \
  "ds_read_b64_tr_b16 a[130:131], v12 offset:8704\n" \
  "ds_read_b64_tr_b16 a[52:53], v11 offset:12288\n" \
  "ds_read_b64_tr_b16 a[54:55], v11 offset:12800\n" \
  "ds_read_b64_tr_b16 a[132:133], v12 offset:12288\n" \
  "ds_read_b64_tr_b16 a[134:135], v12 offset:12800\n" \
  "ds_read_b64_tr_b16 a[56:57], v11 offset:16384\n" \
  "ds_read_b64_tr_b16 a[58:59], v11 offset:16896\n" \
  "ds_read_b64_tr_b16 a[136:137], v12 offset:16384\n" \
  "ds_read_b64_tr_b16 a[138:139], v12 offset:16896\n" \
  "ds_read_b64_tr_b16 a[60:61], v11 offset:20480\n" \
  "ds_read_b64_tr_b16 a[62:63], v11 offset:20992\n" \
  "ds_read_b64_tr_b16 a[140:141], v12 offset:20480\n" \
  "ds_read_b64_tr_b16 a[142:143], v12 offset:20992\n" \
  "ds_read_b64_tr_b16 a[64:65], v11 offset:24576\n" \
  "ds_read_b64_tr_b16 a[66:67], v11 offset:25088\n" \
  "ds_read_b64_tr_b16 a[144:145], v12 offset:24576\n" \
  "ds_read_b64_tr_b16 a[146:147], v12 offset:25088\n" \
  "ds_read_b64_tr_b16 a[68:69], v11 offset:28672\n" \
  "ds_read_b64_tr_b16 a[70:71], v11 offset:29184\n" \
  "ds_read_b64_tr_b16 a[148:149], v12 offset:28672\n" \
  "ds_read_b64_tr_b16 a[150:151], v12 offset:29184\n" \
  "ds_read_b64_tr_b16 a[72:73], v11 offset:32768\n" \
  "ds_read_b64_tr_b16 a[74:75], v11 offset:33280\n" \
  "ds_read_b64_tr_b16 a[152:153], v12 offset:32768\n" \
  "ds_read_b64_tr_b16 a[154:155], v12 offset:33280\n" \
  "ds_read_b64_tr_b16 a[76:77], v11 offset:36864\n" \
  "ds_read_b64_tr_b16 a[78:79], v11 offset:37376\n" \
  "ds_read_b64_tr_b16 a[156:157], v12 offset:36864\n" \
  "ds_read_b64_tr_b16 a[158:159], v12 offset:37376\n" \
  "s_waitcnt lgkmcnt(0)\n" \
  "s_barrier\n" \
  "s_add_u32 m0, s58, 0\n" \
  "s_add_u32 s54, s57, 0\n" \
  "buffer_load_dwordx4 v1, s[36:39], s54 offen offset:0 sc1 lds\n" \
  "buffer_load_dwordx4 v1, s[36:39], s54 offen offset:1024 sc1 lds\n" \
  "buffer_load_dwordx4 v1, s[36:39], s54 offen offset:2048 sc1 lds\n" \
  "buffer_load_dwordx4 v1, s[36:39], s54 offen offset:3072 sc1 lds\n" \
  "s_add_u32 m0, s58, 4096\n" \
  "s_add_u32 s54, s57, 4096\n" \
  "buffer_load_dwordx4 v1, s[36:39], s54 offen offset:0 sc1 lds\n" \
  "buffer_load_dwordx4 v1, s[36:39], s54 offen offset:1024 sc1 lds\n" \
  "buffer_load_dwordx4 v1, s[36:39], s54 offen offset:2048 sc1 lds\n" \
  "buffer_load_dwordx4 v1, s[36:39], s54 offen offset:3072 sc1 lds\n" \
  "s_add_u32 m0, s58, 8192\n" \
  "s_add_u32 s54, s57, 8192\n" \
  "buffer_load_dwordx4 v1, s[36:39], s54 offen offset:0 sc1 lds\n" \
  "buffer_load_dwordx4 v1, s[36:39], s54 offen offset:1024 sc1 lds\n" \
  "s_add_u32 s57, s57, 71680\n" \
  "s_min_u32 s57, s57, s76\n" \
  "s_add_u32 s58, s58, 40960\n" \
  "s_sub_u32 s54, s58, 122880\n" \
  "s_cmp_ge_u32 s58, 122880\n" \
  "s_cselect_b32 s58, s54, s58\n" \
  "v_add_u32 v11, 40960, v11\n" \
  "v_add_u32 v12, 40960, v12\n" \
  "ds_read_b64_tr_b16 a[160:161], v11 offset:0\n" \
  "ds_read_b64_tr_b16 a[162:163], v11 offset:512\n" \
  "ds_read_b64_tr_b16 a[240:241], v12 offset:0\n" \
  "ds_read_b64_tr_b16 a[242:243], v12 offset:512\n" \
  "ds_read_b64_tr_b16 a[164:165], v11 offset:4096\n" \
  "ds_read_b64_tr_b16 a[166:167], v11 offset:4608\n" \
  "ds_read_b64_tr_b16 a[244:245], v12 offset:4096\n" \
  "ds_read_b64_tr_b16 a[246:247], v12 offset:4608\n" \
  "ds_read_b64_tr_b16 a[168:169], v11 offset:8192\n" \
  "ds_read_b64_tr_b16 a[170:171], v11 offset:8704\n" \
  "ds_read_b64_tr_b16 a[248:249], v12 offset:8192\n" \
  "ds_read_b64_tr_b16 a[250:251], v12 offset:8704\n" \
  "ds_read_b64_tr_b16 a[172:173], v11 offset:12288\n" \
  "ds_read_b64_tr_b16 a[174:175], v11 offset:12800\n" \
  "ds_read_b64_tr_b16 a[252:253], v12 offset:12288\n" \
  "ds_read_b64_tr_b16 a[254:255], v12 offset:12800\n" \
  "ds_read_b64_tr_b16 a[176:177], v11 offset:16384\n" \
  "ds_read_b64_tr_b16 a[178:179], v11 offset:16896\n" \
  "ds_read_b64_tr_b16 v[192:193], v12 offset:16384\n" \
  "ds_read_b64_tr_b16 v[194:195], v12 offset:16896\n" \
  "ds_read_b64_tr_b16 a[180:181], v11 offset:20480\n" \
  "ds_read_b64_tr_b16 a[182:183], v11 offset:20992\n" \
  "ds_read_b64_tr_b16 v[196:197], v12 offset:20480\n" \
  "ds_read_b64_tr_b16 v[198:199], v12 offset:20992\n" \
  "ds_read_b64_tr_b16 a[184:185], v11 offset:24576\n" \
  "ds_read_b64_tr_b16 a[186:187], v11 offset:25088\n" \
  "ds_read_b64_tr_b16 v[200:201], v12 offset:24576\n" \
  "ds_read_b64_tr_b16 v[202:203], v12 offset:25088\n" \
  "ds_read_b64_tr_b16 a[188:189], v11 offset:28672\n" \
  "ds_read_b64_tr_b16 a[190:191], v11 offset:29184\n" \
  "ds_read_b64_tr_b16 v[204:205], v12 offset:28672\n" \
  "ds_read_b64_tr_b16 v[206:207], v12 offset:29184\n" \
  "ds_read_b64_tr_b16 a[192:193], v11 offset:32768\n" \
  "ds_read_b64_tr_b16 a[194:195], v11 offset:33280\n" \
  "ds_read_b64_tr_b16 v[208:209], v12 offset:32768\n" \
  "ds_read_b64_tr_b16 v[210:211], v12 offset:33280\n" \
  "ds_read_b64_tr_b16 a[196:197], v11 offset:36864\n" \
  "ds_read_b64_tr_b16 a[198:199], v11 offset:37376\n" \
  "ds_read_b64_tr_b16 v[212:213], v12 offset:36864\n" \
  "ds_read_b64_tr_b16 v[214:215], v12 offset:37376\n" \
  "v_add_u32 v11, 40960, v11\n" \
  "v_add_u32 v12, 40960, v12\n" \
  "ds_read_b64_tr_b16 a[200:201], v11 offset:0\n" \
  "ds_read_b64_tr_b16 a[202:203], v11 offset:512\n" \
  "ds_read_b64_tr_b16 v[216:217], v12 offset:0\n" \
  "ds_read_b64_tr_b16 v[218:219], v12 offset:512\n" \
  "ds_read_b64_tr_b16 a[204:205], v11 offset:4096\n" \
  "ds_read_b64_tr_b16 a[206:207], v11 offset:4608\n" \
  "ds_read_b64_tr_b16 v[220:221], v12 offset:4096\n" \
  "ds_read_b64_tr_b16 v[222:223], v12 offset:4608\n" \
  "ds_read_b64_tr_b16 a[208:209], v11 offset:8192\n" \
  "ds_read_b64_tr_b16 a[210:211], v11 offset:8704\n" \
  "ds_read_b64_tr_b16 v[224:225], v12 offset:8192\n" \
  "ds_read_b64_tr_b16 v[226:227], v12 offset:8704\n" \
  "ds_read_b64_tr_b16 a[212:213], v11 offset:12288\n" \
  "ds_read_b64_tr_b16 a[214:215], v11 offset:12800\n" \
  "ds_read_b64_tr_b16 v[228:229], v12 offset:12288\n" \
  "ds_read_b64_tr_b16 v[230:231], v12 offset:12800\n" \
  "ds_read_b64_tr_b16 a[216:217], v11 offset:16384\n" \
  "ds_read_b64_tr_b16 a[218:219], v11 offset:16896\n" \
  "ds_read_b64_tr_b16 v[232:233], v12 offset:16384\n" \
  "ds_read_b64_tr_b16 v[234:235], v12 offset:16896\n" \
  "ds_read_b64_tr_b16 a[220:221], v11 offset:20480\n" \
  "ds_read_b64_tr_b16 a[222:223], v11 offset:20992\n" \
  "ds_read_b64_tr_b16 v[236:237], v12 offset:20480\n" \
  "ds_read_b64_tr_b16 v[238:239], v12 offset:20992\n" \
  "ds_read_b64_tr_b16 a[224:225], v11 offset:24576\n" \
  "ds_read_b64_tr_b16 a[226:227], v11 offset:25088\n" \
  "ds_read_b64_tr_b16 v[240:241], v12 offset:24576\n" \
  "ds_read_b64_tr_b16 v[242:243], v12 offset:25088\n" \
  "ds_read_b64_tr_b16 a[228:229], v11 offset:28672\n" \
  "ds_read_b64_tr_b16 a[230:231], v11 offset:29184\n" \
  "ds_read_b64_tr_b16 v[244:245], v12 offset:28672\n" \
  "ds_read_b64_tr_b16 v[246:247], v12 offset:29184\n" \
  "ds_read_b64_tr_b16 a[232:233], v11 offset:32768\n" \
  "ds_read_b64_tr_b16 a[234:235], v11 offset:33280\n" \
  "ds_read_b64_tr_b16 v[248:249], v12 offset:32768\n" \
  "ds_read_b64_tr_b16 v[250:251], v12 offset:33280\n" \
  "ds_read_b64_tr_b16 a[236:237], v11 offset:36864\n" \
  "ds_read_b64_tr_b16 a[238:239], v11 offset:37376\n" \
  "ds_read_b64_tr_b16 v[252:253], v12 offset:36864\n" \
  "ds_read_b64_tr_b16 v[254:255], v12 offset:37376\n" \
  "s_waitcnt vmcnt(10)\n" \
  "s_waitcnt lgkmcnt(0)\n" \
  "s_barrier\n" \
  "s_mul_i32 s52, s51, 20480\n" \
  "v_add_u32 v8, s52, v1\n" \
  "v_mov_b32 v9, v8\n" \
  "s_mov_b32 s59, 40960\n" \
  "s_mov_b32 s55, 0\n" \
  "ds_read_b128 v[16:19], v9 offset:0\n" \
  "ds_read_b128 v[20:23], v9 offset:10240\n" \
  "ds_read_b128 v[24:27], v9 offset:1024\n" \
  "v_mov_b32 v7, 0xf0000000\n" \
  "WBLP_%=:\n" \
  "v_add_u32 v10, s59, v8\n" \
  "s_add_u32 s59, s59, 40960\n" \
  "s_cmp_ge_u32 s59, 122880\n" \
  "s_cselect_b32 s59, 0, s59\n" \
  "s_waitcnt lgkmcnt(2)\n" \
  "v_mfma_f32_16x16x32_bf16 v[40:43], a[0:3], v[16:19], 0\n" \
  "ds_read_b128 v[28:31], v9 offset:11264\n" \
  "v_mfma_f32_16x16x32_bf16 v[44:47], a[80:83], v[16:19], 0\n" \
  "v_mfma_f32_16x16x32_bf16 v[48:51], a[160:163], v[16:19], 0\n" \
  "v_mfma_f32_16x16x32_bf16 v[52:55], a[240:243], v[16:19], 0\n" \
  "s_waitcnt lgkmcnt(2)\n" \
  "v_mfma_f32_16x16x32_bf16 v[56:59], a[0:3], v[20:23], 0\n" \
  "ds_read_b128 v[16:19], v9 offset:2048\n" \
  "v_mfma_f32_16x16x32_bf16 v[60:63], a[80:83], v[20:23], 0\n" \
  "s_add_u32 m0, s58, 0\n" \
  "s_add_u32 s54, s57, 0\n" \
  "buffer_load_dwordx4 v1, s[36:39], s54 offen offset:0 sc1 lds\n" \
  "v_mfma_f32_16x16x32_bf16 v[64:67], a[160:163], v[20:23], 0\n" \
  "v_mfma_f32_16x16x32_bf16 v[68:71], a[240:243], v[20:23], 0\n" \
  "s_waitcnt lgkmcnt(2)\n" \
  "v_mfma_f32_16x16x32_bf16 v[40:43], a[4:7], v[24:27], v[40:43]\n" \
  "ds_read_b128 v[20:23], v9 offset:12288\n" \
  "v_mul_f32 v112, 0xbfb8aa3b, v72\n" \
  "v_mfma_f32_16x16x32_bf16 v[44:47], a[84:87], v[24:27], v[44:47]\n" \
  "v_mul_f32 v113, 0xbfb8aa3b, v73\n" \
  "buffer_load_dwordx4 v1, s[36:39], s54 offen offset:1024 sc1 lds\n" \
  "v_mfma_f32_16x16x32_bf16 v[48:51], a[164:167], v[24:27], v[48:51]\n" \
  "v_mul_f32 v114, 0xbfb8aa3b, v74\n" \
  "v_mfma_f32_16x16x32_bf16 v[52:55], a[244:247], v[24:27], v[52:55]\n" \
  "v_mul_f32 v115, 0xbfb8aa3b, v75\n" \
  "s_waitcnt lgkmcnt(2)\n" \
  "v_mfma_f32_16x16x32_bf16 v[56:59], a[4:7], v[28:31], v[56:59]\n" \
  "ds_read_b128 v[24:27], v9 offset:3072\n" \
  "v_exp_f32 v112, v112\n" \
  "v_mfma_f32_16x16x32_bf16 v[60:63], a[84:87], v[28:31], v[60:63]\n" \
  "v_exp_f32 v113, v113\n" \
  "v_mfma_f32_16x16x32_bf16 v[64:67], a[164:167], v[28:31], v[64:67]\n" \
  "v_exp_f32 v114, v114\n" \
  "v_mfma_f32_16x16x32_bf16 v[68:71], a[244:247], v[28:31], v[68:71]\n" \
  "v_exp_f32 v115, v115\n" \
  "s_waitcnt lgkmcnt(2)\n" \
  "v_mfma_f32_16x16x32_bf16 v[40:43], a[8:11], v[16:19], v[40:43]\n" \
  "ds_read_b128 v[28:31], v9 offset:13312\n" \
  "v_add_f32 v112, 1.0, v112\n" \
  "v_mfma_f32_16x16x32_bf16 v[44:47], a[88:91], v[16:19], v[44:47]\n" \
  "v_add_f32 v113, 1.0, v113\n" \
  "buffer_load_dwordx4 v1, s[36:39], s54 offen offset:2048 sc1 lds\n" \
  "v_mfma_f32_16x16x32_bf16 v[48:51], a[168:171], v[16:19], v[48:51]\n" \
  "v_add_f32 v114, 1.0, v114\n" \
  "v_mfma_f32_16x16x32_bf16 v[52:55], a[248:251], v[16:19], v[52:55]\n" \
  "v_add_f32 v115, 1.0, v115\n" \
  "s_waitcnt lgkmcnt(2)\n" \
  "v_mfma_f32_16x16x32_bf16 v[56:59], a[8:11], v[20:23], v[56:59]\n" \
  "ds_read_b128 v[16:19], v9 offset:4096\n" \
  "v_rcp_f32 v112, v112\n" \
  "v_mfma_f32_16x16x32_bf16 v[60:63], a[88:91], v[20:23], v[60:63]\n" \
  "v_rcp_f32 v113, v113\n" \
  "buffer_load_dwordx4 v1, s[36:39], s54 offen offset:3072 sc1 lds\n" \
  "v_mfma_f32_16x16x32_bf16 v[64:67], a[168:171], v[20:23], v[64:67]\n" \
  "v_rcp_f32 v114, v114\n" \
  "v_mfma_f32_16x16x32_bf16 v[68:71], a[248:251], v[20:23], v[68:71]\n" \
  "v_rcp_f32 v115, v115\n" \
  "s_waitcnt lgkmcnt(2)\n" \
  "v_mfma_f32_16x16x32_bf16 v[40:43], a[12:15], v[24:27], v[40:43]\n" \
  "ds_read_b128 v[20:23], v9 offset:14336\n" \
  "v_mul_f32 v72, v72, v112\n" \
  "v_mfma_f32_16x16x32_bf16 v[44:47], a[92:95], v[24:27], v[44:47]\n" \
  "v_mul_f32 v73, v73, v113\n" \
  "v_mfma_f32_16x16x32_bf16 v[48:51], a[172:175], v[24:27], v[48:51]\n" \
  "v_mul_f32 v74, v74, v114\n" \
  "v_mfma_f32_16x16x32_bf16 v[52:55], a[252:255], v[24:27], v[52:55]\n" \
  "v_mul_f32 v75, v75, v115\n" \
  "s_waitcnt lgkmcnt(2)\n" \
  "v_mfma_f32_16x16x32_bf16 v[56:59], a[12:15], v[28:31], v[56:59]\n" \
  "ds_read_b128 v[24:27], v9 offset:5120\n" \
  "v_mul_f32 v72, v72, v80\n" \
  "v_mfma_f32_16x16x32_bf16 v[60:63], a[92:95], v[28:31], v[60:63]\n" \
  "v_mul_f32 v73, v73, v81\n" \
  "s_add_u32 m0, s58, 4096\n" \
  "s_add_u32 s54, s57, 4096\n" \
  "buffer_load_dwordx4 v1, s[36:39], s54 offen offset:0 sc1 lds\n" \
  "v_mfma_f32_16x16x32_bf16 v[64:67], a[172:175], v[28:31], v[64:67]\n" \
  "v_mul_f32 v74, v74, v82\n" \
  "v_mfma_f32_16x16x32_bf16 v[68:71], a[252:255], v[28:31], v[68:71]\n" \
  "v_mul_f32 v75, v75, v83\n" \
  "s_waitcnt lgkmcnt(2)\n" \
  "v_mfma_f32_16x16x32_bf16 v[40:43], a[16:19], v[16:19], v[40:43]\n" \
  "ds_read_b128 v[28:31], v9 offset:15360\n" \
  "v_cvt_pk_bf16_f32 v104, v72, v73\n" \
  "v_mfma_f32_16x16x32_bf16 v[44:47], a[96:99], v[16:19], v[44:47]\n" \
  "v_cvt_pk_bf16_f32 v105, v74, v75\n" \
  "buffer_load_dwordx4 v1, s[36:39], s54 offen offset:1024 sc1 lds\n" \
  "v_mfma_f32_16x16x32_bf16 v[48:51], a[176:179], v[16:19], v[48:51]\n" \
  "v_mul_f32 v112, 0xbfb8aa3b, v76\n" \
  "v_mfma_f32_16x16x32_bf16 v[52:55], v[192:195], v[16:19], v[52:55]\n" \
  "v_mul_f32 v113, 0xbfb8aa3b, v77\n" \
  "s_waitcnt lgkmcnt(2)\n" \
  "v_mfma_f32_16x16x32_bf16 v[56:59], a[16:19], v[20:23], v[56:59]\n" \
  "ds_read_b128 v[16:19], v9 offset:6144\n" \
  "v_mul_f32 v114, 0xbfb8aa3b, v78\n" \
  "v_mfma_f32_16x16x32_bf16 v[60:63], a[96:99], v[20:23], v[60:63]\n" \
  "v_mul_f32 v115, 0xbfb8aa3b, v79\n" \
  "v_mfma_f32_16x16x32_bf16 v[64:67], a[176:179], v[20:23], v[64:67]\n" \
  "v_exp_f32 v112, v112\n" \
  "v_mfma_f32_16x16x32_bf16 v[68:71], v[192:195], v[20:23], v[68:71]\n" \
  "v_exp_f32 v113, v113\n" \
  "s_waitcnt lgkmcnt(2)\n" \
  "v_mfma_f32_16x16x32_bf16 v[40:43], a[20:23], v[24:27], v[40:43]\n" \
  "ds_read_b128 v[20:23], v9 offset:16384\n" \
  "v_exp_f32 v114, v114\n" \
  "v_mfma_f32_16x16x32_bf16 v[44:47], a[100:103], v[24:27], v[44:47]\n" \
  "v_exp_f32 v115, v115\n" \
  "buffer_load_dwordx4 v1, s[36:39], s54 offen offset:2048 sc1 lds\n" \
  "v_mfma_f32_16x16x32_bf16 v[48:51], a[180:183], v[24:27], v[48:51]\n" \
  "v_add_f32 v112, 1.0, v112\n" \
  "v_mfma_f32_16x16x32_bf16 v[52:55], v[196:199], v[24:27], v[52:55]\n" \
  "v_add_f32 v113, 1.0, v113\n" \
  "s_waitcnt lgkmcnt(2)\n" \
  "v_mfma_f32_16x16x32_bf16 v[56:59], a[20:23], v[28:31], v[56:59]\n" \
  "ds_read_b128 v[24:27], v9 offset:7168\n" \
  "v_add_f32 v114, 1.0, v114\n" \
  "v_mfma_f32_16x16x32_bf16 v[60:63], a[100:103], v[28:31], v[60:63]\n" \
  "v_add_f32 v115, 1.0, v115\n" \
  "buffer_load_dwordx4 v1, s[36:39], s54 offen offset:3072 sc1 lds\n" \
  "v_mfma_f32_16x16x32_bf16 v[64:67], a[180:183], v[28:31], v[64:67]\n" \
  "v_rcp_f32 v112, v112\n" \
  "v_mfma_f32_16x16x32_bf16 v[68:71], v[196:199], v[28:31], v[68:71]\n" \
  "v_rcp_f32 v113, v113\n" \
  "s_waitcnt lgkmcnt(2)\n" \
  "v_mfma_f32_16x16x32_bf16 v[40:43], a[24:27], v[16:19], v[40:43]\n" \
  "ds_read_b128 v[28:31], v9 offset:17408\n" \
  "v_rcp_f32 v114, v114\n" \
  "v_mfma_f32_16x16x32_bf16 v[44:47], a[104:107], v[16:19], v[44:47]\n" \
  "v_rcp_f32 v115, v115\n" \
  "v_mfma_f32_16x16x32_bf16 v[48:51], a[184:187], v[16:19], v[48:51]\n" \
  "v_mul_f32 v76, v76, v112\n" \
  "v_mfma_f32_16x16x32_bf16 v[52:55], v[200:203], v[16:19], v[52:55]\n" \
  "v_mul_f32 v77, v77, v113\n" \
  "s_waitcnt lgkmcnt(2)\n" \
  "v_mfma_f32_16x16x32_bf16 v[56:59], a[24:27], v[20:23], v[56:59]\n" \
  "ds_read_b128 v[16:19], v9 offset:8192\n" \
  "v_mul_f32 v78, v78, v114\n" \
  "v_mfma_f32_16x16x32_bf16 v[60:63], a[104:107], v[20:23], v[60:63]\n" \
  "v_mul_f32 v79, v79, v115\n" \
  "s_add_u32 m0, s58, 8192\n" \
  "s_add_u32 s54, s57, 8192\n" \
  "buffer_load_dwordx4 v1, s[36:39], s54 offen offset:0 sc1 lds\n" \
  "v_mfma_f32_16x16x32_bf16 v[64:67], a[184:187], v[20:23], v[64:67]\n" \
  "v_mul_f32 v76, v76, v84\n" \
  "v_mfma_f32_16x16x32_bf16 v[68:71], v[200:203], v[20:23], v[68:71]\n" \
  "v_mul_f32 v77, v77, v85\n" \
  "s_waitcnt lgkmcnt(2)\n" \
  "v_mfma_f32_16x16x32_bf16 v[40:43], a[28:31], v[24:27], v[40:43]\n" \
  "ds_read_b128 v[20:23], v9 offset:18432\n" \
  "v_mul_f32 v78, v78, v86\n" \
  "v_mfma_f32_16x16x32_bf16 v[44:47], a[108:111], v[24:27], v[44:47]\n" \
  "v_mul_f32 v79, v79, v87\n" \
  "buffer_load_dwordx4 v1, s[36:39], s54 offen offset:1024 sc1 lds\n" \
  "v_mfma_f32_16x16x32_bf16 v[48:51], a[188:191], v[24:27], v[48:51]\n" \
  "v_cvt_pk_bf16_f32 v106, v76, v77\n" \
  "v_mfma_f32_16x16x32_bf16 v[52:55], v[204:207], v[24:27], v[52:55]\n" \
  "v_cvt_pk_bf16_f32 v107, v78, v79\n" \
  "s_waitcnt lgkmcnt(2)\n" \
  "v_mfma_f32_16x16x32_bf16 v[56:59], a[28:31], v[28:31], v[56:59]\n" \
  "ds_read_b128 v[24:27], v9 offset:9216\n" \
  "s_add_u32 s52, s60, 0\n" \
  "buffer_store_dwordx4 v[104:107], v7, s[36:39], s52 offen offset:0 sc1\n" \
  "v_mfma_f32_16x16x32_bf16 v[60:63], a[108:111], v[28:31], v[60:63]\n" \
  "v_mul_f32 v112, 0xbfb8aa3b, v88\n" \
  "v_mfma_f32_16x16x32_bf16 v[64:67], a[188:191], v[28:31], v[64:67]\n" \
  "v_mul_f32 v113, 0xbfb8aa3b, v89\n" \
  "v_mfma_f32_16x16x32_bf16 v[68:71], v[204:207], v[28:31], v[68:71]\n" \
  "v_mul_f32 v114, 0xbfb8aa3b, v90\n" \
  "s_waitcnt lgkmcnt(2)\n" \
  "v_mfma_f32_16x16x32_bf16 v[40:43], a[32:35], v[16:19], v[40:43]\n" \
  "ds_read_b128 v[28:31], v9 offset:19456\n" \
  "v_mul_f32 v115, 0xbfb8aa3b, v91\n" \
  "v_mfma_f32_16x16x32_bf16 v[44:47], a[112:115], v[16:19], v[44:47]\n" \
  "v_exp_f32 v112, v112\n" \
  "v_mfma_f32_16x16x32_bf16 v[48:51], a[192:195], v[16:19], v[48:51]\n" \
  "v_exp_f32 v113, v113\n" \
  "v_mfma_f32_16x16x32_bf16 v[52:55], v[208:211], v[16:19], v[52:55]\n" \
  "v_exp_f32 v114, v114\n" \
  "s_waitcnt lgkmcnt(0)\n" \
  "s_waitcnt vmcnt(10)\n" \
  "s_barrier\n" \
  "v_mfma_f32_16x16x32_bf16 v[56:59], a[32:35], v[20:23], v[56:59]\n" \
  "ds_read_b128 v[16:19], v10 offset:0\n" \
  "v_mfma_f32_16x16x32_bf16 v[60:63], a[112:115], v[20:23], v[60:63]\n" \
  "v_mfma_f32_16x16x32_bf16 v[64:67], a[192:195], v[20:23], v[64:67]\n" \
  "v_mfma_f32_16x16x32_bf16 v[68:71], v[208:211], v[20:23], v[68:71]\n" \
  "v_mfma_f32_16x16x32_bf16 v[40:43], a[36:39], v[24:27], v[40:43]\n" \
  "ds_read_b128 v[20:23], v10 offset:10240\n" \
  "v_mfma_f32_16x16x32_bf16 v[44:47], a[116:119], v[24:27], v[44:47]\n" \
  "v_mfma_f32_16x16x32_bf16 v[48:51], a[196:199], v[24:27], v[48:51]\n" \
  "v_mfma_f32_16x16x32_bf16 v[52:55], v[212:215], v[24:27], v[52:55]\n" \
  "v_mfma_f32_16x16x32_bf16 v[56:59], a[36:39], v[28:31], v[56:59]\n" \
  "ds_read_b128 v[24:27], v10 offset:1024\n" \
  "v_mfma_f32_16x16x32_bf16 v[60:63], a[116:119], v[28:31], v[60:63]\n" \
  "v_mfma_f32_16x16x32_bf16 v[64:67], a[196:199], v[28:31], v[64:67]\n" \
  "v_mfma_f32_16x16x32_bf16 v[68:71], v[212:215], v[28:31], v[68:71]\n" \
  "s_add_u32 s57, s57, 10240\n" \
  "s_add_u32 s58, s58, 40960\n" \
  "s_sub_u32 s54, s58, 122880\n" \
  "s_cmp_ge_u32 s58, 122880\n" \
  "s_cselect_b32 s58, s54, s58\n" \
  "v_mov_b32 v9, v10\n" \
  "v_add_u32 v10, s59, v8\n" \
  "s_add_u32 s59, s59, 40960\n" \
  "s_cmp_ge_u32 s59, 122880\n" \
  "s_cselect_b32 s59, 0, s59\n" \
  "s_waitcnt lgkmcnt(2)\n" \
  "v_mfma_f32_16x16x32_bf16 v[40:43], a[40:43], v[16:19], v[40:43]\n" \
  "ds_read_b128 v[28:31], v9 offset:11264\n" \
  "v_exp_f32 v115, v115\n" \
  "v_mfma_f32_16x16x32_bf16 v[44:47], a[120:123], v[16:19], v[44:47]\n" \
  "v_add_f32 v112, 1.0, v112\n" \
  "v_mfma_f32_16x16x32_bf16 v[48:51], a[200:203], v[16:19], v[48:51]\n" \
  "v_add_f32 v113, 1.0, v113\n" \
  "v_mfma_f32_16x16x32_bf16 v[52:55], v[216:219], v[16:19], v[52:55]\n" \
  "v_add_f32 v114, 1.0, v114\n" \
  "s_waitcnt lgkmcnt(2)\n" \
  "v_mfma_f32_16x16x32_bf16 v[56:59], a[40:43], v[20:23], v[56:59]\n" \
  "ds_read_b128 v[16:19], v9 offset:2048\n" \
  "v_add_f32 v115, 1.0, v115\n" \
  "v_mfma_f32_16x16x32_bf16 v[60:63], a[120:123], v[20:23], v[60:63]\n" \
  "v_rcp_f32 v112, v112\n" \
  "s_add_u32 m0, s58, 0\n" \
  "s_add_u32 s54, s57, 0\n" \
  "buffer_load_dwordx4 v1, s[36:39], s54 offen offset:0 sc1 lds\n" \
  "v_mfma_f32_16x16x32_bf16 v[64:67], a[200:203], v[20:23], v[64:67]\n" \
  "v_rcp_f32 v113, v113\n" \
  "v_mfma_f32_16x16x32_bf16 v[68:71], v[216:219], v[20:23], v[68:71]\n" \
  "v_rcp_f32 v114, v114\n" \
  "s_waitcnt lgkmcnt(2)\n" \
  "v_mfma_f32_16x16x32_bf16 v[40:43], a[44:47], v[24:27], v[40:43]\n" \
  "ds_read_b128 v[20:23], v9 offset:12288\n" \
  "v_rcp_f32 v115, v115\n" \
  "v_mfma_f32_16x16x32_bf16 v[44:47], a[124:127], v[24:27], v[44:47]\n" \
  "v_mul_f32 v88, v88, v112\n" \
  "buffer_load_dwordx4 v1, s[36:39], s54 offen offset:1024 sc1 lds\n" \
  "v_mfma_f32_16x16x32_bf16 v[48:51], a[204:207], v[24:27], v[48:51]\n" \
  "v_mul_f32 v89, v89, v113\n" \
  "v_mfma_f32_16x16x32_bf16 v[52:55], v[220:223], v[24:27], v[52:55]\n" \
  "v_mul_f32 v90, v90, v114\n" \
  "s_waitcnt lgkmcnt(2)\n" \
  "v_mfma_f32_16x16x32_bf16 v[56:59], a[44:47], v[28:31], v[56:59]\n" \
  "ds_read_b128 v[24:27], v9 offset:3072\n" \
  "v_mul_f32 v91, v91, v115\n" \
  "v_mfma_f32_16x16x32_bf16 v[60:63], a[124:127], v[28:31], v[60:63]\n" \
  "v_mul_f32 v88, v88, v96\n" \
  "v_mfma_f32_16x16x32_bf16 v[64:67], a[204:207], v[28:31], v[64:67]\n" \
  "v_mul_f32 v89, v89, v97\n" \
  "v_mfma_f32_16x16x32_bf16 v[68:71], v[220:223], v[28:31], v[68:71]\n" \
  "v_mul_f32 v90, v90, v98\n" \
  "s_waitcnt lgkmcnt(2)\n" \
  "v_mfma_f32_16x16x32_bf16 v[40:43], a[48:51], v[16:19], v[40:43]\n" \
  "ds_read_b128 v[28:31], v9 offset:13312\n" \
  "v_mul_f32 v91, v91, v99\n" \
  "v_mfma_f32_16x16x32_bf16 v[44:47], a[128:131], v[16:19], v[44:47]\n" \
  "v_cvt_pk_bf16_f32 v108, v88, v89\n" \
  "buffer_load_dwordx4 v1, s[36:39], s54 offen offset:2048 sc1 lds\n" \
  "v_mfma_f32_16x16x32_bf16 v[48:51], a[208:211], v[16:19], v[48:51]\n" \
  "v_cvt_pk_bf16_f32 v109, v90, v91\n" \
  "v_mfma_f32_16x16x32_bf16 v[52:55], v[224:227], v[16:19], v[52:55]\n" \
  "v_mul_f32 v112, 0xbfb8aa3b, v92\n" \
  "s_waitcnt lgkmcnt(2)\n" \
  "v_mfma_f32_16x16x32_bf16 v[56:59], a[48:51], v[20:23], v[56:59]\n" \
  "ds_read_b128 v[16:19], v9 offset:4096\n" \
  "v_mul_f32 v113, 0xbfb8aa3b, v93\n" \
  "v_mfma_f32_16x16x32_bf16 v[60:63], a[128:131], v[20:23], v[60:63]\n" \
  "v_mul_f32 v114, 0xbfb8aa3b, v94\n" \
  "buffer_load_dwordx4 v1, s[36:39], s54 offen offset:3072 sc1 lds\n" \
  "v_mfma_f32_16x16x32_bf16 v[64:67], a[208:211], v[20:23], v[64:67]\n" \
  "v_mul_f32 v115, 0xbfb8aa3b, v95\n" \
  "v_mfma_f32_16x16x32_bf16 v[68:71], v[224:227], v[20:23], v[68:71]\n" \
  "v_exp_f32 v112, v112\n" \
  "s_waitcnt lgkmcnt(2)\n" \
  "v_mfma_f32_16x16x32_bf16 v[40:43], a[52:55], v[24:27], v[40:43]\n" \
  "ds_read_b128 v[20:23], v9 offset:14336\n" \
  "v_exp_f32 v113, v113\n" \
  "v_mfma_f32_16x16x32_bf16 v[44:47], a[132:135], v[24:27], v[44:47]\n" \
  "v_exp_f32 v114, v114\n" \
  "v_mfma_f32_16x16x32_bf16 v[48:51], a[212:215], v[24:27], v[48:51]\n" \
  "v_exp_f32 v115, v115\n" \
  "v_mfma_f32_16x16x32_bf16 v[52:55], v[228:231], v[24:27], v[52:55]\n" \
  "v_add_f32 v112, 1.0, v112\n" \
  "s_waitcnt lgkmcnt(2)\n" \
  "v_mfma_f32_16x16x32_bf16 v[56:59], a[52:55], v[28:31], v[56:59]\n" \
  "ds_read_b128 v[24:27], v9 offset:5120\n" \
  "v_add_f32 v113, 1.0, v113\n" \
  "v_mfma_f32_16x16x32_bf16 v[60:63], a[132:135], v[28:31], v[60:63]\n" \
  "v_add_f32 v114, 1.0, v114\n" \
  "s_add_u32 m0, s58, 4096\n" \
  "s_add_u32 s54, s57, 4096\n" \
  "buffer_load_dwordx4 v1, s[36:39], s54 offen offset:0 sc1 lds\n" \
  "v_mfma_f32_16x16x32_bf16 v[64:67], a[212:215], v[28:31], v[64:67]\n" \
  "v_add_f32 v115, 1.0, v115\n" \
  "v_mfma_f32_16x16x32_bf16 v[68:71], v[228:231], v[28:31], v[68:71]\n" \
  "v_rcp_f32 v112, v112\n" \
  "s_waitcnt lgkmcnt(2)\n" \
  "v_mfma_f32_16x16x32_bf16 v[40:43], a[56:59], v[16:19], v[40:43]\n" \
  "ds_read_b128 v[28:31], v9 offset:15360\n" \
  "v_rcp_f32 v113, v113\n" \
  "v_mfma_f32_16x16x32_bf16 v[44:47], a[136:139], v[16:19], v[44:47]\n" \
  "v_rcp_f32 v114, v114\n" \
  "buffer_load_dwordx4 v1, s[36:39], s54 offen offset:1024 sc1 lds\n" \
  "v_mfma_f32_16x16x32_bf16 v[48:51], a[216:219], v[16:19], v[48:51]\n" \
  "v_rcp_f32 v115, v115\n" \
  "v_mfma_f32_16x16x32_bf16 v[52:55], v[232:235], v[16:19], v[52:55]\n" \
  "v_mul_f32 v92, v92, v112\n" \
  "s_waitcnt lgkmcnt(2)\n" \
  "v_mfma_f32_16x16x32_bf16 v[56:59], a[56:59], v[20:23], v[56:59]\n" \
  "ds_read_b128 v[16:19], v9 offset:6144\n" \
  "v_mul_f32 v93, v93, v113\n" \
  "v_mfma_f32_16x16x32_bf16 v[60:63], a[136:139], v[20:23], v[60:63]\n" \
  "v_mul_f32 v94, v94, v114\n" \
  "v_mfma_f32_16x16x32_bf16 v[64:67], a[216:219], v[20:23], v[64:67]\n" \
  "v_mul_f32 v95, v95, v115\n" \
  "v_mfma_f32_16x16x32_bf16 v[68:71], v[232:235], v[20:23], v[68:71]\n" \
  "v_mul_f32 v92, v92, v100\n" \
  "s_waitcnt lgkmcnt(2)\n" \
  "v_mfma_f32_16x16x32_bf16 v[40:43], a[60:63], v[24:27], v[40:43]\n" \
  "ds_read_b128 v[20:23], v9 offset:16384\n" \
  "v_mul_f32 v93, v93, v101\n" \
  "v_mfma_f32_16x16x32_bf16 v[44:47], a[140:143], v[24:27], v[44:47]\n" \
  "v_mul_f32 v94, v94, v102\n" \
  "buffer_load_dwordx4 v1, s[36:39], s54 offen offset:2048 sc1 lds\n" \
  "v_mfma_f32_16x16x32_bf16 v[48:51], a[220:223], v[24:27], v[48:51]\n" \
  "v_mul_f32 v95, v95, v103\n" \
  "v_mfma_f32_16x16x32_bf16 v[52:55], v[236:239], v[24:27], v[52:55]\n" \
  "v_cvt_pk_bf16_f32 v110, v92, v93\n" \
  "s_waitcnt lgkmcnt(2)\n" \
  "v_mfma_f32_16x16x32_bf16 v[56:59], a[60:63], v[28:31], v[56:59]\n" \
  "ds_read_b128 v[24:27], v9 offset:7168\n" \
  "v_cvt_pk_bf16_f32 v111, v94, v95\n" \
  "v_mfma_f32_16x16x32_bf16 v[60:63], a[140:143], v[28:31], v[60:63]\n" \
  "s_add_u32 s52, s60, 55296\n" \
  "buffer_store_dwordx4 v[108:111], v7, s[36:39], s52 offen offset:0 sc1\n" \
  "buffer_load_dwordx4 v1, s[36:39], s54 offen offset:3072 sc1 lds\n" \
  "v_mfma_f32_16x16x32_bf16 v[64:67], a[220:223], v[28:31], v[64:67]\n" \
  "s_add_u32 s60, s60, 221184\n" \
  "v_mfma_f32_16x16x32_bf16 v[68:71], v[236:239], v[28:31], v[68:71]\n" \
  "s_waitcnt lgkmcnt(2)\n" \
  "v_mfma_f32_16x16x32_bf16 v[40:43], a[64:67], v[16:19], v[40:43]\n" \
  "ds_read_b128 v[28:31], v9 offset:17408\n" \
  "v_mfma_f32_16x16x32_bf16 v[44:47], a[144:147], v[16:19], v[44:47]\n" \
  "v_mfma_f32_16x16x32_bf16 v[48:51], a[224:227], v[16:19], v[48:51]\n" \
  "v_mfma_f32_16x16x32_bf16 v[52:55], v[240:243], v[16:19], v[52:55]\n" \
  "s_waitcnt lgkmcnt(2)\n" \
  "v_mfma_f32_16x16x32_bf16 v[56:59], a[64:67], v[20:23], v[56:59]\n" \
  "ds_read_b128 v[16:19], v9 offset:8192\n" \
  "v_mfma_f32_16x16x32_bf16 v[60:63], a[144:147], v[20:23], v[60:63]\n" \
  "s_add_u32 m0, s58, 8192\n" \
  "s_add_u32 s54, s57, 8192\n" \
  "buffer_load_dwordx4 v1, s[36:39], s54 offen offset:0 sc1 lds\n" \
  "v_mfma_f32_16x16x32_bf16 v[64:67], a[224:227], v[20:23], v[64:67]\n" \
  "v_mfma_f32_16x16x32_bf16 v[68:71], v[240:243], v[20:23], v[68:71]\n" \
  "s_waitcnt lgkmcnt(2)\n" \
  "v_mfma_f32_16x16x32_bf16 v[40:43], a[68:71], v[24:27], v[40:43]\n" \
  "ds_read_b128 v[20:23], v9 offset:18432\n" \
  "v_mfma_f32_16x16x32_bf16 v[44:47], a[148:151], v[24:27], v[44:47]\n" \
  "buffer_load_dwordx4 v1, s[36:39], s54 offen offset:1024 sc1 lds\n" \
  "v_mfma_f32_16x16x32_bf16 v[48:51], a[228:231], v[24:27], v[48:51]\n" \
  "v_mfma_f32_16x16x32_bf16 v[52:55], v[244:247], v[24:27], v[52:55]\n" \
  "s_waitcnt lgkmcnt(2)\n" \
  "v_mfma_f32_16x16x32_bf16 v[56:59], a[68:71], v[28:31], v[56:59]\n" \
  "ds_read_b128 v[24:27], v9 offset:9216\n" \
  "v_mfma_f32_16x16x32_bf16 v[60:63], a[148:151], v[28:31], v[60:63]\n" \
  "v_mfma_f32_16x16x32_bf16 v[64:67], a[228:231], v[28:31], v[64:67]\n" \
  "v_mfma_f32_16x16x32_bf16 v[68:71], v[244:247], v[28:31], v[68:71]\n" \
  "s_waitcnt lgkmcnt(2)\n" \
  "v_mfma_f32_16x16x32_bf16 v[40:43], a[72:75], v[16:19], v[40:43]\n" \
  "ds_read_b128 v[28:31], v9 offset:19456\n" \
  "v_mfma_f32_16x16x32_bf16 v[44:47], a[152:155], v[16:19], v[44:47]\n" \
  "v_mfma_f32_16x16x32_bf16 v[48:51], a[232:235], v[16:19], v[48:51]\n" \
  "v_mfma_f32_16x16x32_bf16 v[52:55], v[248:251], v[16:19], v[52:55]\n" \
  "s_waitcnt lgkmcnt(0)\n" \
  "s_waitcnt vmcnt(10)\n" \
  "s_barrier\n" \
  "v_mfma_f32_16x16x32_bf16 v[56:59], a[72:75], v[20:23], v[56:59]\n" \
  "ds_read_b128 v[16:19], v10 offset:0\n" \
  "v_mfma_f32_16x16x32_bf16 v[60:63], a[152:155], v[20:23], v[60:63]\n" \
  "v_mfma_f32_16x16x32_bf16 v[64:67], a[232:235], v[20:23], v[64:67]\n" \
  "v_mfma_f32_16x16x32_bf16 v[68:71], v[248:251], v[20:23], v[68:71]\n" \
  "v_mfma_f32_16x16x32_bf16 v[40:43], a[76:79], v[24:27], v[40:43]\n" \
  "ds_read_b128 v[20:23], v10 offset:10240\n" \
  "v_mfma_f32_16x16x32_bf16 v[44:47], a[156:159], v[24:27], v[44:47]\n" \
  "v_mfma_f32_16x16x32_bf16 v[48:51], a[236:239], v[24:27], v[48:51]\n" \
  "v_mfma_f32_16x16x32_bf16 v[52:55], v[252:255], v[24:27], v[52:55]\n" \
  "v_mfma_f32_16x16x32_bf16 v[56:59], a[76:79], v[28:31], v[56:59]\n" \
  "ds_read_b128 v[24:27], v10 offset:1024\n" \
  "v_mfma_f32_16x16x32_bf16 v[60:63], a[156:159], v[28:31], v[60:63]\n" \
  "v_mfma_f32_16x16x32_bf16 v[64:67], a[236:239], v[28:31], v[64:67]\n" \
  "v_mfma_f32_16x16x32_bf16 v[68:71], v[252:255], v[28:31], v[68:71]\n" \
  "s_add_u32 s57, s57, 71680\n" \
  "s_min_u32 s57, s57, s76\n" \
  "s_add_u32 s58, s58, 40960\n" \
  "s_sub_u32 s54, s58, 122880\n" \
  "s_cmp_ge_u32 s58, 122880\n" \
  "s_cselect_b32 s58, s54, s58\n" \
  "v_mov_b32 v9, v10\n" \
  "v_mov_b32 v7, v1\n" \
  "s_add_u32 s55, s55, 1\n" \
  "WBP1_%=:\n" \
  "v_add_u32 v10, s59, v8\n" \
  "s_add_u32 s59, s59, 40960\n" \
  "s_cmp_ge_u32 s59, 122880\n" \
  "s_cselect_b32 s59, 0, s59\n" \
  "s_waitcnt lgkmcnt(2)\n" \
  "v_mfma_f32_16x16x32_bf16 v[72:75], a[0:3], v[16:19], 0\n" \
  "ds_read_b128 v[28:31], v9 offset:11264\n" \
  "v_mfma_f32_16x16x32_bf16 v[76:79], a[80:83], v[16:19], 0\n" \
  "v_mfma_f32_16x16x32_bf16 v[80:83], a[160:163], v[16:19], 0\n" \
  "v_mfma_f32_16x16x32_bf16 v[84:87], a[240:243], v[16:19], 0\n" \
  "s_waitcnt lgkmcnt(2)\n" \
  "v_mfma_f32_16x16x32_bf16 v[88:91], a[0:3], v[20:23], 0\n" \
  "ds_read_b128 v[16:19], v9 offset:2048\n" \
  "v_mfma_f32_16x16x32_bf16 v[92:95], a[80:83], v[20:23], 0\n" \
  "s_add_u32 m0, s58, 0\n" \
  "s_add_u32 s54, s57, 0\n" \
  "buffer_load_dwordx4 v1, s[36:39], s54 offen offset:0 sc1 lds\n" \
  "v_mfma_f32_16x16x32_bf16 v[96:99], a[160:163], v[20:23], 0\n" \
  "v_mfma_f32_16x16x32_bf16 v[100:103], a[240:243], v[20:23], 0\n" \
  "s_waitcnt lgkmcnt(2)\n" \
  "v_mfma_f32_16x16x32_bf16 v[72:75], a[4:7], v[24:27], v[72:75]\n" \
  "ds_read_b128 v[20:23], v9 offset:12288\n" \
  "v_mul_f32 v112, 0xbfb8aa3b, v40\n" \
  "v_mfma_f32_16x16x32_bf16 v[76:79], a[84:87], v[24:27], v[76:79]\n" \
  "v_mul_f32 v113, 0xbfb8aa3b, v41\n" \
  "buffer_load_dwordx4 v1, s[36:39], s54 offen offset:1024 sc1 lds\n" \
  "v_mfma_f32_16x16x32_bf16 v[80:83], a[164:167], v[24:27], v[80:83]\n" \
  "v_mul_f32 v114, 0xbfb8aa3b, v42\n" \
  "v_mfma_f32_16x16x32_bf16 v[84:87], a[244:247], v[24:27], v[84:87]\n" \
  "v_mul_f32 v115, 0xbfb8aa3b, v43\n" \
  "s_waitcnt lgkmcnt(2)\n" \
  "v_mfma_f32_16x16x32_bf16 v[88:91], a[4:7], v[28:31], v[88:91]\n" \
  "ds_read_b128 v[24:27], v9 offset:3072\n" \
  "v_exp_f32 v112, v112\n" \
  "v_mfma_f32_16x16x32_bf16 v[92:95], a[84:87], v[28:31], v[92:95]\n" \
  "v_exp_f32 v113, v113\n" \
  "v_mfma_f32_16x16x32_bf16 v[96:99], a[164:167], v[28:31], v[96:99]\n" \
  "v_exp_f32 v114, v114\n" \
  "v_mfma_f32_16x16x32_bf16 v[100:103], a[244:247], v[28:31], v[100:103]\n" \
  "v_exp_f32 v115, v115\n" \
  "s_waitcnt lgkmcnt(2)\n" \
  "v_mfma_f32_16x16x32_bf16 v[72:75], a[8:11], v[16:19], v[72:75]\n" \
  "ds_read_b128 v[28:31], v9 offset:13312\n" \
  "v_add_f32 v112, 1.0, v112\n" \
  "v_mfma_f32_16x16x32_bf16 v[76:79], a[88:91], v[16:19], v[76:79]\n" \
  "v_add_f32 v113, 1.0, v113\n" \
  "buffer_load_dwordx4 v1, s[36:39], s54 offen offset:2048 sc1 lds\n" \
  "v_mfma_f32_16x16x32_bf16 v[80:83], a[168:171], v[16:19], v[80:83]\n" \
  "v_add_f32 v114, 1.0, v114\n" \
  "v_mfma_f32_16x16x32_bf16 v[84:87], a[248:251], v[16:19], v[84:87]\n" \
  "v_add_f32 v115, 1.0, v115\n" \
  "s_waitcnt lgkmcnt(2)\n" \
  "v_mfma_f32_16x16x32_bf16 v[88:91], a[8:11], v[20:23], v[88:91]\n" \
  "ds_read_b128 v[16:19], v9 offset:4096\n" \
  "v_rcp_f32 v112, v112\n" \
  "v_mfma_f32_16x16x32_bf16 v[92:95], a[88:91], v[20:23], v[92:95]\n" \
  "v_rcp_f32 v113, v113\n" \
  "buffer_load_dwordx4 v1, s[36:39], s54 offen offset:3072 sc1 lds\n" \
  "v_mfma_f32_16x16x32_bf16 v[96:99], a[168:171], v[20:23], v[96:99]\n" \
  "v_rcp_f32 v114, v114\n" \
  "v_mfma_f32_16x16x32_bf16 v[100:103], a[248:251], v[20:23], v[100:103]\n" \
  "v_rcp_f32 v115, v115\n" \
  "s_waitcnt lgkmcnt(2)\n" \
  "v_mfma_f32_16x16x32_bf16 v[72:75], a[12:15], v[24:27], v[72:75]\n" \
  "ds_read_b128 v[20:23], v9 offset:14336\n" \
  "v_mul_f32 v40, v40, v112\n" \
  "v_mfma_f32_16x16x32_bf16 v[76:79], a[92:95], v[24:27], v[76:79]\n" \
  "v_mul_f32 v41, v41, v113\n" \
  "v_mfma_f32_16x16x32_bf16 v[80:83], a[172:175], v[24:27], v[80:83]\n" \
  "v_mul_f32 v42, v42, v114\n" \
  "v_mfma_f32_16x16x32_bf16 v[84:87], a[252:255], v[24:27], v[84:87]\n" \
  "v_mul_f32 v43, v43, v115\n" \
  "s_waitcnt lgkmcnt(2)\n" \
  "v_mfma_f32_16x16x32_bf16 v[88:91], a[12:15], v[28:31], v[88:91]\n" \
  "ds_read_b128 v[24:27], v9 offset:5120\n" \
  "v_mul_f32 v40, v40, v48\n" \
  "v_mfma_f32_16x16x32_bf16 v[92:95], a[92:95], v[28:31], v[92:95]\n" \
  "v_mul_f32 v41, v41, v49\n" \
  "s_add_u32 m0, s58, 4096\n" \
  "s_add_u32 s54, s57, 4096\n" \
  "buffer_load_dwordx4 v1, s[36:39], s54 offen offset:0 sc1 lds\n" \
  "v_mfma_f32_16x16x32_bf16 v[96:99], a[172:175], v[28:31], v[96:99]\n" \
  "v_mul_f32 v42, v42, v50\n" \
  "v_mfma_f32_16x16x32_bf16 v[100:103], a[252:255], v[28:31], v[100:103]\n" \
  "v_mul_f32 v43, v43, v51\n" \
  "s_waitcnt lgkmcnt(2)\n" \
  "v_mfma_f32_16x16x32_bf16 v[72:75], a[16:19], v[16:19], v[72:75]\n" \
  "ds_read_b128 v[28:31], v9 offset:15360\n" \
  "v_cvt_pk_bf16_f32 v104, v40, v41\n" \
  "v_mfma_f32_16x16x32_bf16 v[76:79], a[96:99], v[16:19], v[76:79]\n" \
  "v_cvt_pk_bf16_f32 v105, v42, v43\n" \
  "buffer_load_dwordx4 v1, s[36:39], s54 offen offset:1024 sc1 lds\n" \
  "v_mfma_f32_16x16x32_bf16 v[80:83], a[176:179], v[16:19], v[80:83]\n" \
  "v_mul_f32 v112, 0xbfb8aa3b, v44\n" \
  "v_mfma_f32_16x16x32_bf16 v[84:87], v[192:195], v[16:19], v[84:87]\n" \
  "v_mul_f32 v113, 0xbfb8aa3b, v45\n" \
  "s_waitcnt lgkmcnt(2)\n" \
  "v_mfma_f32_16x16x32_bf16 v[88:91], a[16:19], v[20:23], v[88:91]\n" \
  "ds_read_b128 v[16:19], v9 offset:6144\n" \
  "v_mul_f32 v114, 0xbfb8aa3b, v46\n" \
  "v_mfma_f32_16x16x32_bf16 v[92:95], a[96:99], v[20:23], v[92:95]\n" \
  "v_mul_f32 v115, 0xbfb8aa3b, v47\n" \
  "v_mfma_f32_16x16x32_bf16 v[96:99], a[176:179], v[20:23], v[96:99]\n" \
  "v_exp_f32 v112, v112\n" \
  "v_mfma_f32_16x16x32_bf16 v[100:103], v[192:195], v[20:23], v[100:103]\n" \
  "v_exp_f32 v113, v113\n" \
  "s_waitcnt lgkmcnt(2)\n" \
  "v_mfma_f32_16x16x32_bf16 v[72:75], a[20:23], v[24:27], v[72:75]\n" \
  "ds_read_b128 v[20:23], v9 offset:16384\n" \
  "v_exp_f32 v114, v114\n" \
  "v_mfma_f32_16x16x32_bf16 v[76:79], a[100:103], v[24:27], v[76:79]\n" \
  "v_exp_f32 v115, v115\n" \
  "buffer_load_dwordx4 v1, s[36:39], s54 offen offset:2048 sc1 lds\n" \
  "v_mfma_f32_16x16x32_bf16 v[80:83], a[180:183], v[24:27], v[80:83]\n" \
  "v_add_f32 v112, 1.0, v112\n" \
  "v_mfma_f32_16x16x32_bf16 v[84:87], v[196:199], v[24:27], v[84:87]\n" \
  "v_add_f32 v113, 1.0, v113\n" \
  "s_waitcnt lgkmcnt(2)\n" \
  "v_mfma_f32_16x16x32_bf16 v[88:91], a[20:23], v[28:31], v[88:91]\n" \
  "ds_read_b128 v[24:27], v9 offset:7168\n" \
  "v_add_f32 v114, 1.0, v114\n" \
  "v_mfma_f32_16x16x32_bf16 v[92:95], a[100:103], v[28:31], v[92:95]\n" \
  "v_add_f32 v115, 1.0, v115\n" \
  "buffer_load_dwordx4 v1, s[36:39], s54 offen offset:3072 sc1 lds\n" \
  "v_mfma_f32_16x16x32_bf16 v[96:99], a[180:183], v[28:31], v[96:99]\n" \
  "v_rcp_f32 v112, v112\n" \
  "v_mfma_f32_16x16x32_bf16 v[100:103], v[196:199], v[28:31], v[100:103]\n" \
  "v_rcp_f32 v113, v113\n" \
  "s_waitcnt lgkmcnt(2)\n" \
  "v_mfma_f32_16x16x32_bf16 v[72:75], a[24:27], v[16:19], v[72:75]\n" \
  "ds_read_b128 v[28:31], v9 offset:17408\n" \
  "v_rcp_f32 v114, v114\n" \
  "v_mfma_f32_16x16x32_bf16 v[76:79], a[104:107], v[16:19], v[76:79]\n" \
  "v_rcp_f32 v115, v115\n" \
  "v_mfma_f32_16x16x32_bf16 v[80:83], a[184:187], v[16:19], v[80:83]\n" \
  "v_mul_f32 v44, v44, v112\n" \
  "v_mfma_f32_16x16x32_bf16 v[84:87], v[200:203], v[16:19], v[84:87]\n" \
  "v_mul_f32 v45, v45, v113\n" \
  "s_waitcnt lgkmcnt(2)\n" \
  "v_mfma_f32_16x16x32_bf16 v[88:91], a[24:27], v[20:23], v[88:91]\n" \
  "ds_read_b128 v[16:19], v9 offset:8192\n" \
  "v_mul_f32 v46, v46, v114\n" \
  "v_mfma_f32_16x16x32_bf16 v[92:95], a[104:107], v[20:23], v[92:95]\n" \
  "v_mul_f32 v47, v47, v115\n" \
  "s_add_u32 m0, s58, 8192\n" \
  "s_add_u32 s54, s57, 8192\n" \
  "buffer_load_dwordx4 v1, s[36:39], s54 offen offset:0 sc1 lds\n" \
  "v_mfma_f32_16x16x32_bf16 v[96:99], a[184:187], v[20:23], v[96:99]\n" \
  "v_mul_f32 v44, v44, v52\n" \
  "v_mfma_f32_16x16x32_bf16 v[100:103], v[200:203], v[20:23], v[100:103]\n" \
  "v_mul_f32 v45, v45, v53\n" \
  "s_waitcnt lgkmcnt(2)\n" \
  "v_mfma_f32_16x16x32_bf16 v[72:75], a[28:31], v[24:27], v[72:75]\n" \
  "ds_read_b128 v[20:23], v9 offset:18432\n" \
  "v_mul_f32 v46, v46, v54\n" \
  "v_mfma_f32_16x16x32_bf16 v[76:79], a[108:111], v[24:27], v[76:79]\n" \
  "v_mul_f32 v47, v47, v55\n" \
  "buffer_load_dwordx4 v1, s[36:39], s54 offen offset:1024 sc1 lds\n" \
  "v_mfma_f32_16x16x32_bf16 v[80:83], a[188:191], v[24:27], v[80:83]\n" \
  "v_cvt_pk_bf16_f32 v106, v44, v45\n" \
  "v_mfma_f32_16x16x32_bf16 v[84:87], v[204:207], v[24:27], v[84:87]\n" \
  "v_cvt_pk_bf16_f32 v107, v46, v47\n" \
  "s_waitcnt lgkmcnt(2)\n" \
  "v_mfma_f32_16x16x32_bf16 v[88:91], a[28:31], v[28:31], v[88:91]\n" \
  "ds_read_b128 v[24:27], v9 offset:9216\n" \
  "s_add_u32 s52, s60, 0\n" \
  "buffer_store_dwordx4 v[104:107], v7, s[36:39], s52 offen offset:0 sc1\n" \
  "v_mfma_f32_16x16x32_bf16 v[92:95], a[108:111], v[28:31], v[92:95]\n" \
  "v_mul_f32 v112, 0xbfb8aa3b, v56\n" \
  "v_mfma_f32_16x16x32_bf16 v[96:99], a[188:191], v[28:31], v[96:99]\n" \
  "v_mul_f32 v113, 0xbfb8aa3b, v57\n" \
  "v_mfma_f32_16x16x32_bf16 v[100:103], v[204:207], v[28:31], v[100:103]\n" \
  "v_mul_f32 v114, 0xbfb8aa3b, v58\n" \
  "s_waitcnt lgkmcnt(2)\n" \
  "v_mfma_f32_16x16x32_bf16 v[72:75], a[32:35], v[16:19], v[72:75]\n" \
  "ds_read_b128 v[28:31], v9 offset:19456\n" \
  "v_mul_f32 v115, 0xbfb8aa3b, v59\n" \
  "v_mfma_f32_16x16x32_bf16 v[76:79], a[112:115], v[16:19], v[76:79]\n" \
  "v_exp_f32 v112, v112\n" \
  "v_mfma_f32_16x16x32_bf16 v[80:83], a[192:195], v[16:19], v[80:83]\n" \
  "v_exp_f32 v113, v113\n" \
  "v_mfma_f32_16x16x32_bf16 v[84:87], v[208:211], v[16:19], v[84:87]\n" \
  "v_exp_f32 v114, v114\n" \
  "s_waitcnt lgkmcnt(0)\n" \
  "s_waitcnt vmcnt(10)\n" \
  "s_barrier\n" \
  "v_mfma_f32_16x16x32_bf16 v[88:91], a[32:35], v[20:23], v[88:91]\n" \
  "ds_read_b128 v[16:19], v10 offset:0\n" \
  "v_mfma_f32_16x16x32_bf16 v[92:95], a[112:115], v[20:23], v[92:95]\n" \
  "v_mfma_f32_16x16x32_bf16 v[96:99], a[192:195], v[20:23], v[96:99]\n" \
  "v_mfma_f32_16x16x32_bf16 v[100:103], v[208:211], v[20:23], v[100:103]\n" \
  "v_mfma_f32_16x16x32_bf16 v[72:75], a[36:39], v[24:27], v[72:75]\n" \
  "ds_read_b128 v[20:23], v10 offset:10240\n" \
  "v_mfma_f32_16x16x32_bf16 v[76:79], a[116:119], v[24:27], v[76:79]\n" \
  "v_mfma_f32_16x16x32_bf16 v[80:83], a[196:199], v[24:27], v[80:83]\n" \
  "v_mfma_f32_16x16x32_bf16 v[84:87], v[212:215], v[24:27], v[84:87]\n" \
  "v_mfma_f32_16x16x32_bf16 v[88:91], a[36:39], v[28:31], v[88:91]\n" \
  "ds_read_b128 v[24:27], v10 offset:1024\n" \
  "v_mfma_f32_16x16x32_bf16 v[92:95], a[116:119], v[28:31], v[92:95]\n" \
  "v_mfma_f32_16x16x32_bf16 v[96:99], a[196:199], v[28:31], v[96:99]\n" \
  "v_mfma_f32_16x16x32_bf16 v[100:103], v[212:215], v[28:31], v[100:103]\n" \
  "s_add_u32 s57, s57, 10240\n" \
  "s_add_u32 s58, s58, 40960\n" \
  "s_sub_u32 s54, s58, 122880\n" \
  "s_cmp_ge_u32 s58, 122880\n" \
  "s_cselect_b32 s58, s54, s58\n" \
  "v_mov_b32 v9, v10\n" \
  "v_add_u32 v10, s59, v8\n" \
  "s_add_u32 s59, s59, 40960\n" \
  "s_cmp_ge_u32 s59, 122880\n" \
  "s_cselect_b32 s59, 0, s59\n" \
  "s_waitcnt lgkmcnt(2)\n" \
  "v_mfma_f32_16x16x32_bf16 v[72:75], a[40:43], v[16:19], v[72:75]\n" \
  "ds_read_b128 v[28:31], v9 offset:11264\n" \
  "v_exp_f32 v115, v115\n" \
  "v_mfma_f32_16x16x32_bf16 v[76:79], a[120:123], v[16:19], v[76:79]\n" \
  "v_add_f32 v112, 1.0, v112\n" \
  "v_mfma_f32_16x16x32_bf16 v[80:83], a[200:203], v[16:19], v[80:83]\n" \
  "v_add_f32 v113, 1.0, v113\n" \
  "v_mfma_f32_16x16x32_bf16 v[84:87], v[216:219], v[16:19], v[84:87]\n" \
  "v_add_f32 v114, 1.0, v114\n" \
  "s_waitcnt lgkmcnt(2)\n" \
  "v_mfma_f32_16x16x32_bf16 v[88:91], a[40:43], v[20:23], v[88:91]\n" \
  "ds_read_b128 v[16:19], v9 offset:2048\n" \
  "v_add_f32 v115, 1.0, v115\n" \
  "v_mfma_f32_16x16x32_bf16 v[92:95], a[120:123], v[20:23], v[92:95]\n" \
  "v_rcp_f32 v112, v112\n" \
  "s_add_u32 m0, s58, 0\n" \
  "s_add_u32 s54, s57, 0\n" \
  "buffer_load_dwordx4 v1, s[36:39], s54 offen offset:0 sc1 lds\n" \
  "v_mfma_f32_16x16x32_bf16 v[96:99], a[200:203], v[20:23], v[96:99]\n" \
  "v_rcp_f32 v113, v113\n" \
  "v_mfma_f32_16x16x32_bf16 v[100:103], v[216:219], v[20:23], v[100:103]\n" \
  "v_rcp_f32 v114, v114\n" \
  "s_waitcnt lgkmcnt(2)\n" \
  "v_mfma_f32_16x16x32_bf16 v[72:75], a[44:47], v[24:27], v[72:75]\n" \
  "ds_read_b128 v[20:23], v9 offset:12288\n" \
  "v_rcp_f32 v115, v115\n" \
  "v_mfma_f32_16x16x32_bf16 v[76:79], a[124:127], v[24:27], v[76:79]\n" \
  "v_mul_f32 v56, v56, v112\n" \
  "buffer_load_dwordx4 v1, s[36:39], s54 offen offset:1024 sc1 lds\n" \
  "v_mfma_f32_16x16x32_bf16 v[80:83], a[204:207], v[24:27], v[80:83]\n" \
  "v_mul_f32 v57, v57, v113\n" \
  "v_mfma_f32_16x16x32_bf16 v[84:87], v[220:223], v[24:27], v[84:87]\n" \
  "v_mul_f32 v58, v58, v114\n" \
  "s_waitcnt lgkmcnt(2)\n" \
  "v_mfma_f32_16x16x32_bf16 v[88:91], a[44:47], v[28:31], v[88:91]\n" \
  "ds_read_b128 v[24:27], v9 offset:3072\n" \
  "v_mul_f32 v59, v59, v115\n" \
  "v_mfma_f32_16x16x32_bf16 v[92:95], a[124:127], v[28:31], v[92:95]\n" \
  "v_mul_f32 v56, v56, v64\n" \
  "v_mfma_f32_16x16x32_bf16 v[96:99], a[204:207], v[28:31], v[96:99]\n" \
  "v_mul_f32 v57, v57, v65\n" \
  "v_mfma_f32_16x16x32_bf16 v[100:103], v[220:223], v[28:31], v[100:103]\n" \
  "v_mul_f32 v58, v58, v66\n" \
  "s_waitcnt lgkmcnt(2)\n" \
  "v_mfma_f32_16x16x32_bf16 v[72:75], a[48:51], v[16:19], v[72:75]\n" \
  "ds_read_b128 v[28:31], v9 offset:13312\n" \
  "v_mul_f32 v59, v59, v67\n" \
  "v_mfma_f32_16x16x32_bf16 v[76:79], a[128:131], v[16:19], v[76:79]\n" \
  "v_cvt_pk_bf16_f32 v108, v56, v57\n" \
  "buffer_load_dwordx4 v1, s[36:39], s54 offen offset:2048 sc1 lds\n" \
  "v_mfma_f32_16x16x32_bf16 v[80:83], a[208:211], v[16:19], v[80:83]\n" \
  "v_cvt_pk_bf16_f32 v109, v58, v59\n" \
  "v_mfma_f32_16x16x32_bf16 v[84:87], v[224:227], v[16:19], v[84:87]\n" \
  "v_mul_f32 v112, 0xbfb8aa3b, v60\n" \
  "s_waitcnt lgkmcnt(2)\n" \
  "v_mfma_f32_16x16x32_bf16 v[88:91], a[48:51], v[20:23], v[88:91]\n" \
  "ds_read_b128 v[16:19], v9 offset:4096\n" \
  "v_mul_f32 v113, 0xbfb8aa3b, v61\n" \
  "v_mfma_f32_16x16x32_bf16 v[92:95], a[128:131], v[20:23], v[92:95]\n" \
  "v_mul_f32 v114, 0xbfb8aa3b, v62\n" \
  "buffer_load_dwordx4 v1, s[36:39], s54 offen offset:3072 sc1 lds\n" \
  "v_mfma_f32_16x16x32_bf16 v[96:99], a[208:211], v[20:23], v[96:99]\n" \
  "v_mul_f32 v115, 0xbfb8aa3b, v63\n" \
  "v_mfma_f32_16x16x32_bf16 v[100:103], v[224:227], v[20:23], v[100:103]\n" \
  "v_exp_f32 v112, v112\n" \
  "s_waitcnt lgkmcnt(2)\n" \
  "v_mfma_f32_16x16x32_bf16 v[72:75], a[52:55], v[24:27], v[72:75]\n" \
  "ds_read_b128 v[20:23], v9 offset:14336\n" \
  "v_exp_f32 v113, v113\n" \
  "v_mfma_f32_16x16x32_bf16 v[76:79], a[132:135], v[24:27], v[76:79]\n" \
  "v_exp_f32 v114, v114\n" \
  "v_mfma_f32_16x16x32_bf16 v[80:83], a[212:215], v[24:27], v[80:83]\n" \
  "v_exp_f32 v115, v115\n" \
  "v_mfma_f32_16x16x32_bf16 v[84:87], v[228:231], v[24:27], v[84:87]\n" \
  "v_add_f32 v112, 1.0, v112\n" \
  "s_waitcnt lgkmcnt(2)\n" \
  "v_mfma_f32_16x16x32_bf16 v[88:91], a[52:55], v[28:31], v[88:91]\n" \
  "ds_read_b128 v[24:27], v9 offset:5120\n" \
  "v_add_f32 v113, 1.0, v113\n" \
  "v_mfma_f32_16x16x32_bf16 v[92:95], a[132:135], v[28:31], v[92:95]\n" \
  "v_add_f32 v114, 1.0, v114\n" \
  "s_add_u32 m0, s58, 4096\n" \
  "s_add_u32 s54, s57, 4096\n" \
  "buffer_load_dwordx4 v1, s[36:39], s54 offen offset:0 sc1 lds\n" \
  "v_mfma_f32_16x16x32_bf16 v[96:99], a[212:215], v[28:31], v[96:99]\n" \
  "v_add_f32 v115, 1.0, v115\n" \
  "v_mfma_f32_16x16x32_bf16 v[100:103], v[228:231], v[28:31], v[100:103]\n" \
  "v_rcp_f32 v112, v112\n" \
  "s_waitcnt lgkmcnt(2)\n" \
  "v_mfma_f32_16x16x32_bf16 v[72:75], a[56:59], v[16:19], v[72:75]\n" \
  "ds_read_b128 v[28:31], v9 offset:15360\n" \
  "v_rcp_f32 v113, v113\n" \
  "v_mfma_f32_16x16x32_bf16 v[76:79], a[136:139], v[16:19], v[76:79]\n" \
  "v_rcp_f32 v114, v114\n" \
  "buffer_load_dwordx4 v1, s[36:39], s54 offen offset:1024 sc1 lds\n" \
  "v_mfma_f32_16x16x32_bf16 v[80:83], a[216:219], v[16:19], v[80:83]\n" \
  "v_rcp_f32 v115, v115\n" \
  "v_mfma_f32_16x16x32_bf16 v[84:87], v[232:235], v[16:19], v[84:87]\n" \
  "v_mul_f32 v60, v60, v112\n" \
  "s_waitcnt lgkmcnt(2)\n" \
  "v_mfma_f32_16x16x32_bf16 v[88:91], a[56:59], v[20:23], v[88:91]\n" \
  "ds_read_b128 v[16:19], v9 offset:6144\n" \
  "v_mul_f32 v61, v61, v113\n" \
  "v_mfma_f32_16x16x32_bf16 v[92:95], a[136:139], v[20:23], v[92:95]\n" \
  "v_mul_f32 v62, v62, v114\n" \
  "v_mfma_f32_16x16x32_bf16 v[96:99], a[216:219], v[20:23], v[96:99]\n" \
  "v_mul_f32 v63, v63, v115\n" \
  "v_mfma_f32_16x16x32_bf16 v[100:103], v[232:235], v[20:23], v[100:103]\n" \
  "v_mul_f32 v60, v60, v68\n" \
  "s_waitcnt lgkmcnt(2)\n" \
  "v_mfma_f32_16x16x32_bf16 v[72:75], a[60:63], v[24:27], v[72:75]\n" \
  "ds_read_b128 v[20:23], v9 offset:16384\n" \
  "v_mul_f32 v61, v61, v69\n" \
  "v_mfma_f32_16x16x32_bf16 v[76:79], a[140:143], v[24:27], v[76:79]\n" \
  "v_mul_f32 v62, v62, v70\n" \
  "buffer_load_dwordx4 v1, s[36:39], s54 offen offset:2048 sc1 lds\n" \
  "v_mfma_f32_16x16x32_bf16 v[80:83], a[220:223], v[24:27], v[80:83]\n" \
  "v_mul_f32 v63, v63, v71\n" \
  "v_mfma_f32_16x16x32_bf16 v[84:87], v[236:239], v[24:27], v[84:87]\n" \
  "v_cvt_pk_bf16_f32 v110, v60, v61\n" \
  "s_waitcnt lgkmcnt(2)\n" \
  "v_mfma_f32_16x16x32_bf16 v[88:91], a[60:63], v[28:31], v[88:91]\n" \
  "ds_read_b128 v[24:27], v9 offset:7168\n" \
  "v_cvt_pk_bf16_f32 v111, v62, v63\n" \
  "v_mfma_f32_16x16x32_bf16 v[92:95], a[140:143], v[28:31], v[92:95]\n" \
  "s_add_u32 s52, s60, 55296\n" \
  "buffer_store_dwordx4 v[108:111], v7, s[36:39], s52 offen offset:0 sc1\n" \
  "buffer_load_dwordx4 v1, s[36:39], s54 offen offset:3072 sc1 lds\n" \
  "v_mfma_f32_16x16x32_bf16 v[96:99], a[220:223], v[28:31], v[96:99]\n" \
  "s_add_u32 s60, s60, 221184\n" \
  "v_mfma_f32_16x16x32_bf16 v[100:103], v[236:239], v[28:31], v[100:103]\n" \
  "s_waitcnt lgkmcnt(2)\n" \
  "v_mfma_f32_16x16x32_bf16 v[72:75], a[64:67], v[16:19], v[72:75]\n" \
  "ds_read_b128 v[28:31], v9 offset:17408\n" \
  "v_mfma_f32_16x16x32_bf16 v[76:79], a[144:147], v[16:19], v[76:79]\n" \
  "v_mfma_f32_16x16x32_bf16 v[80:83], a[224:227], v[16:19], v[80:83]\n" \
  "v_mfma_f32_16x16x32_bf16 v[84:87], v[240:243], v[16:19], v[84:87]\n" \
  "s_waitcnt lgkmcnt(2)\n" \
  "v_mfma_f32_16x16x32_bf16 v[88:91], a[64:67], v[20:23], v[88:91]\n" \
  "ds_read_b128 v[16:19], v9 offset:8192\n" \
  "v_mfma_f32_16x16x32_bf16 v[92:95], a[144:147], v[20:23], v[92:95]\n" \
  "s_add_u32 m0, s58, 8192\n" \
  "s_add_u32 s54, s57, 8192\n" \
  "buffer_load_dwordx4 v1, s[36:39], s54 offen offset:0 sc1 lds\n" \
  "v_mfma_f32_16x16x32_bf16 v[96:99], a[224:227], v[20:23], v[96:99]\n" \
  "v_mfma_f32_16x16x32_bf16 v[100:103], v[240:243], v[20:23], v[100:103]\n" \
  "s_waitcnt lgkmcnt(2)\n" \
  "v_mfma_f32_16x16x32_bf16 v[72:75], a[68:71], v[24:27], v[72:75]\n" \
  "ds_read_b128 v[20:23], v9 offset:18432\n" \
  "v_mfma_f32_16x16x32_bf16 v[76:79], a[148:151], v[24:27], v[76:79]\n" \
  "buffer_load_dwordx4 v1, s[36:39], s54 offen offset:1024 sc1 lds\n" \
  "v_mfma_f32_16x16x32_bf16 v[80:83], a[228:231], v[24:27], v[80:83]\n" \
  "v_mfma_f32_16x16x32_bf16 v[84:87], v[244:247], v[24:27], v[84:87]\n" \
  "s_waitcnt lgkmcnt(2)\n" \
  "v_mfma_f32_16x16x32_bf16 v[88:91], a[68:71], v[28:31], v[88:91]\n" \
  "ds_read_b128 v[24:27], v9 offset:9216\n" \
  "v_mfma_f32_16x16x32_bf16 v[92:95], a[148:151], v[28:31], v[92:95]\n" \
  "v_mfma_f32_16x16x32_bf16 v[96:99], a[228:231], v[28:31], v[96:99]\n" \
  "v_mfma_f32_16x16x32_bf16 v[100:103], v[244:247], v[28:31], v[100:103]\n" \
  "s_waitcnt lgkmcnt(2)\n" \
  "v_mfma_f32_16x16x32_bf16 v[72:75], a[72:75], v[16:19], v[72:75]\n" \
  "ds_read_b128 v[28:31], v9 offset:19456\n" \
  "v_mfma_f32_16x16x32_bf16 v[76:79], a[152:155], v[16:19], v[76:79]\n" \
  "v_mfma_f32_16x16x32_bf16 v[80:83], a[232:235], v[16:19], v[80:83]\n" \
  "v_mfma_f32_16x16x32_bf16 v[84:87], v[248:251], v[16:19], v[84:87]\n" \
  "s_waitcnt lgkmcnt(0)\n" \
  "s_waitcnt vmcnt(10)\n" \
  "s_barrier\n" \
  "v_mfma_f32_16x16x32_bf16 v[88:91], a[72:75], v[20:23], v[88:91]\n" \
  "ds_read_b128 v[16:19], v10 offset:0\n" \
  "v_mfma_f32_16x16x32_bf16 v[92:95], a[152:155], v[20:23], v[92:95]\n" \
  "v_mfma_f32_16x16x32_bf16 v[96:99], a[232:235], v[20:23], v[96:99]\n" \
  "v_mfma_f32_16x16x32_bf16 v[100:103], v[248:251], v[20:23], v[100:103]\n" \
  "v_mfma_f32_16x16x32_bf16 v[72:75], a[76:79], v[24:27], v[72:75]\n" \
  "ds_read_b128 v[20:23], v10 offset:10240\n" \
  "v_mfma_f32_16x16x32_bf16 v[76:79], a[156:159], v[24:27], v[76:79]\n" \
  "v_mfma_f32_16x16x32_bf16 v[80:83], a[236:239], v[24:27], v[80:83]\n" \
  "v_mfma_f32_16x16x32_bf16 v[84:87], v[252:255], v[24:27], v[84:87]\n" \
  "v_mfma_f32_16x16x32_bf16 v[88:91], a[76:79], v[28:31], v[88:91]\n" \
  "ds_read_b128 v[24:27], v10 offset:1024\n" \
  "v_mfma_f32_16x16x32_bf16 v[92:95], a[156:159], v[28:31], v[92:95]\n" \
  "v_mfma_f32_16x16x32_bf16 v[96:99], a[236:239], v[28:31], v[96:99]\n" \
  "v_mfma_f32_16x16x32_bf16 v[100:103], v[252:255], v[28:31], v[100:103]\n" \
  "s_add_u32 s57, s57, 71680\n" \
  "s_min_u32 s57, s57, s76\n" \
  "s_add_u32 s58, s58, 40960\n" \
  "s_sub_u32 s54, s58, 122880\n" \
  "s_cmp_ge_u32 s58, 122880\n" \
  "s_cselect_b32 s58, s54, s58\n" \
  "v_mov_b32 v9, v10\n" \
  "s_add_u32 s55, s55, 1\n" \
  "s_cmp_lt_u32 s55, s56\n" \
  "s_cbranch_scc1 WBLP_%=\n" \
  "v_mul_f32 v112, 0xbfb8aa3b, v72\n" \
  "v_mul_f32 v113, 0xbfb8aa3b, v73\n" \
  "v_mul_f32 v114, 0xbfb8aa3b, v74\n" \
  "v_mul_f32 v115, 0xbfb8aa3b, v75\n" \
  "v_exp_f32 v112, v112\n" \
  "v_exp_f32 v113, v113\n" \
  "v_exp_f32 v114, v114\n" \
  "v_exp_f32 v115, v115\n" \
  "v_add_f32 v112, 1.0, v112\n" \
  "v_add_f32 v113, 1.0, v113\n" \
  "v_add_f32 v114, 1.0, v114\n" \
  "v_add_f32 v115, 1.0, v115\n" \
  "v_rcp_f32 v112, v112\n" \
  "v_rcp_f32 v113, v113\n" \
  "v_rcp_f32 v114, v114\n" \
  "v_rcp_f32 v115, v115\n" \
  "v_mul_f32 v72, v72, v112\n" \
  "v_mul_f32 v73, v73, v113\n" \
  "v_mul_f32 v74, v74, v114\n" \
  "v_mul_f32 v75, v75, v115\n" \
  "v_mul_f32 v72, v72, v80\n" \
  "v_mul_f32 v73, v73, v81\n" \
  "v_mul_f32 v74, v74, v82\n" \
  "v_mul_f32 v75, v75, v83\n" \
  "v_cvt_pk_bf16_f32 v104, v72, v73\n" \
  "v_cvt_pk_bf16_f32 v105, v74, v75\n" \
  "v_mul_f32 v112, 0xbfb8aa3b, v76\n" \
  "v_mul_f32 v113, 0xbfb8aa3b, v77\n" \
  "v_mul_f32 v114, 0xbfb8aa3b, v78\n" \
  "v_mul_f32 v115, 0xbfb8aa3b, v79\n" \
  "v_exp_f32 v112, v112\n" \
  "v_exp_f32 v113, v113\n" \
  "v_exp_f32 v114, v114\n" \
  "v_exp_f32 v115, v115\n" \
  "v_add_f32 v112, 1.0, v112\n" \
  "v_add_f32 v113, 1.0, v113\n" \
  "v_add_f32 v114, 1.0, v114\n" \
  "v_add_f32 v115, 1.0, v115\n" \
  "v_rcp_f32 v112, v112\n" \
  "v_rcp_f32 v113, v113\n" \
  "v_rcp_f32 v114, v114\n" \
  "v_rcp_f32 v115, v115\n" \
  "v_mul_f32 v76, v76, v112\n" \
  "v_mul_f32 v77, v77, v113\n" \
  "v_mul_f32 v78, v78, v114\n" \
  "v_mul_f32 v79, v79, v115\n" \
  "v_mul_f32 v76, v76, v84\n" \
  "v_mul_f32 v77, v77, v85\n" \
  "v_mul_f32 v78, v78, v86\n" \
  "v_mul_f32 v79, v79, v87\n" \
  "v_cvt_pk_bf16_f32 v106, v76, v77\n" \
  "v_cvt_pk_bf16_f32 v107, v78, v79\n" \
  "s_add_u32 s52, s60, 0\n" \
  "buffer_store_dwordx4 v[104:107], v7, s[36:39], s52 offen offset:0 sc1\n" \
  "v_mul_f32 v112, 0xbfb8aa3b, v88\n" \
  "v_mul_f32 v113, 0xbfb8aa3b, v89\n" \
  "v_mul_f32 v114, 0xbfb8aa3b, v90\n" \
  "v_mul_f32 v115, 0xbfb8aa3b, v91\n" \
  "v_exp_f32 v112, v112\n" \
  "v_exp_f32 v113, v113\n" \
  "v_exp_f32 v114, v114\n" \
  "v_exp_f32 v115, v115\n" \
  "v_add_f32 v112, 1.0, v112\n" \
  "v_add_f32 v113, 1.0, v113\n" \
  "v_add_f32 v114, 1.0, v114\n" \
  "v_add_f32 v115, 1.0, v115\n" \
  "v_rcp_f32 v112, v112\n" \
  "v_rcp_f32 v113, v113\n" \
  "v_rcp_f32 v114, v114\n" \
  "v_rcp_f32 v115, v115\n" \
  "v_mul_f32 v88, v88, v112\n" \
  "v_mul_f32 v89, v89, v113\n" \
  "v_mul_f32 v90, v90, v114\n" \
  "v_mul_f32 v91, v91, v115\n" \
  "v_mul_f32 v88, v88, v96\n" \
  "v_mul_f32 v89, v89, v97\n" \
  "v_mul_f32 v90, v90, v98\n" \
  "v_mul_f32 v91, v91, v99\n" \
  "v_cvt_pk_bf16_f32 v108, v88, v89\n" \
  "v_cvt_pk_bf16_f32 v109, v90, v91\n" \
  "v_mul_f32 v112, 0xbfb8aa3b, v92\n" \
  "v_mul_f32 v113, 0xbfb8aa3b, v93\n" \
  "v_mul_f32 v114, 0xbfb8aa3b, v94\n" \
  "v_mul_f32 v115, 0xbfb8aa3b, v95\n" \
  "v_exp_f32 v112, v112\n" \
  "v_exp_f32 v113, v113\n" \
  "v_exp_f32 v114, v114\n" \
  "v_exp_f32 v115, v115\n" \
  "v_add_f32 v112, 1.0, v112\n" \
  "v_add_f32 v113, 1.0, v113\n" \
  "v_add_f32 v114, 1.0, v114\n" \
  "v_add_f32 v115, 1.0, v115\n" \
  "v_rcp_f32 v112, v112\n" \
  "v_rcp_f32 v113, v113\n" \
  "v_rcp_f32 v114, v114\n" \
  "v_rcp_f32 v115, v115\n" \
  "v_mul_f32 v92, v92, v112\n" \
  "v_mul_f32 v93, v93, v113\n" \
  "v_mul_f32 v94, v94, v114\n" \
  "v_mul_f32 v95, v95, v115\n" \
  "v_mul_f32 v92, v92, v100\n" \
  "v_mul_f32 v93, v93, v101\n" \
  "v_mul_f32 v94, v94, v102\n" \
  "v_mul_f32 v95, v95, v103\n" \
  "v_cvt_pk_bf16_f32 v110, v92, v93\n" \
  "v_cvt_pk_bf16_f32 v111, v94, v95\n" \
  "s_add_u32 s52, s60, 55296\n" \
  "buffer_store_dwordx4 v[108:111], v7, s[36:39], s52 offen offset:0 sc1\n" \
  "s_add_u32 s60, s60, 221184\n" \
  "s_waitcnt vmcnt(0) lgkmcnt(0)\n" \
  "s_barrier\n" \
  "WHSKIP_%=:\n" \
  "s_waitcnt vmcnt(0)\n" \
  "s_barrier\n" \
  "s_cmp_lg_u32 s48, 0\n" \
  "s_cbranch_scc1 WHD1_%=\n" \
  "s_and_b32 s52, s49, 3\n" \
  "s_lshl_b32 s52, s52, 8\n" \
  "s_add_u32 s52, s52, 12583936\n" \
  "v_mov_b32 v4, 1\n" \
  "s_mov_b64 exec, 1\n" \
  "buffer_atomic_add v4, off, s[36:39], s52 sc1\n" \
  "s_mov_b64 exec, -1\n" \
  "WHD1_%=:\n" \
  "s_cmp_ge_u32 s49, 120\n" \
  "s_cbranch_scc1 WIDLE_%=\n" \
  "s_and_b32 s63, s49, 1\n" \
  "s_mul_i32 s65, s49, 3277\n" \
  "s_lshr_b32 s65, s65, 16\n" \
  "s_mul_i32 s52, s65, 20\n" \
  "s_sub_u32 s64, s49, s52\n" \
  "s_lshr_b32 s64, s64, 1\n" \
  "v_lshrrev_b32 v4, 3, v0\n" \
  "v_mul_u32_u24 v11, 1280, v4\n" \
  "v_and_b32 v4, 6, v4\n" \
  "v_and_b32 v5, 7, v0\n" \
  "v_xor_b32 v4, v4, v5\n" \
  "v_lshl_add_u32 v11, v4, 4, v11\n" \
  "s_mul_i32 s72, s48, 32768\n" \
  "v_bfe_u32 v4, v0, 2, 2\n" \
  "v_lshl_add_u32 v12, v3, 2, v4\n" \
  "v_bfe_u32 v4, v12, 1, 2\n" \
  "v_xor_b32 v4, 0, v4\n" \
  "v_lshlrev_b32 v12, 7, v12\n" \
  "v_lshl_add_u32 v12, v4, 5, v12\n" \
  "v_and_b32 v4, 3, v0\n" \
  "v_lshl_add_u32 v12, v4, 3, v12\n" \
  "v_add_u32 v12, s72, v12\n" \
  "v_bfe_u32 v4, v0, 2, 2\n" \
  "v_lshl_add_u32 v13, v3, 2, v4\n" \
  "v_bfe_u32 v4, v13, 1, 2\n" \
  "v_xor_b32 v4, 1, v4\n" \
  "v_lshlrev_b32 v13, 7, v13\n" \
  "v_lshl_add_u32 v13, v4, 5, v13\n" \
  "v_and_b32 v4, 3, v0\n" \
  "v_lshl_add_u32 v13, v4, 3, v13\n" \
  "v_add_u32 v13, s72, v13\n" \
  "v_bfe_u32 v4, v0, 2, 2\n" \
  "v_lshl_add_u32 v14, v3, 2, v4\n" \
  "v_bfe_u32 v4, v14, 1, 2\n" \
  "v_xor_b32 v4, 2, v4\n" \
  "v_lshlrev_b32 v14, 7, v14\n" \
  "v_lshl_add_u32 v14, v4, 5, v14\n" \
  "v_and_b32 v4, 3, v0\n" \
  "v_lshl_add_u32 v14, v4, 3, v14\n" \
  "v_add_u32 v14, s72, v14\n" \
  "v_bfe_u32 v4, v0, 2, 2\n" \
  "v_lshl_add_u32 v15, v3, 2, v4\n" \
  "v_bfe_u32 v4, v15, 1, 2\n" \
  "v_xor_b32 v4, 3, v4\n" \
  "v_lshlrev_b32 v15, 7, v15\n" \
  "v_lshl_add_u32 v15, v4, 5, v15\n" \
  "v_and_b32 v4, 3, v0\n" \
  "v_lshl_add_u32 v15, v4, 3, v15\n" \
  "v_add_u32 v15, s72, v15\n" \
  "s_mul_i32 s73, s48, 13\n" \
  "s_min_u32 s52, s48, 2\n" \
  "s_add_u32 s73, s73, s52\n" \
  "s_min_u32 s73, s73, 40\n" \
  "s_add_u32 s52, s73, 0\n" \
  "s_mul_i32 s52, s52, 40960\n" \
  "s_lshl_b32 s53, s64, 7\n" \
  "s_add_u32 s52, s52, s53\n" \
  "s_add_u32 s80, s30, s52\n" \
  "s_addc_u32 s81, s31, 0\n" \
  "s_add_u32 m0, s72, 0\n" \
  "s_nop 0\n" \
  "global_load_lds_dwordx4 v11, s[80:81]\n" \
  "s_waitcnt vmcnt(4)\n" \
  "s_add_u32 s80, s80, 10240\n" \
  "s_addc_u32 s81, s81, 0\n" \
  "s_add_u32 m0, s72, 1024\n" \
  "s_nop 0\n" \
  "global_load_lds_dwordx4 v11, s[80:81]\n" \
  "s_waitcnt vmcnt(4)\n" \
  "s_add_u32 s80, s80, 10240\n" \
  "s_addc_u32 s81, s81, 0\n" \
  "s_add_u32 m0, s72, 2048\n" \
  "s_nop 0\n" \
  "global_load_lds_dwordx4 v11, s[80:81]\n" \
  "s_waitcnt vmcnt(4)\n" \
  "s_add_u32 s80, s80, 10240\n" \
  "s_addc_u32 s81, s81, 0\n" \
  "s_add_u32 m0, s72, 3072\n" \
  "s_nop 0\n" \
  "global_load_lds_dwordx4 v11, s[80:81]\n" \
  "s_waitcnt vmcnt(4)\n" \
  "s_add_u32 s80, s80, 10240\n" \
  "s_addc_u32 s81, s81, 0\n" \
  "s_add_u32 m0, s72, 4096\n" \
  "s_nop 0\n" \
  "global_load_lds_dwordx4 v11, s[80:81]\n" \
  "s_waitcnt vmcnt(4)\n" \
  "s_add_u32 s80, s80, 10240\n" \
  "s_addc_u32 s81, s81, 0\n" \
  "s_add_u32 m0, s72, 5120\n" \
  "s_nop 0\n" \
  "global_load_lds_dwordx4 v11, s[80:81]\n" \
  "s_waitcnt vmcnt(4)\n" \
  "s_add_u32 s80, s80, 10240\n" \
  "s_addc_u32 s81, s81, 0\n" \
  "s_add_u32 m0, s72, 6144\n" \
  "s_nop 0\n" \
  "global_load_lds_dwordx4 v11, s[80:81]\n" \
  "s_waitcnt vmcnt(4)\n" \
  "s_add_u32 s80, s80, 10240\n" \
  "s_addc_u32 s81, s81, 0\n" \
  "s_add_u32 m0, s72, 7168\n" \
  "s_nop 0\n" \
  "global_load_lds_dwordx4 v11, s[80:81]\n" \
  "s_waitcnt vmcnt(4)\n" \
  "s_add_u32 s80, s80, 10240\n" \
  "s_addc_u32 s81, s81, 0\n" \
  "s_add_u32 m0, s72, 8192\n" \
  "s_nop 0\n" \
  "global_load_lds_dwordx4 v11, s[80:81]\n" \
  "s_waitcnt vmcnt(4)\n" \
  "s_add_u32 s80, s80, 10240\n" \
  "s_addc_u32 s81, s81, 0\n" \
  "s_add_u32 m0, s72, 9216\n" \
  "s_nop 0\n" \
  "global_load_lds_dwordx4 v11, s[80:81]\n" \
  "s_waitcnt vmcnt(4)\n" \
  "s_add_u32 s80, s80, 10240\n" \
  "s_addc_u32 s81, s81, 0\n" \
  "s_add_u32 m0, s72, 10240\n" \
  "s_nop 0\n" \
  "global_load_lds_dwordx4 v11, s[80:81]\n" \
  "s_waitcnt vmcnt(4)\n" \
  "s_add_u32 s80, s80, 10240\n" \
  "s_addc_u32 s81, s81, 0\n" \
  "s_add_u32 m0, s72, 11264\n" \
  "s_nop 0\n" \
  "global_load_lds_dwordx4 v11, s[80:81]\n" \
  "s_waitcnt vmcnt(4)\n" \
  "s_add_u32 s80, s80, 10240\n" \
  "s_addc_u32 s81, s81, 0\n" \
  "s_add_u32 m0, s72, 12288\n" \
  "s_nop 0\n" \
  "global_load_lds_dwordx4 v11, s[80:81]\n" \
  "s_waitcnt vmcnt(4)\n" \
  "s_add_u32 s80, s80, 10240\n" \
  "s_addc_u32 s81, s81, 0\n" \
  "s_add_u32 m0, s72, 13312\n" \
  "s_nop 0\n" \
  "global_load_lds_dwordx4 v11, s[80:81]\n" \
  "s_waitcnt vmcnt(4)\n" \
  "s_add_u32 s80, s80, 10240\n" \
  "s_addc_u32 s81, s81, 0\n" \
  "s_add_u32 m0, s72, 14336\n" \
  "s_nop 0\n" \
  "global_load_lds_dwordx4 v11, s[80:81]\n" \
  "s_waitcnt vmcnt(4)\n" \
  "s_add_u32 s80, s80, 10240\n" \
  "s_addc_u32 s81, s81, 0\n" \
  "s_add_u32 m0, s72, 15360\n" \
  "s_nop 0\n" \
  "global_load_lds_dwordx4 v11, s[80:81]\n" \
  "s_waitcnt vmcnt(4)\n" \
  "s_add_u32 s80, s80, 10240\n" \
  "s_addc_u32 s81, s81, 0\n" \
  "s_add_u32 m0, s72, 16384\n" \
  "s_nop 0\n" \
  "global_load_lds_dwordx4 v11, s[80:81]\n" \
  "s_waitcnt vmcnt(4)\n" \
  "s_add_u32 s80, s80, 10240\n" \
  "s_addc_u32 s81, s81, 0\n" \
  "s_add_u32 m0, s72, 17408\n" \
  "s_nop 0\n" \
  "global_load_lds_dwordx4 v11, s[80:81]\n" \
  "s_waitcnt vmcnt(4)\n" \
  "s_add_u32 s80, s80, 10240\n" \
  "s_addc_u32 s81, s81, 0\n" \
  "s_add_u32 m0, s72, 18432\n" \
  "s_nop 0\n" \
  "global_load_lds_dwordx4 v11, s[80:81]\n" \
  "s_waitcnt vmcnt(4)\n" \
  "s_add_u32 s80, s80, 10240\n" \
  "s_addc_u32 s81, s81, 0\n" \
  "s_add_u32 m0, s72, 19456\n" \
  "s_nop 0\n" \
  "global_load_lds_dwordx4 v11, s[80:81]\n" \
  "s_waitcnt vmcnt(4)\n" \
  "s_add_u32 s80, s80, 10240\n" \
  "s_addc_u32 s81, s81, 0\n" \
  "s_add_u32 m0, s72, 20480\n" \
  "s_nop 0\n" \
  "global_load_lds_dwordx4 v11, s[80:81]\n" \
  "s_waitcnt vmcnt(4)\n" \
  "s_add_u32 s80, s80, 10240\n" \
  "s_addc_u32 s81, s81, 0\n" \
  "s_add_u32 m0, s72, 21504\n" \
  "s_nop 0\n" \
  "global_load_lds_dwordx4 v11, s[80:81]\n" \
  "s_waitcnt vmcnt(4)\n" \
  "s_add_u32 s80, s80, 10240\n" \
  "s_addc_u32 s81, s81, 0\n" \
  "s_add_u32 m0, s72, 22528\n" \
  "s_nop 0\n" \
  "global_load_lds_dwordx4 v11, s[80:81]\n" \
  "s_waitcnt vmcnt(4)\n" \
  "s_add_u32 s80, s80, 10240\n" \
  "s_addc_u32 s81, s81, 0\n" \
  "s_add_u32 m0, s72, 23552\n" \
  "s_nop 0\n" \
  "global_load_lds_dwordx4 v11, s[80:81]\n" \
  "s_waitcnt vmcnt(4)\n" \
  "s_add_u32 s80, s80, 10240\n" \
  "s_addc_u32 s81, s81, 0\n" \
  "s_add_u32 m0, s72, 24576\n" \
  "s_nop 0\n" \
  "global_load_lds_dwordx4 v11, s[80:81]\n" \
  "s_waitcnt vmcnt(4)\n" \
  "s_add_u32 s80, s80, 10240\n" \
  "s_addc_u32 s81, s81, 0\n" \
  "s_add_u32 m0, s72, 25600\n" \
  "s_nop 0\n" \
  "global_load_lds_dwordx4 v11, s[80:81]\n" \
  "s_waitcnt vmcnt(4)\n" \
  "s_add_u32 s80, s80, 10240\n" \
  "s_addc_u32 s81, s81, 0\n" \
  "s_add_u32 m0, s72, 26624\n" \
  "s_nop 0\n" \
  "global_load_lds_dwordx4 v11, s[80:81]\n" \
  "s_waitcnt vmcnt(4)\n" \
  "s_add_u32 s80, s80, 10240\n" \
  "s_addc_u32 s81, s81, 0\n" \
  "s_add_u32 m0, s72, 27648\n" \
  "s_nop 0\n" \
  "global_load_lds_dwordx4 v11, s[80:81]\n" \
  "s_waitcnt vmcnt(4)\n" \
  "s_waitcnt vmcnt(0)\n" \
  "ds_read_b64_tr_b16 a[0:1], v12 offset:0\n" \
  "ds_read_b64_tr_b16 a[2:3], v12 offset:2048\n" \
  "ds_read_b64_tr_b16 a[56:57], v13 offset:0\n" \
  "ds_read_b64_tr_b16 a[58:59], v13 offset:2048\n" \
  "ds_read_b64_tr_b16 a[112:113], v14 offset:0\n" \
  "ds_read_b64_tr_b16 a[114:115], v14 offset:2048\n" \
  "ds_read_b64_tr_b16 a[168:169], v15 offset:0\n" \
  "ds_read_b64_tr_b16 a[170:171], v15 offset:2048\n" \
  "ds_read_b64_tr_b16 a[4:5], v12 offset:4096\n" \
  "ds_read_b64_tr_b16 a[6:7], v12 offset:6144\n" \
  "ds_read_b64_tr_b16 a[60:61], v13 offset:4096\n" \
  "ds_read_b64_tr_b16 a[62:63], v13 offset:6144\n" \
  "ds_read_b64_tr_b16 a[116:117], v14 offset:4096\n" \
  "ds_read_b64_tr_b16 a[118:119], v14 offset:6144\n" \
  "ds_read_b64_tr_b16 a[172:173], v15 offset:4096\n" \
  "ds_read_b64_tr_b16 a[174:175], v15 offset:6144\n" \
  "ds_read_b64_tr_b16 a[8:9], v12 offset:8192\n" \
  "ds_read_b64_tr_b16 a[10:11], v12 offset:10240\n" \
  "ds_read_b64_tr_b16 a[64:65], v13 offset:8192\n" \
  "ds_read_b64_tr_b16 a[66:67], v13 offset:10240\n" \
  "ds_read_b64_tr_b16 a[120:121], v14 offset:8192\n" \
  "ds_read_b64_tr_b16 a[122:123], v14 offset:10240\n" \
  "ds_read_b64_tr_b16 a[176:177], v15 offset:8192\n" \
  "ds_read_b64_tr_b16 a[178:179], v15 offset:10240\n" \
  "ds_read_b64_tr_b16 a[12:13], v12 offset:12288\n" \
  "ds_read_b64_tr_b16 a[14:15], v12 offset:14336\n" \
  "ds_read_b64_tr_b16 a[68:69], v13 offset:12288\n" \
  "ds_read_b64_tr_b16 a[70:71], v13 offset:14336\n" \
  "ds_read_b64_tr_b16 a[124:125], v14 offset:12288\n" \
  "ds_read_b64_tr_b16 a[126:127], v14 offset:14336\n" \
  "ds_read_b64_tr_b16 a[180:181], v15 offset:12288\n" \
  "ds_read_b64_tr_b16 a[182:183], v15 offset:14336\n" \
  "ds_read_b64_tr_b16 a[16:17], v12 offset:16384\n" \
  "ds_read_b64_tr_b16 a[18:19], v12 offset:18432\n" \
  "ds_read_b64_tr_b16 a[72:73], v13 offset:16384\n" \
  "ds_read_b64_tr_b16 a[74:75], v13 offset:18432\n" \
  "ds_read_b64_tr_b16 a[128:129], v14 offset:16384\n" \
  "ds_read_b64_tr_b16 a[130:131], v14 offset:18432\n" \
  "ds_read_b64_tr_b16 a[184:185], v15 offset:16384\n" \
  "ds_read_b64_tr_b16 a[186:187], v15 offset:18432\n" \
  "ds_read_b64_tr_b16 a[20:21], v12 offset:20480\n" \
  "ds_read_b64_tr_b16 a[22:23], v12 offset:22528\n" \
  "ds_read_b64_tr_b16 a[76:77], v13 offset:20480\n" \
  "ds_read_b64_tr_b16 a[78:79], v13 offset:22528\n" \
  "ds_read_b64_tr_b16 a[132:133], v14 offset:20480\n" \
  "ds_read_b64_tr_b16 a[134:135], v14 offset:22528\n" \
  "ds_read_b64_tr_b16 a[188:189], v15 offset:20480\n" \
  "ds_read_b64_tr_b16 a[190:191], v15 offset:22528\n" \
  "ds_read_b64_tr_b16 a[24:25], v12 offset:24576\n" \
  "ds_read_b64_tr_b16 a[26:27], v12 offset:26624\n" \
  "ds_read_b64_tr_b16 a[80:81], v13 offset:24576\n" \
  "ds_read_b64_tr_b16 a[82:83], v13 offset:26624\n" \
  "ds_read_b64_tr_b16 a[136:137], v14 offset:24576\n" \
  "ds_read_b64_tr_b16 a[138:139], v14 offset:26624\n" \
  "ds_read_b64_tr_b16 a[192:193], v15 offset:24576\n" \
  "ds_read_b64_tr_b16 a[194:195], v15 offset:26624\n" \
  "s_waitcnt lgkmcnt(0)\n" \
  "s_add_u32 s52, s73, 7\n" \
  "s_mul_i32 s52, s52, 40960\n" \
  "s_lshl_b32 s53, s64, 7\n" \
  "s_add_u32 s52, s52, s53\n" \
  "s_add_u32 s80, s30, s52\n" \
  "s_addc_u32 s81, s31, 0\n" \
  "s_add_u32 m0, s72, 0\n" \
  "s_nop 0\n" \
  "global_load_lds_dwordx4 v11, s[80:81]\n" \
  "s_waitcnt vmcnt(4)\n" \
  "s_add_u32 s80, s80, 10240\n" \
  "s_addc_u32 s81, s81, 0\n" \
  "s_add_u32 m0, s72, 1024\n" \
  "s_nop 0\n" \
  "global_load_lds_dwordx4 v11, s[80:81]\n" \
  "s_waitcnt vmcnt(4)\n" \
  "s_add_u32 s80, s80, 10240\n" \
  "s_addc_u32 s81, s81, 0\n" \
  "s_add_u32 m0, s72, 2048\n" \
  "s_nop 0\n" \
  "global_load_lds_dwordx4 v11, s[80:81]\n" \
  "s_waitcnt vmcnt(4)\n" \
  "s_add_u32 s80, s80, 10240\n" \
  "s_addc_u32 s81, s81, 0\n" \
  "s_add_u32 m0, s72, 3072\n" \
  "s_nop 0\n" \
  "global_load_lds_dwordx4 v11, s[80:81]\n" \
  "s_waitcnt vmcnt(4)\n" \
  "s_add_u32 s80, s80, 10240\n" \
  "s_addc_u32 s81, s81, 0\n" \
  "s_add_u32 m0, s72, 4096\n" \
  "s_nop 0\n" \
  "global_load_lds_dwordx4 v11, s[80:81]\n" \
  "s_waitcnt vmcnt(4)\n" \
  "s_add_u32 s80, s80, 10240\n" \
  "s_addc_u32 s81, s81, 0\n" \
  "s_add_u32 m0, s72, 5120\n" \
  "s_nop 0\n" \
  "global_load_lds_dwordx4 v11, s[80:81]\n" \
  "s_waitcnt vmcnt(4)\n" \
  "s_add_u32 s80, s80, 10240\n" \
  "s_addc_u32 s81, s81, 0\n" \
  "s_add_u32 m0, s72, 6144\n" \
  "s_nop 0\n" \
  "global_load_lds_dwordx4 v11, s[80:81]\n" \
  "s_waitcnt vmcnt(4)\n" \
  "s_add_u32 s80, s80, 10240\n" \
  "s_addc_u32 s81, s81, 0\n" \
  "s_add_u32 m0, s72, 7168\n" \
  "s_nop 0\n" \
  "global_load_lds_dwordx4 v11, s[80:81]\n" \
  "s_waitcnt vmcnt(4)\n" \
  "s_add_u32 s80, s80, 10240\n" \
  "s_addc_u32 s81, s81, 0\n" \
  "s_add_u32 m0, s72, 8192\n" \
  "s_nop 0\n" \
  "global_load_lds_dwordx4 v11, s[80:81]\n" \
  "s_waitcnt vmcnt(4)\n" \
  "s_add_u32 s80, s80, 10240\n" \
  "s_addc_u32 s81, s81, 0\n" \
  "s_add_u32 m0, s72, 9216\n" \
  "s_nop 0\n" \
  "global_load_lds_dwordx4 v11, s[80:81]\n" \
  "s_waitcnt vmcnt(4)\n" \
  "s_add_u32 s80, s80, 10240\n" \
  "s_addc_u32 s81, s81, 0\n" \
  "s_add_u32 m0, s72, 10240\n" \
  "s_nop 0\n" \
  "global_load_lds_dwordx4 v11, s[80:81]\n" \
  "s_waitcnt vmcnt(4)\n" \
  "s_add_u32 s80, s80, 10240\n" \
  "s_addc_u32 s81, s81, 0\n" \
  "s_add_u32 m0, s72, 11264\n" \
  "s_nop 0\n" \
  "global_load_lds_dwordx4 v11, s[80:81]\n" \
  "s_waitcnt vmcnt(4)\n" \
  "s_add_u32 s80, s80, 10240\n" \
  "s_addc_u32 s81, s81, 0\n" \
  "s_add_u32 m0, s72, 12288\n" \
  "s_nop 0\n" \
  "global_load_lds_dwordx4 v11, s[80:81]\n" \
  "s_waitcnt vmcnt(4)\n" \
  "s_add_u32 s80, s80, 10240\n" \
  "s_addc_u32 s81, s81, 0\n" \
  "s_add_u32 m0, s72, 13312\n" \
  "s_nop 0\n" \
  "global_load_lds_dwordx4 v11, s[80:81]\n" \
  "s_waitcnt vmcnt(4)\n" \
  "s_add_u32 s80, s80, 10240\n" \
  "s_addc_u32 s81, s81, 0\n" \
  "s_add_u32 m0, s72, 14336\n" \
  "s_nop 0\n" \
  "global_load_lds_dwordx4 v11, s[80:81]\n" \
  "s_waitcnt vmcnt(4)\n" \
  "s_add_u32 s80, s80, 10240\n" \
  "s_addc_u32 s81, s81, 0\n" \
  "s_add_u32 m0, s72, 15360\n" \
  "s_nop 0\n" \
  "global_load_lds_dwordx4 v11, s[80:81]\n" \
  "s_waitcnt vmcnt(4)\n" \
  "s_add_u32 s80, s80, 10240\n" \
  "s_addc_u32 s81, s81, 0\n" \
  "s_add_u32 m0, s72, 16384\n" \
  "s_nop 0\n" \
  "global_load_lds_dwordx4 v11, s[80:81]\n" \
  "s_waitcnt vmcnt(4)\n" \
  "s_add_u32 s80, s80, 10240\n" \
  "s_addc_u32 s81, s81, 0\n" \
  "s_add_u32 m0, s72, 17408\n" \
  "s_nop 0\n" \
  "global_load_lds_dwordx4 v11, s[80:81]\n" \
  "s_waitcnt vmcnt(4)\n" \
  "s_add_u32 s80, s80, 10240\n" \
  "s_addc_u32 s81, s81, 0\n" \
  "s_add_u32 m0, s72, 18432\n" \
  "s_nop 0\n" \
  "global_load_lds_dwordx4 v11, s[80:81]\n" \
  "s_waitcnt vmcnt(4)\n" \
  "s_add_u32 s80, s80, 10240\n" \
  "s_addc_u32 s81, s81, 0\n" \
  "s_add_u32 m0, s72, 19456\n" \
  "s_nop 0\n" \
  "global_load_lds_dwordx4 v11, s[80:81]\n" \
  "s_waitcnt vmcnt(4)\n" \
  "s_add_u32 s80, s80, 10240\n" \
  "s_addc_u32 s81, s81, 0\n" \
  "s_add_u32 m0, s72, 20480\n" \
  "s_nop 0\n" \
  "global_load_lds_dwordx4 v11, s[80:81]\n" \
  "s_waitcnt vmcnt(4)\n" \
  "s_add_u32 s80, s80, 10240\n" \
  "s_addc_u32 s81, s81, 0\n" \
  "s_add_u32 m0, s72, 21504\n" \
  "s_nop 0\n" \
  "global_load_lds_dwordx4 v11, s[80:81]\n" \
  "s_waitcnt vmcnt(4)\n" \
  "s_add_u32 s80, s80, 10240\n" \
  "s_addc_u32 s81, s81, 0\n" \
  "s_add_u32 m0, s72, 22528\n" \
  "s_nop 0\n" \
  "global_load_lds_dwordx4 v11, s[80:81]\n" \
  "s_waitcnt vmcnt(4)\n" \
  "s_add_u32 s80, s80, 10240\n" \
  "s_addc_u32 s81, s81, 0\n" \
  "s_add_u32 m0, s72, 23552\n" \
  "s_nop 0\n" \
  "global_load_lds_dwordx4 v11, s[80:81]\n" \
  "s_waitcnt vmcnt(4)\n" \
  "s_add_u32 s80, s80, 10240\n" \
  "s_addc_u32 s81, s81, 0\n" \
  "s_add_u32 m0, s72, 24576\n" \
  "s_nop 0\n" \
  "global_load_lds_dwordx4 v11, s[80:81]\n" \
  "s_waitcnt vmcnt(4)\n" \
  "s_add_u32 s80, s80, 10240\n" \
  "s_addc_u32 s81, s81, 0\n" \
  "s_add_u32 m0, s72, 25600\n" \
  "s_nop 0\n" \
  "global_load_lds_dwordx4 v11, s[80:81]\n" \
  "s_waitcnt vmcnt(4)\n" \
  "s_add_u32 s80, s80, 10240\n" \
  "s_addc_u32 s81, s81, 0\n" \
  "s_add_u32 m0, s72, 26624\n" \
  "s_nop 0\n" \
  "global_load_lds_dwordx4 v11, s[80:81]\n" \
  "s_waitcnt vmcnt(4)\n" \
  "s_add_u32 s80, s80, 10240\n" \
  "s_addc_u32 s81, s81, 0\n" \
  "s_add_u32 m0, s72, 27648\n" \
  "s_nop 0\n" \
  "global_load_lds_dwordx4 v11, s[80:81]\n" \
  "s_waitcnt vmcnt(4)\n" \
  "s_waitcnt vmcnt(0)\n" \
  "ds_read_b64_tr_b16 a[28:29], v12 offset:0\n" \
  "ds_read_b64_tr_b16 a[30:31], v12 offset:2048\n" \
  "ds_read_b64_tr_b16 a[84:85], v13 offset:0\n" \
  "ds_read_b64_tr_b16 a[86:87], v13 offset:2048\n" \
  "ds_read_b64_tr_b16 a[140:141], v14 offset:0\n" \
  "ds_read_b64_tr_b16 a[142:143], v14 offset:2048\n" \
  "ds_read_b64_tr_b16 a[196:197], v15 offset:0\n" \
  "ds_read_b64_tr_b16 a[198:199], v15 offset:2048\n" \
  "ds_read_b64_tr_b16 a[32:33], v12 offset:4096\n" \
  "ds_read_b64_tr_b16 a[34:35], v12 offset:6144\n" \
  "ds_read_b64_tr_b16 a[88:89], v13 offset:4096\n" \
  "ds_read_b64_tr_b16 a[90:91], v13 offset:6144\n" \
  "ds_read_b64_tr_b16 a[144:145], v14 offset:4096\n" \
  "ds_read_b64_tr_b16 a[146:147], v14 offset:6144\n" \
  "ds_read_b64_tr_b16 a[200:201], v15 offset:4096\n" \
  "ds_read_b64_tr_b16 a[202:203], v15 offset:6144\n" \
  "ds_read_b64_tr_b16 a[36:37], v12 offset:8192\n" \
  "ds_read_b64_tr_b16 a[38:39], v12 offset:10240\n" \
  "ds_read_b64_tr_b16 a[92:93], v13 offset:8192\n" \
  "ds_read_b64_tr_b16 a[94:95], v13 offset:10240\n" \
  "ds_read_b64_tr_b16 a[148:149], v14 offset:8192\n" \
  "ds_read_b64_tr_b16 a[150:151], v14 offset:10240\n" \
  "ds_read_b64_tr_b16 a[204:205], v15 offset:8192\n" \
  "ds_read_b64_tr_b16 a[206:207], v15 offset:10240\n" \
  "ds_read_b64_tr_b16 a[40:41], v12 offset:12288\n" \
  "ds_read_b64_tr_b16 a[42:43], v12 offset:14336\n" \
  "ds_read_b64_tr_b16 a[96:97], v13 offset:12288\n" \
  "ds_read_b64_tr_b16 a[98:99], v13 offset:14336\n" \
  "ds_read_b64_tr_b16 a[152:153], v14 offset:12288\n" \
  "ds_read_b64_tr_b16 a[154:155], v14 offset:14336\n" \
  "ds_read_b64_tr_b16 a[208:209], v15 offset:12288\n" \
  "ds_read_b64_tr_b16 a[210:211], v15 offset:14336\n" \
  "ds_read_b64_tr_b16 a[44:45], v12 offset:16384\n" \
  "ds_read_b64_tr_b16 a[46:47], v12 offset:18432\n" \
  "ds_read_b64_tr_b16 a[100:101], v13 offset:16384\n" \
  "ds_read_b64_tr_b16 a[102:103], v13 offset:18432\n" \
  "ds_read_b64_tr_b16 a[156:157], v14 offset:16384\n" \
  "ds_read_b64_tr_b16 a[158:159], v14 offset:18432\n" \
  "ds_read_b64_tr_b16 a[212:213], v15 offset:16384\n" \
  "ds_read_b64_tr_b16 a[214:215], v15 offset:18432\n" \
  "ds_read_b64_tr_b16 a[48:49], v12 offset:20480\n" \
  "ds_read_b64_tr_b16 a[50:51], v12 offset:22528\n" \
  "ds_read_b64_tr_b16 a[104:105], v13 offset:20480\n" \
  "ds_read_b64_tr_b16 a[106:107], v13 offset:22528\n" \
  "ds_read_b64_tr_b16 a[160:161], v14 offset:20480\n" \
  "ds_read_b64_tr_b16 a[162:163], v14 offset:22528\n" \
  "ds_read_b64_tr_b16 a[216:217], v15 offset:20480\n" \
  "ds_read_b64_tr_b16 a[218:219], v15 offset:22528\n" \
  "ds_read_b64_tr_b16 a[52:53], v12 offset:24576\n" \
  "ds_read_b64_tr_b16 a[54:55], v12 offset:26624\n" \
  "ds_read_b64_tr_b16 a[108:109], v13 offset:24576\n" \
  "ds_read_b64_tr_b16 a[110:111], v13 offset:26624\n" \
  "ds_read_b64_tr_b16 a[164:165], v14 offset:24576\n" \
  "ds_read_b64_tr_b16 a[166:167], v14 offset:26624\n" \
  "ds_read_b64_tr_b16 a[220:221], v15 offset:24576\n" \
  "ds_read_b64_tr_b16 a[222:223], v15 offset:26624\n" \
  "s_waitcnt lgkmcnt(0)\n" \
  "s_cmp_lt_u32 s48, 2\n" \
  "s_cbranch_scc1 WNZ_%=\n" \
  "s_cmp_eq_u32 s48, 3\n" \
  "s_cbranch_scc1 WZ3_%=\n" \
  "v_accvgpr_write_b32 a52, 0\n" \
  "v_accvgpr_write_b32 a53, 0\n" \
  "v_accvgpr_write_b32 a54, 0\n" \
  "v_accvgpr_write_b32 a55, 0\n" \
  "v_accvgpr_write_b32 a108, 0\n" \
  "v_accvgpr_write_b32 a109, 0\n" \
  "v_accvgpr_write_b32 a110, 0\n" \
  "v_accvgpr_write_b32 a111, 0\n" \
  "v_accvgpr_write_b32 a164, 0\n" \
  "v_accvgpr_write_b32 a165, 0\n" \
  "v_accvgpr_write_b32 a166, 0\n" \
  "v_accvgpr_write_b32 a167, 0\n" \
  "v_accvgpr_write_b32 a220, 0\n" \
  "v_accvgpr_write_b32 a221, 0\n" \
  "v_accvgpr_write_b32 a222, 0\n" \
  "v_accvgpr_write_b32 a223, 0\n" \
  "s_branch WNZ_%=\n" \
  "WZ3_%=:\n" \
  "v_accvgpr_write_b32 a0, 0\n" \
  "v_accvgpr_write_b32 a1, 0\n" \
  "v_accvgpr_write_b32 a2, 0\n" \
  "v_accvgpr_write_b32 a3, 0\n" \
  "v_accvgpr_write_b32 a56, 0\n" \
  "v_accvgpr_write_b32 a57, 0\n" \
  "v_accvgpr_write_b32 a58, 0\n" \
  "v_accvgpr_write_b32 a59, 0\n" \
  "v_accvgpr_write_b32 a112, 0\n" \
  "v_accvgpr_write_b32 a113, 0\n" \
  "v_accvgpr_write_b32 a114, 0\n" \
  "v_accvgpr_write_b32 a115, 0\n" \
  "v_accvgpr_write_b32 a168, 0\n" \
  "v_accvgpr_write_b32 a169, 0\n" \
  "v_accvgpr_write_b32 a170, 0\n" \
  "v_accvgpr_write_b32 a171, 0\n" \
  "WNZ_%=:\n" \
  "s_cmp_lg_u32 s48, 0\n" \
  "s_cbranch_scc1 WHW1_%=\n" \
  "v_and_b32 v4, 3, v0\n" \
  "v_lshlrev_b32 v4, 8, v4\n" \
  "s_mov_b32 s52, 12583936\n" \
  "WHW0_%=:\n" \
  "buffer_load_dword v5, v4, s[36:39], s52 offen sc1\n" \
  "s_waitcnt vmcnt(0)\n" \
  "v_cmp_ne_u32 vcc, 32, v5\n" \
  "s_nop 4\n" \
  "s_cbranch_vccz WHW1_%=\n" \
  "s_sleep 8\n" \
  "s_add_u32 s62, s62, 1\n" \
  "s_cmp_gt_u32 s62, 131072\n" \
  "s_cbranch_scc1 WFAIL_%=\n" \
  "s_branch WHW0_%=\n" \
  "WHW1_%=:\n" \
  "s_barrier\n" \
  "s_mul_i32 s66, s65, 108\n" \
  "s_mul_i32 s52, s63, 54\n" \
  "s_add_u32 s66, s66, s52\n" \
  "s_add_u32 s66, s66, s73\n" \
  "s_lshl_b32 s66, s66, 10\n" \
  "s_add_u32 s66, s66, 25165824\n" \
  "s_cmp_lt_u32 s65, 4\n" \
  "s_cselect_b32 s53, 6635520, 0\n" \
  "s_add_u32 s54, s66, 0\n" \
  "buffer_load_dwordx4 v[192:195], v1, s[36:39], s54 offen offset:0 sc1\n" \
  "buffer_load_dwordx4 v[196:199], v1, s[36:39], s54 offen offset:1024 sc1\n" \
  "buffer_load_dwordx4 v[200:203], v1, s[36:39], s54 offen offset:2048 sc1\n" \
  "buffer_load_dwordx4 v[204:207], v1, s[36:39], s54 offen offset:3072 sc1\n" \
  "s_add_u32 s54, s66, 4096\n" \
  "buffer_load_dwordx4 v[208:211], v1, s[36:39], s54 offen offset:0 sc1\n" \
  "buffer_load_dwordx4 v[212:215], v1, s[36:39], s54 offen offset:1024 sc1\n" \
  "buffer_load_dwordx4 v[216:219], v1, s[36:39], s54 offen offset:2048 sc1\n" \
  "buffer_load_dwordx4 v[220:223], v1, s[36:39], s54 offen offset:3072 sc1\n" \
  "s_add_u32 s54, s66, 8192\n" \
  "buffer_load_dwordx4 v[224:227], v1, s[36:39], s54 offen offset:0 sc1\n" \
  "buffer_load_dwordx4 v[228:231], v1, s[36:39], s54 offen offset:1024 sc1\n" \
  "buffer_load_dwordx4 v[232:235], v1, s[36:39], s54 offen offset:2048 sc1\n" \
  "buffer_load_dwordx4 v[236:239], v1, s[36:39], s54 offen offset:3072 sc1\n" \
  "s_add_u32 s54, s66, 12288\n" \
  "buffer_load_dwordx4 v[240:243], v1, s[36:39], s54 offen offset:0 sc1\n" \
  "buffer_load_dwordx4 v[244:247], v1, s[36:39], s54 offen offset:1024 sc1\n" \
  "s_waitcnt vmcnt(13)\n" \
  "v_mfma_f32_16x16x32_bf16 v[16:19], a[0:3], v[192:195], 0\n" \
  "s_add_u32 s54, s66, 663552\n" \
  "buffer_load_dwordx4 v[248:251], v1, s[36:39], s54 offen offset:0 sc1\n" \
  "v_mfma_f32_16x16x32_bf16 v[20:23], a[56:59], v[192:195], 0\n" \
  "v_mfma_f32_16x16x32_bf16 v[24:27], a[112:115], v[192:195], 0\n" \
  "v_mfma_f32_16x16x32_bf16 v[28:31], a[168:171], v[192:195], 0\n" \
  "s_waitcnt vmcnt(13)\n" \
  "v_mfma_f32_16x16x32_bf16 v[16:19], a[4:7], v[196:199], v[16:19]\n" \
  "buffer_load_dwordx4 v[192:195], v1, s[36:39], s54 offen offset:1024 sc1\n" \
  "v_mfma_f32_16x16x32_bf16 v[20:23], a[60:63], v[196:199], v[20:23]\n" \
  "v_mfma_f32_16x16x32_bf16 v[24:27], a[116:119], v[196:199], v[24:27]\n" \
  "v_mfma_f32_16x16x32_bf16 v[28:31], a[172:175], v[196:199], v[28:31]\n" \
  "s_waitcnt vmcnt(13)\n" \
  "v_mfma_f32_16x16x32_bf16 v[16:19], a[8:11], v[200:203], v[16:19]\n" \
  "buffer_load_dwordx4 v[196:199], v1, s[36:39], s54 offen offset:2048 sc1\n" \
  "v_mfma_f32_16x16x32_bf16 v[20:23], a[64:67], v[200:203], v[20:23]\n" \
  "v_mfma_f32_16x16x32_bf16 v[24:27], a[120:123], v[200:203], v[24:27]\n" \
  "v_mfma_f32_16x16x32_bf16 v[28:31], a[176:179], v[200:203], v[28:31]\n" \
  "s_waitcnt vmcnt(13)\n" \
  "v_mfma_f32_16x16x32_bf16 v[16:19], a[12:15], v[204:207], v[16:19]\n" \
  "buffer_load_dwordx4 v[200:203], v1, s[36:39], s54 offen offset:3072 sc1\n" \
  "v_mfma_f32_16x16x32_bf16 v[20:23], a[68:71], v[204:207], v[20:23]\n" \
  "v_mfma_f32_16x16x32_bf16 v[24:27], a[124:127], v[204:207], v[24:27]\n" \
  "v_mfma_f32_16x16x32_bf16 v[28:31], a[180:183], v[204:207], v[28:31]\n" \
  "s_waitcnt vmcnt(13)\n" \
  "v_mfma_f32_16x16x32_bf16 v[16:19], a[16:19], v[208:211], v[16:19]\n" \
  "s_add_u32 s54, s66, 667648\n" \
  "buffer_load_dwordx4 v[204:207], v1, s[36:39], s54 offen offset:0 sc1\n" \
  "v_mfma_f32_16x16x32_bf16 v[20:23], a[72:75], v[208:211], v[20:23]\n" \
  "v_mfma_f32_16x16x32_bf16 v[24:27], a[128:131], v[208:211], v[24:27]\n" \
  "v_mfma_f32_16x16x32_bf16 v[28:31], a[184:187], v[208:211], v[28:31]\n" \
  "s_waitcnt vmcnt(13)\n" \
  "v_mfma_f32_16x16x32_bf16 v[16:19], a[20:23], v[212:215], v[16:19]\n" \
  "buffer_load_dwordx4 v[208:211], v1, s[36:39], s54 offen offset:1024 sc1\n" \
  "v_mfma_f32_16x16x32_bf16 v[20:23], a[76:79], v[212:215], v[20:23]\n" \
  "v_mfma_f32_16x16x32_bf16 v[24:27], a[132:135], v[212:215], v[24:27]\n" \
  "v_mfma_f32_16x16x32_bf16 v[28:31], a[188:191], v[212:215], v[28:31]\n" \
  "s_waitcnt vmcnt(13)\n" \
  "v_mfma_f32_16x16x32_bf16 v[16:19], a[24:27], v[216:219], v[16:19]\n" \
  "buffer_load_dwordx4 v[212:215], v1, s[36:39], s54 offen offset:2048 sc1\n" \
  "v_mfma_f32_16x16x32_bf16 v[20:23], a[80:83], v[216:219], v[20:23]\n" \
  "v_mfma_f32_16x16x32_bf16 v[24:27], a[136:139], v[216:219], v[24:27]\n" \
  "v_mfma_f32_16x16x32_bf16 v[28:31], a[192:195], v[216:219], v[28:31]\n" \
  "s_waitcnt vmcnt(13)\n" \
  "v_mfma_f32_16x16x32_bf16 v[16:19], a[28:31], v[220:223], v[16:19]\n" \
  "buffer_load_dwordx4 v[216:219], v1, s[36:39], s54 offen offset:3072 sc1\n" \
  "v_mfma_f32_16x16x32_bf16 v[20:23], a[84:87], v[220:223], v[20:23]\n" \
  "v_mfma_f32_16x16x32_bf16 v[24:27], a[140:143], v[220:223], v[24:27]\n" \
  "v_mfma_f32_16x16x32_bf16 v[28:31], a[196:199], v[220:223], v[28:31]\n" \
  "s_waitcnt vmcnt(13)\n" \
  "v_mfma_f32_16x16x32_bf16 v[16:19], a[32:35], v[224:227], v[16:19]\n" \
  "s_add_u32 s54, s66, 671744\n" \
  "buffer_load_dwordx4 v[220:223], v1, s[36:39], s54 offen offset:0 sc1\n" \
  "v_mfma_f32_16x16x32_bf16 v[20:23], a[88:91], v[224:227], v[20:23]\n" \
  "v_mfma_f32_16x16x32_bf16 v[24:27], a[144:147], v[224:227], v[24:27]\n" \
  "v_mfma_f32_16x16x32_bf16 v[28:31], a[200:203], v[224:227], v[28:31]\n" \
  "s_waitcnt vmcnt(13)\n" \
  "v_mfma_f32_16x16x32_bf16 v[16:19], a[36:39], v[228:231], v[16:19]\n" \
  "buffer_load_dwordx4 v[224:227], v1, s[36:39], s54 offen offset:1024 sc1\n" \
  "v_mfma_f32_16x16x32_bf16 v[20:23], a[92:95], v[228:231], v[20:23]\n" \
  "v_mfma_f32_16x16x32_bf16 v[24:27], a[148:151], v[228:231], v[24:27]\n" \
  "v_mfma_f32_16x16x32_bf16 v[28:31], a[204:207], v[228:231], v[28:31]\n" \
  "s_waitcnt vmcnt(13)\n" \
  "v_mfma_f32_16x16x32_bf16 v[16:19], a[40:43], v[232:235], v[16:19]\n" \
  "buffer_load_dwordx4 v[228:231], v1, s[36:39], s54 offen offset:2048 sc1\n" \
  "v_mfma_f32_16x16x32_bf16 v[20:23], a[96:99], v[232:235], v[20:23]\n" \
  "v_mfma_f32_16x16x32_bf16 v[24:27], a[152:155], v[232:235], v[24:27]\n" \
  "v_mfma_f32_16x16x32_bf16 v[28:31], a[208:211], v[232:235], v[28:31]\n" \
  "s_waitcnt vmcnt(13)\n" \
  "v_mfma_f32_16x16x32_bf16 v[16:19], a[44:47], v[236:239], v[16:19]\n" \
  "buffer_load_dwordx4 v[232:235], v1, s[36:39], s54 offen offset:3072 sc1\n" \
  "v_mfma_f32_16x16x32_bf16 v[20:23], a[100:103], v[236:239], v[20:23]\n" \
  "v_mfma_f32_16x16x32_bf16 v[24:27], a[156:159], v[236:239], v[24:27]\n" \
  "v_mfma_f32_16x16x32_bf16 v[28:31], a[212:215], v[236:239], v[28:31]\n" \
  "s_waitcnt vmcnt(13)\n" \
  "v_mfma_f32_16x16x32_bf16 v[16:19], a[48:51], v[240:243], v[16:19]\n" \
  "s_add_u32 s54, s66, 675840\n" \
  "buffer_load_dwordx4 v[236:239], v1, s[36:39], s54 offen offset:0 sc1\n" \
  "v_mfma_f32_16x16x32_bf16 v[20:23], a[104:107], v[240:243], v[20:23]\n" \
  "v_mfma_f32_16x16x32_bf16 v[24:27], a[160:163], v[240:243], v[24:27]\n" \
  "v_mfma_f32_16x16x32_bf16 v[28:31], a[216:219], v[240:243], v[28:31]\n" \
  "s_waitcnt vmcnt(13)\n" \
  "v_mfma_f32_16x16x32_bf16 v[16:19], a[52:55], v[244:247], v[16:19]\n" \
  "buffer_load_dwordx4 v[240:243], v1, s[36:39], s54 offen offset:1024 sc1\n" \
  "v_mfma_f32_16x16x32_bf16 v[20:23], a[108:111], v[244:247], v[20:23]\n" \
  "v_mfma_f32_16x16x32_bf16 v[24:27], a[164:167], v[244:247], v[24:27]\n" \
  "v_mfma_f32_16x16x32_bf16 v[28:31], a[220:223], v[244:247], v[28:31]\n" \
  "s_waitcnt vmcnt(13)\n" \
  "v_mfma_f32_16x16x32_bf16 v[32:35], a[0:3], v[248:251], 0\n" \
  "s_add_u32 s54, s66, 1327104\n" \
  "buffer_load_dwordx4 v[244:247], v1, s[36:39], s54 offen offset:0 sc1\n" \
  "v_mfma_f32_16x16x32_bf16 v[36:39], a[56:59], v[248:251], 0\n" \
  "v_mfma_f32_16x16x32_bf16 v[40:43], a[112:115], v[248:251], 0\n" \
  "v_mfma_f32_16x16x32_bf16 v[44:47], a[168:171], v[248:251], 0\n" \
  "s_waitcnt vmcnt(13)\n" \
  "v_mfma_f32_16x16x32_bf16 v[32:35], a[4:7], v[192:195], v[32:35]\n" \
  "buffer_load_dwordx4 v[248:251], v1, s[36:39], s54 offen offset:1024 sc1\n" \
  "v_mfma_f32_16x16x32_bf16 v[36:39], a[60:63], v[192:195], v[36:39]\n" \
  "v_mfma_f32_16x16x32_bf16 v[40:43], a[116:119], v[192:195], v[40:43]\n" \
  "v_mfma_f32_16x16x32_bf16 v[44:47], a[172:175], v[192:195], v[44:47]\n" \
  "s_waitcnt vmcnt(13)\n" \
  "v_mfma_f32_16x16x32_bf16 v[32:35], a[8:11], v[196:199], v[32:35]\n" \
  "buffer_load_dwordx4 v[192:195], v1, s[36:39], s54 offen offset:2048 sc1\n" \
  "v_mfma_f32_16x16x32_bf16 v[36:39], a[64:67], v[196:199], v[36:39]\n" \
  "v_mfma_f32_16x16x32_bf16 v[40:43], a[120:123], v[196:199], v[40:43]\n" \
  "v_mfma_f32_16x16x32_bf16 v[44:47], a[176:179], v[196:199], v[44:47]\n" \
  "s_waitcnt vmcnt(13)\n" \
  "v_mfma_f32_16x16x32_bf16 v[32:35], a[12:15], v[200:203], v[32:35]\n" \
  "buffer_load_dwordx4 v[196:199], v1, s[36:39], s54 offen offset:3072 sc1\n" \
  "v_mfma_f32_16x16x32_bf16 v[36:39], a[68:71], v[200:203], v[36:39]\n" \
  "v_mfma_f32_16x16x32_bf16 v[40:43], a[124:127], v[200:203], v[40:43]\n" \
  "v_mfma_f32_16x16x32_bf16 v[44:47], a[180:183], v[200:203], v[44:47]\n" \
  "s_waitcnt vmcnt(13)\n" \
  "v_mfma_f32_16x16x32_bf16 v[32:35], a[16:19], v[204:207], v[32:35]\n" \
  "s_add_u32 s54, s66, 1331200\n" \
  "buffer_load_dwordx4 v[200:203], v1, s[36:39], s54 offen offset:0 sc1\n" \
  "v_mfma_f32_16x16x32_bf16 v[36:39], a[72:75], v[204:207], v[36:39]\n" \
  "v_mfma_f32_16x16x32_bf16 v[40:43], a[128:131], v[204:207], v[40:43]\n" \
  "v_mfma_f32_16x16x32_bf16 v[44:47], a[184:187], v[204:207], v[44:47]\n" \
  "s_waitcnt vmcnt(13)\n" \
  "v_mfma_f32_16x16x32_bf16 v[32:35], a[20:23], v[208:211], v[32:35]\n" \
  "buffer_load_dwordx4 v[204:207], v1, s[36:39], s54 offen offset:1024 sc1\n" \
  "v_mfma_f32_16x16x32_bf16 v[36:39], a[76:79], v[208:211], v[36:39]\n" \
  "v_mfma_f32_16x16x32_bf16 v[40:43], a[132:135], v[208:211], v[40:43]\n" \
  "v_mfma_f32_16x16x32_bf16 v[44:47], a[188:191], v[208:211], v[44:47]\n" \
  "s_waitcnt vmcnt(13)\n" \
  "v_mfma_f32_16x16x32_bf16 v[32:35], a[24:27], v[212:215], v[32:35]\n" \
  "buffer_load_dwordx4 v[208:211], v1, s[36:39], s54 offen offset:2048 sc1\n" \
  "v_mfma_f32_16x16x32_bf16 v[36:39], a[80:83], v[212:215], v[36:39]\n" \
  "v_mfma_f32_16x16x32_bf16 v[40:43], a[136:139], v[212:215], v[40:43]\n" \
  "v_mfma_f32_16x16x32_bf16 v[44:47], a[192:195], v[212:215], v[44:47]\n" \
  "s_waitcnt vmcnt(13)\n" \
  "v_mfma_f32_16x16x32_bf16 v[32:35], a[28:31], v[216:219], v[32:35]\n" \
  "buffer_load_dwordx4 v[212:215], v1, s[36:39], s54 offen offset:3072 sc1\n" \
  "v_mfma_f32_16x16x32_bf16 v[36:39], a[84:87], v[216:219], v[36:39]\n" \
  "v_mfma_f32_16x16x32_bf16 v[40:43], a[140:143], v[216:219], v[40:43]\n" \
  "v_mfma_f32_16x16x32_bf16 v[44:47], a[196:199], v[216:219], v[44:47]\n" \
  "s_waitcnt vmcnt(13)\n" \
  "v_mfma_f32_16x16x32_bf16 v[32:35], a[32:35], v[220:223], v[32:35]\n" \
  "s_add_u32 s54, s66, 1335296\n" \
  "buffer_load_dwordx4 v[216:219], v1, s[36:39], s54 offen offset:0 sc1\n" \
  "v_mfma_f32_16x16x32_bf16 v[36:39], a[88:91], v[220:223], v[36:39]\n" \
  "v_mfma_f32_16x16x32_bf16 v[40:43], a[144:147], v[220:223], v[40:43]\n" \
  "v_mfma_f32_16x16x32_bf16 v[44:47], a[200:203], v[220:223], v[44:47]\n" \
  "s_waitcnt vmcnt(13)\n" \
  "v_mfma_f32_16x16x32_bf16 v[32:35], a[36:39], v[224:227], v[32:35]\n" \
  "buffer_load_dwordx4 v[220:223], v1, s[36:39], s54 offen offset:1024 sc1\n" \
  "v_mfma_f32_16x16x32_bf16 v[36:39], a[92:95], v[224:227], v[36:39]\n" \
  "v_mfma_f32_16x16x32_bf16 v[40:43], a[148:151], v[224:227], v[40:43]\n" \
  "v_mfma_f32_16x16x32_bf16 v[44:47], a[204:207], v[224:227], v[44:47]\n" \
  "s_waitcnt vmcnt(13)\n" \
  "v_mfma_f32_16x16x32_bf16 v[32:35], a[40:43], v[228:231], v[32:35]\n" \
  "buffer_load_dwordx4 v[224:227], v1, s[36:39], s54 offen offset:2048 sc1\n" \
  "v_mfma_f32_16x16x32_bf16 v[36:39], a[96:99], v[228:231], v[36:39]\n" \
  "v_mfma_f32_16x16x32_bf16 v[40:43], a[152:155], v[228:231], v[40:43]\n" \
  "v_mfma_f32_16x16x32_bf16 v[44:47], a[208:211], v[228:231], v[44:47]\n" \
  "s_waitcnt vmcnt(13)\n" \
  "v_mfma_f32_16x16x32_bf16 v[32:35], a[44:47], v[232:235], v[32:35]\n" \
  "buffer_load_dwordx4 v[228:231], v1, s[36:39], s54 offen offset:3072 sc1\n" \
  "v_mfma_f32_16x16x32_bf16 v[36:39], a[100:103], v[232:235], v[36:39]\n" \
  "v_mfma_f32_16x16x32_bf16 v[40:43], a[156:159], v[232:235], v[40:43]\n" \
  "v_mfma_f32_16x16x32_bf16 v[44:47], a[212:215], v[232:235], v[44:47]\n" \
  "s_waitcnt vmcnt(13)\n" \
  "v_mfma_f32_16x16x32_bf16 v[32:35], a[48:51], v[236:239], v[32:35]\n" \
  "s_add_u32 s54, s66, 1339392\n" \
  "buffer_load_dwordx4 v[232:235], v1, s[36:39], s54 offen offset:0 sc1\n" \
  "v_mfma_f32_16x16x32_bf16 v[36:39], a[104:107], v[236:239], v[36:39]\n" \
  "v_mfma_f32_16x16x32_bf16 v[40:43], a[160:163], v[236:239], v[40:43]\n" \
  "v_mfma_f32_16x16x32_bf16 v[44:47], a[216:219], v[236:239], v[44:47]\n" \
  "s_waitcnt vmcnt(13)\n" \
  "v_mfma_f32_16x16x32_bf16 v[32:35], a[52:55], v[240:243], v[32:35]\n" \
  "buffer_load_dwordx4 v[236:239], v1, s[36:39], s54 offen offset:1024 sc1\n" \
  "v_mfma_f32_16x16x32_bf16 v[36:39], a[108:111], v[240:243], v[36:39]\n" \
  "v_mfma_f32_16x16x32_bf16 v[40:43], a[164:167], v[240:243], v[40:43]\n" \
  "v_mfma_f32_16x16x32_bf16 v[44:47], a[220:223], v[240:243], v[44:47]\n" \
  "s_waitcnt vmcnt(13)\n" \
  "v_mfma_f32_16x16x32_bf16 v[48:51], a[0:3], v[244:247], 0\n" \
  "s_add_u32 s54, s66, 1990656\n" \
  "buffer_load_dwordx4 v[240:243], v1, s[36:39], s54 offen offset:0 sc1\n" \
  "v_mfma_f32_16x16x32_bf16 v[52:55], a[56:59], v[244:247], 0\n" \
  "v_mfma_f32_16x16x32_bf16 v[56:59], a[112:115], v[244:247], 0\n" \
  "v_mfma_f32_16x16x32_bf16 v[60:63], a[168:171], v[244:247], 0\n" \
  "s_waitcnt vmcnt(13)\n" \
  "v_mfma_f32_16x16x32_bf16 v[48:51], a[4:7], v[248:251], v[48:51]\n" \
  "buffer_load_dwordx4 v[244:247], v1, s[36:39], s54 offen offset:1024 sc1\n" \
  "v_mfma_f32_16x16x32_bf16 v[52:55], a[60:63], v[248:251], v[52:55]\n" \
  "v_mfma_f32_16x16x32_bf16 v[56:59], a[116:119], v[248:251], v[56:59]\n" \
  "v_mfma_f32_16x16x32_bf16 v[60:63], a[172:175], v[248:251], v[60:63]\n" \
  "s_waitcnt vmcnt(13)\n" \
  "v_mfma_f32_16x16x32_bf16 v[48:51], a[8:11], v[192:195], v[48:51]\n" \
  "buffer_load_dwordx4 v[248:251], v1, s[36:39], s54 offen offset:2048 sc1\n" \
  "v_mfma_f32_16x16x32_bf16 v[52:55], a[64:67], v[192:195], v[52:55]\n" \
  "v_mfma_f32_16x16x32_bf16 v[56:59], a[120:123], v[192:195], v[56:59]\n" \
  "v_mfma_f32_16x16x32_bf16 v[60:63], a[176:179], v[192:195], v[60:63]\n" \
  "s_waitcnt vmcnt(13)\n" \
  "v_mfma_f32_16x16x32_bf16 v[48:51], a[12:15], v[196:199], v[48:51]\n" \
  "buffer_load_dwordx4 v[192:195], v1, s[36:39], s54 offen offset:3072 sc1\n" \
  "v_mfma_f32_16x16x32_bf16 v[52:55], a[68:71], v[196:199], v[52:55]\n" \
  "v_mfma_f32_16x16x32_bf16 v[56:59], a[124:127], v[196:199], v[56:59]\n" \
  "v_mfma_f32_16x16x32_bf16 v[60:63], a[180:183], v[196:199], v[60:63]\n" \
  "s_waitcnt vmcnt(13)\n" \
  "v_mfma_f32_16x16x32_bf16 v[48:51], a[16:19], v[200:203], v[48:51]\n" \
  "s_add_u32 s54, s66, 1994752\n" \
  "buffer_load_dwordx4 v[196:199], v1, s[36:39], s54 offen offset:0 sc1\n" \
  "v_mfma_f32_16x16x32_bf16 v[52:55], a[72:75], v[200:203], v[52:55]\n" \
  "v_mfma_f32_16x16x32_bf16 v[56:59], a[128:131], v[200:203], v[56:59]\n" \
  "v_mfma_f32_16x16x32_bf16 v[60:63], a[184:187], v[200:203], v[60:63]\n" \
  "s_waitcnt vmcnt(13)\n" \
  "v_mfma_f32_16x16x32_bf16 v[48:51], a[20:23], v[204:207], v[48:51]\n" \
  "buffer_load_dwordx4 v[200:203], v1, s[36:39], s54 offen offset:1024 sc1\n" \
  "v_mfma_f32_16x16x32_bf16 v[52:55], a[76:79], v[204:207], v[52:55]\n" \
  "v_mfma_f32_16x16x32_bf16 v[56:59], a[132:135], v[204:207], v[56:59]\n" \
  "v_mfma_f32_16x16x32_bf16 v[60:63], a[188:191], v[204:207], v[60:63]\n" \
  "s_waitcnt vmcnt(13)\n" \
  "v_mfma_f32_16x16x32_bf16 v[48:51], a[24:27], v[208:211], v[48:51]\n" \
  "buffer_load_dwordx4 v[204:207], v1, s[36:39], s54 offen offset:2048 sc1\n" \
  "v_mfma_f32_16x16x32_bf16 v[52:55], a[80:83], v[208:211], v[52:55]\n" \
  "v_mfma_f32_16x16x32_bf16 v[56:59], a[136:139], v[208:211], v[56:59]\n" \
  "v_mfma_f32_16x16x32_bf16 v[60:63], a[192:195], v[208:211], v[60:63]\n" \
  "s_waitcnt vmcnt(13)\n" \
  "v_mfma_f32_16x16x32_bf16 v[48:51], a[28:31], v[212:215], v[48:51]\n" \
  "buffer_load_dwordx4 v[208:211], v1, s[36:39], s54 offen offset:3072 sc1\n" \
  "v_mfma_f32_16x16x32_bf16 v[52:55], a[84:87], v[212:215], v[52:55]\n" \
  "v_mfma_f32_16x16x32_bf16 v[56:59], a[140:143], v[212:215], v[56:59]\n" \
  "v_mfma_f32_16x16x32_bf16 v[60:63], a[196:199], v[212:215], v[60:63]\n" \
  "s_waitcnt vmcnt(13)\n" \
  "v_mfma_f32_16x16x32_bf16 v[48:51], a[32:35], v[216:219], v[48:51]\n" \
  "s_add_u32 s54, s66, 1998848\n" \
  "buffer_load_dwordx4 v[212:215], v1, s[36:39], s54 offen offset:0 sc1\n" \
  "v_mfma_f32_16x16x32_bf16 v[52:55], a[88:91], v[216:219], v[52:55]\n" \
  "v_mfma_f32_16x16x32_bf16 v[56:59], a[144:147], v[216:219], v[56:59]\n" \
  "v_mfma_f32_16x16x32_bf16 v[60:63], a[200:203], v[216:219], v[60:63]\n" \
  "s_waitcnt vmcnt(13)\n" \
  "v_mfma_f32_16x16x32_bf16 v[48:51], a[36:39], v[220:223], v[48:51]\n" \
  "buffer_load_dwordx4 v[216:219], v1, s[36:39], s54 offen offset:1024 sc1\n" \
  "v_mfma_f32_16x16x32_bf16 v[52:55], a[92:95], v[220:223], v[52:55]\n" \
  "v_mfma_f32_16x16x32_bf16 v[56:59], a[148:151], v[220:223], v[56:59]\n" \
  "v_mfma_f32_16x16x32_bf16 v[60:63], a[204:207], v[220:223], v[60:63]\n" \
  "s_waitcnt vmcnt(13)\n" \
  "v_mfma_f32_16x16x32_bf16 v[48:51], a[40:43], v[224:227], v[48:51]\n" \
  "buffer_load_dwordx4 v[220:223], v1, s[36:39], s54 offen offset:2048 sc1\n" \
  "v_mfma_f32_16x16x32_bf16 v[52:55], a[96:99], v[224:227], v[52:55]\n" \
  "v_mfma_f32_16x16x32_bf16 v[56:59], a[152:155], v[224:227], v[56:59]\n" \
  "v_mfma_f32_16x16x32_bf16 v[60:63], a[208:211], v[224:227], v[60:63]\n" \
  "s_waitcnt vmcnt(13)\n" \
  "v_mfma_f32_16x16x32_bf16 v[48:51], a[44:47], v[228:231], v[48:51]\n" \
  "buffer_load_dwordx4 v[224:227], v1, s[36:39], s54 offen offset:3072 sc1\n" \
  "v_mfma_f32_16x16x32_bf16 v[52:55], a[100:103], v[228:231], v[52:55]\n" \
  "v_mfma_f32_16x16x32_bf16 v[56:59], a[156:159], v[228:231], v[56:59]\n" \
  "v_mfma_f32_16x16x32_bf16 v[60:63], a[212:215], v[228:231], v[60:63]\n" \
  "s_waitcnt vmcnt(13)\n" \
  "v_mfma_f32_16x16x32_bf16 v[48:51], a[48:51], v[232:235], v[48:51]\n" \
  "s_add_u32 s54, s66, 2002944\n" \
  "buffer_load_dwordx4 v[228:231], v1, s[36:39], s54 offen offset:0 sc1\n" \
  "v_mfma_f32_16x16x32_bf16 v[52:55], a[104:107], v[232:235], v[52:55]\n" \
  "v_mfma_f32_16x16x32_bf16 v[56:59], a[160:163], v[232:235], v[56:59]\n" \
  "v_mfma_f32_16x16x32_bf16 v[60:63], a[216:219], v[232:235], v[60:63]\n" \
  "s_waitcnt vmcnt(13)\n" \
  "v_mfma_f32_16x16x32_bf16 v[48:51], a[52:55], v[236:239], v[48:51]\n" \
  "buffer_load_dwordx4 v[232:235], v1, s[36:39], s54 offen offset:1024 sc1\n" \
  "v_mfma_f32_16x16x32_bf16 v[52:55], a[108:111], v[236:239], v[52:55]\n" \
  "v_mfma_f32_16x16x32_bf16 v[56:59], a[164:167], v[236:239], v[56:59]\n" \
  "v_mfma_f32_16x16x32_bf16 v[60:63], a[220:223], v[236:239], v[60:63]\n" \
  "s_waitcnt vmcnt(13)\n" \
  "v_mfma_f32_16x16x32_bf16 v[64:67], a[0:3], v[240:243], 0\n" \
  "s_add_u32 s54, s66, 2654208\n" \
  "buffer_load_dwordx4 v[236:239], v1, s[36:39], s54 offen offset:0 sc1\n" \
  "v_mfma_f32_16x16x32_bf16 v[68:71], a[56:59], v[240:243], 0\n" \
  "v_mfma_f32_16x16x32_bf16 v[72:75], a[112:115], v[240:243], 0\n" \
  "v_mfma_f32_16x16x32_bf16 v[76:79], a[168:171], v[240:243], 0\n" \
  "s_waitcnt vmcnt(13)\n" \
  "v_mfma_f32_16x16x32_bf16 v[64:67], a[4:7], v[244:247], v[64:67]\n" \
  "buffer_load_dwordx4 v[240:243], v1, s[36:39], s54 offen offset:1024 sc1\n" \
  "v_mfma_f32_16x16x32_bf16 v[68:71], a[60:63], v[244:247], v[68:71]\n" \
  "v_mfma_f32_16x16x32_bf16 v[72:75], a[116:119], v[244:247], v[72:75]\n" \
  "v_mfma_f32_16x16x32_bf16 v[76:79], a[172:175], v[244:247], v[76:79]\n" \
  "s_waitcnt vmcnt(13)\n" \
  "v_mfma_f32_16x16x32_bf16 v[64:67], a[8:11], v[248:251], v[64:67]\n" \
  "buffer_load_dwordx4 v[244:247], v1, s[36:39], s54 offen offset:2048 sc1\n" \
  "v_mfma_f32_16x16x32_bf16 v[68:71], a[64:67], v[248:251], v[68:71]\n" \
  "v_mfma_f32_16x16x32_bf16 v[72:75], a[120:123], v[248:251], v[72:75]\n" \
  "v_mfma_f32_16x16x32_bf16 v[76:79], a[176:179], v[248:251], v[76:79]\n" \
  "s_waitcnt vmcnt(13)\n" \
  "v_mfma_f32_16x16x32_bf16 v[64:67], a[12:15], v[192:195], v[64:67]\n" \
  "buffer_load_dwordx4 v[248:251], v1, s[36:39], s54 offen offset:3072 sc1\n" \
  "v_mfma_f32_16x16x32_bf16 v[68:71], a[68:71], v[192:195], v[68:71]\n" \
  "v_mfma_f32_16x16x32_bf16 v[72:75], a[124:127], v[192:195], v[72:75]\n" \
  "v_mfma_f32_16x16x32_bf16 v[76:79], a[180:183], v[192:195], v[76:79]\n" \
  "s_waitcnt vmcnt(13)\n" \
  "v_mfma_f32_16x16x32_bf16 v[64:67], a[16:19], v[196:199], v[64:67]\n" \
  "s_add_u32 s54, s66, 2658304\n" \
  "buffer_load_dwordx4 v[192:195], v1, s[36:39], s54 offen offset:0 sc1\n" \
  "v_mfma_f32_16x16x32_bf16 v[68:71], a[72:75], v[196:199], v[68:71]\n" \
  "v_mfma_f32_16x16x32_bf16 v[72:75], a[128:131], v[196:199], v[72:75]\n" \
  "v_mfma_f32_16x16x32_bf16 v[76:79], a[184:187], v[196:199], v[76:79]\n" \
  "s_waitcnt vmcnt(13)\n" \
  "v_mfma_f32_16x16x32_bf16 v[64:67], a[20:23], v[200:203], v[64:67]\n" \
  "buffer_load_dwordx4 v[196:199], v1, s[36:39], s54 offen offset:1024 sc1\n" \
  "v_mfma_f32_16x16x32_bf16 v[68:71], a[76:79], v[200:203], v[68:71]\n" \
  "v_mfma_f32_16x16x32_bf16 v[72:75], a[132:135], v[200:203], v[72:75]\n" \
  "v_mfma_f32_16x16x32_bf16 v[76:79], a[188:191], v[200:203], v[76:79]\n" \
  "s_waitcnt vmcnt(13)\n" \
  "v_mfma_f32_16x16x32_bf16 v[64:67], a[24:27], v[204:207], v[64:67]\n" \
  "buffer_load_dwordx4 v[200:203], v1, s[36:39], s54 offen offset:2048 sc1\n" \
  "v_mfma_f32_16x16x32_bf16 v[68:71], a[80:83], v[204:207], v[68:71]\n" \
  "v_mfma_f32_16x16x32_bf16 v[72:75], a[136:139], v[204:207], v[72:75]\n" \
  "v_mfma_f32_16x16x32_bf16 v[76:79], a[192:195], v[204:207], v[76:79]\n" \
  "s_waitcnt vmcnt(13)\n" \
  "v_mfma_f32_16x16x32_bf16 v[64:67], a[28:31], v[208:211], v[64:67]\n" \
  "buffer_load_dwordx4 v[204:207], v1, s[36:39], s54 offen offset:3072 sc1\n" \
  "v_mfma_f32_16x16x32_bf16 v[68:71], a[84:87], v[208:211], v[68:71]\n" \
  "v_mfma_f32_16x16x32_bf16 v[72:75], a[140:143], v[208:211], v[72:75]\n" \
  "v_mfma_f32_16x16x32_bf16 v[76:79], a[196:199], v[208:211], v[76:79]\n" \
  "s_waitcnt vmcnt(13)\n" \
  "v_mfma_f32_16x16x32_bf16 v[64:67], a[32:35], v[212:215], v[64:67]\n" \
  "s_add_u32 s54, s66, 2662400\n" \
  "buffer_load_dwordx4 v[208:211], v1, s[36:39], s54 offen offset:0 sc1\n" \
  "v_mfma_f32_16x16x32_bf16 v[68:71], a[88:91], v[212:215], v[68:71]\n" \
  "v_mfma_f32_16x16x32_bf16 v[72:75], a[144:147], v[212:215], v[72:75]\n" \
  "v_mfma_f32_16x16x32_bf16 v[76:79], a[200:203], v[212:215], v[76:79]\n" \
  "s_waitcnt vmcnt(13)\n" \
  "v_mfma_f32_16x16x32_bf16 v[64:67], a[36:39], v[216:219], v[64:67]\n" \
  "buffer_load_dwordx4 v[212:215], v1, s[36:39], s54 offen offset:1024 sc1\n" \
  "v_mfma_f32_16x16x32_bf16 v[68:71], a[92:95], v[216:219], v[68:71]\n" \
  "v_mfma_f32_16x16x32_bf16 v[72:75], a[148:151], v[216:219], v[72:75]\n" \
  "v_mfma_f32_16x16x32_bf16 v[76:79], a[204:207], v[216:219], v[76:79]\n" \
  "s_waitcnt vmcnt(13)\n" \
  "v_mfma_f32_16x16x32_bf16 v[64:67], a[40:43], v[220:223], v[64:67]\n" \
  "buffer_load_dwordx4 v[216:219], v1, s[36:39], s54 offen offset:2048 sc1\n" \
  "v_mfma_f32_16x16x32_bf16 v[68:71], a[96:99], v[220:223], v[68:71]\n" \
  "v_mfma_f32_16x16x32_bf16 v[72:75], a[152:155], v[220:223], v[72:75]\n" \
  "v_mfma_f32_16x16x32_bf16 v[76:79], a[208:211], v[220:223], v[76:79]\n" \
  "s_waitcnt vmcnt(13)\n" \
  "v_mfma_f32_16x16x32_bf16 v[64:67], a[44:47], v[224:227], v[64:67]\n" \
  "buffer_load_dwordx4 v[220:223], v1, s[36:39], s54 offen offset:3072 sc1\n" \
  "v_mfma_f32_16x16x32_bf16 v[68:71], a[100:103], v[224:227], v[68:71]\n" \
  "v_mfma_f32_16x16x32_bf16 v[72:75], a[156:159], v[224:227], v[72:75]\n" \
  "v_mfma_f32_16x16x32_bf16 v[76:79], a[212:215], v[224:227], v[76:79]\n" \
  "s_waitcnt vmcnt(13)\n" \
  "v_mfma_f32_16x16x32_bf16 v[64:67], a[48:51], v[228:231], v[64:67]\n" \
  "s_add_u32 s54, s66, 2666496\n" \
  "buffer_load_dwordx4 v[224:227], v1, s[36:39], s54 offen offset:0 sc1\n" \
  "v_mfma_f32_16x16x32_bf16 v[68:71], a[104:107], v[228:231], v[68:71]\n" \
  "v_mfma_f32_16x16x32_bf16 v[72:75], a[160:163], v[228:231], v[72:75]\n" \
  "v_mfma_f32_16x16x32_bf16 v[76:79], a[216:219], v[228:231], v[76:79]\n" \
  "s_waitcnt vmcnt(13)\n" \
  "v_mfma_f32_16x16x32_bf16 v[64:67], a[52:55], v[232:235], v[64:67]\n" \
  "buffer_load_dwordx4 v[228:231], v1, s[36:39], s54 offen offset:1024 sc1\n" \
  "v_mfma_f32_16x16x32_bf16 v[68:71], a[108:111], v[232:235], v[68:71]\n" \
  "v_mfma_f32_16x16x32_bf16 v[72:75], a[164:167], v[232:235], v[72:75]\n" \
  "v_mfma_f32_16x16x32_bf16 v[76:79], a[220:223], v[232:235], v[76:79]\n" \
  "s_waitcnt vmcnt(13)\n" \
  "v_mfma_f32_16x16x32_bf16 v[80:83], a[0:3], v[236:239], 0\n" \
  "s_add_u32 s54, s66, 3317760\n" \
  "buffer_load_dwordx4 v[232:235], v1, s[36:39], s54 offen offset:0 sc1\n" \
  "v_mfma_f32_16x16x32_bf16 v[84:87], a[56:59], v[236:239], 0\n" \
  "v_mfma_f32_16x16x32_bf16 v[88:91], a[112:115], v[236:239], 0\n" \
  "v_mfma_f32_16x16x32_bf16 v[92:95], a[168:171], v[236:239], 0\n" \
  "s_waitcnt vmcnt(13)\n" \
  "v_mfma_f32_16x16x32_bf16 v[80:83], a[4:7], v[240:243], v[80:83]\n" \
  "buffer_load_dwordx4 v[236:239], v1, s[36:39], s54 offen offset:1024 sc1\n" \
  "v_mfma_f32_16x16x32_bf16 v[84:87], a[60:63], v[240:243], v[84:87]\n" \
  "v_mfma_f32_16x16x32_bf16 v[88:91], a[116:119], v[240:243], v[88:91]\n" \
  "v_mfma_f32_16x16x32_bf16 v[92:95], a[172:175], v[240:243], v[92:95]\n" \
  "s_waitcnt vmcnt(13)\n" \
  "v_mfma_f32_16x16x32_bf16 v[80:83], a[8:11], v[244:247], v[80:83]\n" \
  "buffer_load_dwordx4 v[240:243], v1, s[36:39], s54 offen offset:2048 sc1\n" \
  "v_mfma_f32_16x16x32_bf16 v[84:87], a[64:67], v[244:247], v[84:87]\n" \
  "v_mfma_f32_16x16x32_bf16 v[88:91], a[120:123], v[244:247], v[88:91]\n" \
  "v_mfma_f32_16x16x32_bf16 v[92:95], a[176:179], v[244:247], v[92:95]\n" \
  "s_waitcnt vmcnt(13)\n" \
  "v_mfma_f32_16x16x32_bf16 v[80:83], a[12:15], v[248:251], v[80:83]\n" \
  "buffer_load_dwordx4 v[244:247], v1, s[36:39], s54 offen offset:3072 sc1\n" \
  "v_mfma_f32_16x16x32_bf16 v[84:87], a[68:71], v[248:251], v[84:87]\n" \
  "v_mfma_f32_16x16x32_bf16 v[88:91], a[124:127], v[248:251], v[88:91]\n" \
  "v_mfma_f32_16x16x32_bf16 v[92:95], a[180:183], v[248:251], v[92:95]\n" \
  "s_waitcnt vmcnt(13)\n" \
  "v_mfma_f32_16x16x32_bf16 v[80:83], a[16:19], v[192:195], v[80:83]\n" \
  "s_add_u32 s54, s66, 3321856\n" \
  "buffer_load_dwordx4 v[248:251], v1, s[36:39], s54 offen offset:0 sc1\n" \
  "v_mfma_f32_16x16x32_bf16 v[84:87], a[72:75], v[192:195], v[84:87]\n" \
  "v_mfma_f32_16x16x32_bf16 v[88:91], a[128:131], v[192:195], v[88:91]\n" \
  "v_mfma_f32_16x16x32_bf16 v[92:95], a[184:187], v[192:195], v[92:95]\n" \
  "s_waitcnt vmcnt(13)\n" \
  "v_mfma_f32_16x16x32_bf16 v[80:83], a[20:23], v[196:199], v[80:83]\n" \
  "buffer_load_dwordx4 v[192:195], v1, s[36:39], s54 offen offset:1024 sc1\n" \
  "v_mfma_f32_16x16x32_bf16 v[84:87], a[76:79], v[196:199], v[84:87]\n" \
  "v_mfma_f32_16x16x32_bf16 v[88:91], a[132:135], v[196:199], v[88:91]\n" \
  "v_mfma_f32_16x16x32_bf16 v[92:95], a[188:191], v[196:199], v[92:95]\n" \
  "s_waitcnt vmcnt(13)\n" \
  "v_mfma_f32_16x16x32_bf16 v[80:83], a[24:27], v[200:203], v[80:83]\n" \
  "buffer_load_dwordx4 v[196:199], v1, s[36:39], s54 offen offset:2048 sc1\n" \
  "v_mfma_f32_16x16x32_bf16 v[84:87], a[80:83], v[200:203], v[84:87]\n" \
  "v_mfma_f32_16x16x32_bf16 v[88:91], a[136:139], v[200:203], v[88:91]\n" \
  "v_mfma_f32_16x16x32_bf16 v[92:95], a[192:195], v[200:203], v[92:95]\n" \
  "s_waitcnt vmcnt(13)\n" \
  "v_mfma_f32_16x16x32_bf16 v[80:83], a[28:31], v[204:207], v[80:83]\n" \
  "buffer_load_dwordx4 v[200:203], v1, s[36:39], s54 offen offset:3072 sc1\n" \
  "v_mfma_f32_16x16x32_bf16 v[84:87], a[84:87], v[204:207], v[84:87]\n" \
  "v_mfma_f32_16x16x32_bf16 v[88:91], a[140:143], v[204:207], v[88:91]\n" \
  "v_mfma_f32_16x16x32_bf16 v[92:95], a[196:199], v[204:207], v[92:95]\n" \
  "s_waitcnt vmcnt(13)\n" \
  "v_mfma_f32_16x16x32_bf16 v[80:83], a[32:35], v[208:211], v[80:83]\n" \
  "s_add_u32 s54, s66, 3325952\n" \
  "buffer_load_dwordx4 v[204:207], v1, s[36:39], s54 offen offset:0 sc1\n" \
  "v_mfma_f32_16x16x32_bf16 v[84:87], a[88:91], v[208:211], v[84:87]\n" \
  "v_mfma_f32_16x16x32_bf16 v[88:91], a[144:147], v[208:211], v[88:91]\n" \
  "v_mfma_f32_16x16x32_bf16 v[92:95], a[200:203], v[208:211], v[92:95]\n" \
  "s_waitcnt vmcnt(13)\n" \
  "v_mfma_f32_16x16x32_bf16 v[80:83], a[36:39], v[212:215], v[80:83]\n" \
  "buffer_load_dwordx4 v[208:211], v1, s[36:39], s54 offen offset:1024 sc1\n" \
  "v_mfma_f32_16x16x32_bf16 v[84:87], a[92:95], v[212:215], v[84:87]\n" \
  "v_mfma_f32_16x16x32_bf16 v[88:91], a[148:151], v[212:215], v[88:91]\n" \
  "v_mfma_f32_16x16x32_bf16 v[92:95], a[204:207], v[212:215], v[92:95]\n" \
  "s_waitcnt vmcnt(13)\n" \
  "v_mfma_f32_16x16x32_bf16 v[80:83], a[40:43], v[216:219], v[80:83]\n" \
  "buffer_load_dwordx4 v[212:215], v1, s[36:39], s54 offen offset:2048 sc1\n" \
  "v_mfma_f32_16x16x32_bf16 v[84:87], a[96:99], v[216:219], v[84:87]\n" \
  "v_mfma_f32_16x16x32_bf16 v[88:91], a[152:155], v[216:219], v[88:91]\n" \
  "v_mfma_f32_16x16x32_bf16 v[92:95], a[208:211], v[216:219], v[92:95]\n" \
  "s_waitcnt vmcnt(13)\n" \
  "v_mfma_f32_16x16x32_bf16 v[80:83], a[44:47], v[220:223], v[80:83]\n" \
  "buffer_load_dwordx4 v[216:219], v1, s[36:39], s54 offen offset:3072 sc1\n" \
  "v_mfma_f32_16x16x32_bf16 v[84:87], a[100:103], v[220:223], v[84:87]\n" \
  "v_mfma_f32_16x16x32_bf16 v[88:91], a[156:159], v[220:223], v[88:91]\n" \
  "v_mfma_f32_16x16x32_bf16 v[92:95], a[212:215], v[220:223], v[92:95]\n" \
  "s_waitcnt vmcnt(13)\n" \
  "v_mfma_f32_16x16x32_bf16 v[80:83], a[48:51], v[224:227], v[80:83]\n" \
  "s_add_u32 s54, s66, 3330048\n" \
  "buffer_load_dwordx4 v[220:223], v1, s[36:39], s54 offen offset:0 sc1\n" \
  "v_mfma_f32_16x16x32_bf16 v[84:87], a[104:107], v[224:227], v[84:87]\n" \
  "v_mfma_f32_16x16x32_bf16 v[88:91], a[160:163], v[224:227], v[88:91]\n" \
  "v_mfma_f32_16x16x32_bf16 v[92:95], a[216:219], v[224:227], v[92:95]\n" \
  "s_waitcnt vmcnt(13)\n" \
  "v_mfma_f32_16x16x32_bf16 v[80:83], a[52:55], v[228:231], v[80:83]\n" \
  "buffer_load_dwordx4 v[224:227], v1, s[36:39], s54 offen offset:1024 sc1\n" \
  "v_mfma_f32_16x16x32_bf16 v[84:87], a[108:111], v[228:231], v[84:87]\n" \
  "v_mfma_f32_16x16x32_bf16 v[88:91], a[164:167], v[228:231], v[88:91]\n" \
  "v_mfma_f32_16x16x32_bf16 v[92:95], a[220:223], v[228:231], v[92:95]\n" \
  "s_waitcnt vmcnt(13)\n" \
  "v_mfma_f32_16x16x32_bf16 v[96:99], a[0:3], v[232:235], 0\n" \
  "s_add_u32 s54, s66, 3981312\n" \
  "buffer_load_dwordx4 v[228:231], v1, s[36:39], s54 offen offset:0 sc1\n" \
  "v_mfma_f32_16x16x32_bf16 v[100:103], a[56:59], v[232:235], 0\n" \
  "v_mfma_f32_16x16x32_bf16 v[104:107], a[112:115], v[232:235], 0\n" \
  "v_mfma_f32_16x16x32_bf16 v[108:111], a[168:171], v[232:235], 0\n" \
  "s_waitcnt vmcnt(13)\n" \
  "v_mfma_f32_16x16x32_bf16 v[96:99], a[4:7], v[236:239], v[96:99]\n" \
  "buffer_load_dwordx4 v[232:235], v1, s[36:39], s54 offen offset:1024 sc1\n" \
  "v_mfma_f32_16x16x32_bf16 v[100:103], a[60:63], v[236:239], v[100:103]\n" \
  "v_mfma_f32_16x16x32_bf16 v[104:107], a[116:119], v[236:239], v[104:107]\n" \
  "v_mfma_f32_16x16x32_bf16 v[108:111], a[172:175], v[236:239], v[108:111]\n" \
  "s_waitcnt vmcnt(13)\n" \
  "v_mfma_f32_16x16x32_bf16 v[96:99], a[8:11], v[240:243], v[96:99]\n" \
  "buffer_load_dwordx4 v[236:239], v1, s[36:39], s54 offen offset:2048 sc1\n" \
  "v_mfma_f32_16x16x32_bf16 v[100:103], a[64:67], v[240:243], v[100:103]\n" \
  "v_mfma_f32_16x16x32_bf16 v[104:107], a[120:123], v[240:243], v[104:107]\n" \
  "v_mfma_f32_16x16x32_bf16 v[108:111], a[176:179], v[240:243], v[108:111]\n" \
  "s_waitcnt vmcnt(13)\n" \
  "v_mfma_f32_16x16x32_bf16 v[96:99], a[12:15], v[244:247], v[96:99]\n" \
  "buffer_load_dwordx4 v[240:243], v1, s[36:39], s54 offen offset:3072 sc1\n" \
  "v_mfma_f32_16x16x32_bf16 v[100:103], a[68:71], v[244:247], v[100:103]\n" \
  "v_mfma_f32_16x16x32_bf16 v[104:107], a[124:127], v[244:247], v[104:107]\n" \
  "v_mfma_f32_16x16x32_bf16 v[108:111], a[180:183], v[244:247], v[108:111]\n" \
  "s_waitcnt vmcnt(13)\n" \
  "v_mfma_f32_16x16x32_bf16 v[96:99], a[16:19], v[248:251], v[96:99]\n" \
  "s_add_u32 s54, s66, 3985408\n" \
  "buffer_load_dwordx4 v[244:247], v1, s[36:39], s54 offen offset:0 sc1\n" \
  "v_mfma_f32_16x16x32_bf16 v[100:103], a[72:75], v[248:251], v[100:103]\n" \
  "v_mfma_f32_16x16x32_bf16 v[104:107], a[128:131], v[248:251], v[104:107]\n" \
  "v_mfma_f32_16x16x32_bf16 v[108:111], a[184:187], v[248:251], v[108:111]\n" \
  "s_waitcnt vmcnt(13)\n" \
  "v_mfma_f32_16x16x32_bf16 v[96:99], a[20:23], v[192:195], v[96:99]\n" \
  "buffer_load_dwordx4 v[248:251], v1, s[36:39], s54 offen offset:1024 sc1\n" \
  "v_mfma_f32_16x16x32_bf16 v[100:103], a[76:79], v[192:195], v[100:103]\n" \
  "v_mfma_f32_16x16x32_bf16 v[104:107], a[132:135], v[192:195], v[104:107]\n" \
  "v_mfma_f32_16x16x32_bf16 v[108:111], a[188:191], v[192:195], v[108:111]\n" \
  "s_waitcnt vmcnt(13)\n" \
  "v_mfma_f32_16x16x32_bf16 v[96:99], a[24:27], v[196:199], v[96:99]\n" \
  "buffer_load_dwordx4 v[192:195], v1, s[36:39], s54 offen offset:2048 sc1\n" \
  "v_mfma_f32_16x16x32_bf16 v[100:103], a[80:83], v[196:199], v[100:103]\n" \
  "v_mfma_f32_16x16x32_bf16 v[104:107], a[136:139], v[196:199], v[104:107]\n" \
  "v_mfma_f32_16x16x32_bf16 v[108:111], a[192:195], v[196:199], v[108:111]\n" \
  "s_waitcnt vmcnt(13)\n" \
  "v_mfma_f32_16x16x32_bf16 v[96:99], a[28:31], v[200:203], v[96:99]\n" \
  "buffer_load_dwordx4 v[196:199], v1, s[36:39], s54 offen offset:3072 sc1\n" \
  "v_mfma_f32_16x16x32_bf16 v[100:103], a[84:87], v[200:203], v[100:103]\n" \
  "v_mfma_f32_16x16x32_bf16 v[104:107], a[140:143], v[200:203], v[104:107]\n" \
  "v_mfma_f32_16x16x32_bf16 v[108:111], a[196:199], v[200:203], v[108:111]\n" \
  "s_waitcnt vmcnt(13)\n" \
  "v_mfma_f32_16x16x32_bf16 v[96:99], a[32:35], v[204:207], v[96:99]\n" \
  "s_add_u32 s54, s66, 3989504\n" \
  "buffer_load_dwordx4 v[200:203], v1, s[36:39], s54 offen offset:0 sc1\n" \
  "v_mfma_f32_16x16x32_bf16 v[100:103], a[88:91], v[204:207], v[100:103]\n" \
  "v_mfma_f32_16x16x32_bf16 v[104:107], a[144:147], v[204:207], v[104:107]\n" \
  "v_mfma_f32_16x16x32_bf16 v[108:111], a[200:203], v[204:207], v[108:111]\n" \
  "s_waitcnt vmcnt(13)\n" \
  "v_mfma_f32_16x16x32_bf16 v[96:99], a[36:39], v[208:211], v[96:99]\n" \
  "buffer_load_dwordx4 v[204:207], v1, s[36:39], s54 offen offset:1024 sc1\n" \
  "v_mfma_f32_16x16x32_bf16 v[100:103], a[92:95], v[208:211], v[100:103]\n" \
  "v_mfma_f32_16x16x32_bf16 v[104:107], a[148:151], v[208:211], v[104:107]\n" \
  "v_mfma_f32_16x16x32_bf16 v[108:111], a[204:207], v[208:211], v[108:111]\n" \
  "s_waitcnt vmcnt(13)\n" \
  "v_mfma_f32_16x16x32_bf16 v[96:99], a[40:43], v[212:215], v[96:99]\n" \
  "buffer_load_dwordx4 v[208:211], v1, s[36:39], s54 offen offset:2048 sc1\n" \
  "v_mfma_f32_16x16x32_bf16 v[100:103], a[96:99], v[212:215], v[100:103]\n" \
  "v_mfma_f32_16x16x32_bf16 v[104:107], a[152:155], v[212:215], v[104:107]\n" \
  "v_mfma_f32_16x16x32_bf16 v[108:111], a[208:211], v[212:215], v[108:111]\n" \
  "s_waitcnt vmcnt(13)\n" \
  "v_mfma_f32_16x16x32_bf16 v[96:99], a[44:47], v[216:219], v[96:99]\n" \
  "buffer_load_dwordx4 v[212:215], v1, s[36:39], s54 offen offset:3072 sc1\n" \
  "v_mfma_f32_16x16x32_bf16 v[100:103], a[100:103], v[216:219], v[100:103]\n" \
  "v_mfma_f32_16x16x32_bf16 v[104:107], a[156:159], v[216:219], v[104:107]\n" \
  "v_mfma_f32_16x16x32_bf16 v[108:111], a[212:215], v[216:219], v[108:111]\n" \
  "s_waitcnt vmcnt(13)\n" \
  "v_mfma_f32_16x16x32_bf16 v[96:99], a[48:51], v[220:223], v[96:99]\n" \
  "s_add_u32 s54, s66, 3993600\n" \
  "buffer_load_dwordx4 v[216:219], v1, s[36:39], s54 offen offset:0 sc1\n" \
  "v_mfma_f32_16x16x32_bf16 v[100:103], a[104:107], v[220:223], v[100:103]\n" \
  "v_mfma_f32_16x16x32_bf16 v[104:107], a[160:163], v[220:223], v[104:107]\n" \
  "v_mfma_f32_16x16x32_bf16 v[108:111], a[216:219], v[220:223], v[108:111]\n" \
  "s_waitcnt vmcnt(13)\n" \
  "v_mfma_f32_16x16x32_bf16 v[96:99], a[52:55], v[224:227], v[96:99]\n" \
  "buffer_load_dwordx4 v[220:223], v1, s[36:39], s54 offen offset:1024 sc1\n" \
  "v_mfma_f32_16x16x32_bf16 v[100:103], a[108:111], v[224:227], v[100:103]\n" \
  "v_mfma_f32_16x16x32_bf16 v[104:107], a[164:167], v[224:227], v[104:107]\n" \
  "v_mfma_f32_16x16x32_bf16 v[108:111], a[220:223], v[224:227], v[108:111]\n" \
  "s_waitcnt vmcnt(13)\n" \
  "v_mfma_f32_16x16x32_bf16 v[112:115], a[0:3], v[228:231], 0\n" \
  "s_add_u32 s54, s66, 4644864\n" \
  "buffer_load_dwordx4 v[224:227], v1, s[36:39], s54 offen offset:0 sc1\n" \
  "v_mfma_f32_16x16x32_bf16 v[116:119], a[56:59], v[228:231], 0\n" \
  "v_mfma_f32_16x16x32_bf16 v[120:123], a[112:115], v[228:231], 0\n" \
  "v_mfma_f32_16x16x32_bf16 v[124:127], a[168:171], v[228:231], 0\n" \
  "s_waitcnt vmcnt(13)\n" \
  "v_mfma_f32_16x16x32_bf16 v[112:115], a[4:7], v[232:235], v[112:115]\n" \
  "buffer_load_dwordx4 v[228:231], v1, s[36:39], s54 offen offset:1024 sc1\n" \
  "v_mfma_f32_16x16x32_bf16 v[116:119], a[60:63], v[232:235], v[116:119]\n" \
  "v_mfma_f32_16x16x32_bf16 v[120:123], a[116:119], v[232:235], v[120:123]\n" \
  "v_mfma_f32_16x16x32_bf16 v[124:127], a[172:175], v[232:235], v[124:127]\n" \
  "s_waitcnt vmcnt(13)\n" \
  "v_mfma_f32_16x16x32_bf16 v[112:115], a[8:11], v[236:239], v[112:115]\n" \
  "buffer_load_dwordx4 v[232:235], v1, s[36:39], s54 offen offset:2048 sc1\n" \
  "v_mfma_f32_16x16x32_bf16 v[116:119], a[64:67], v[236:239], v[116:119]\n" \
  "v_mfma_f32_16x16x32_bf16 v[120:123], a[120:123], v[236:239], v[120:123]\n" \
  "v_mfma_f32_16x16x32_bf16 v[124:127], a[176:179], v[236:239], v[124:127]\n" \
  "s_waitcnt vmcnt(13)\n" \
  "v_mfma_f32_16x16x32_bf16 v[112:115], a[12:15], v[240:243], v[112:115]\n" \
  "buffer_load_dwordx4 v[236:239], v1, s[36:39], s54 offen offset:3072 sc1\n" \
  "v_mfma_f32_16x16x32_bf16 v[116:119], a[68:71], v[240:243], v[116:119]\n" \
  "v_mfma_f32_16x16x32_bf16 v[120:123], a[124:127], v[240:243], v[120:123]\n" \
  "v_mfma_f32_16x16x32_bf16 v[124:127], a[180:183], v[240:243], v[124:127]\n" \
  "s_waitcnt vmcnt(13)\n" \
  "v_mfma_f32_16x16x32_bf16 v[112:115], a[16:19], v[244:247], v[112:115]\n" \
  "s_add_u32 s54, s66, 4648960\n" \
  "buffer_load_dwordx4 v[240:243], v1, s[36:39], s54 offen offset:0 sc1\n" \
  "v_mfma_f32_16x16x32_bf16 v[116:119], a[72:75], v[244:247], v[116:119]\n" \
  "v_mfma_f32_16x16x32_bf16 v[120:123], a[128:131], v[244:247], v[120:123]\n" \
  "v_mfma_f32_16x16x32_bf16 v[124:127], a[184:187], v[244:247], v[124:127]\n" \
  "s_waitcnt vmcnt(13)\n" \
  "v_mfma_f32_16x16x32_bf16 v[112:115], a[20:23], v[248:251], v[112:115]\n" \
  "buffer_load_dwordx4 v[244:247], v1, s[36:39], s54 offen offset:1024 sc1\n" \
  "v_mfma_f32_16x16x32_bf16 v[116:119], a[76:79], v[248:251], v[116:119]\n" \
  "v_mfma_f32_16x16x32_bf16 v[120:123], a[132:135], v[248:251], v[120:123]\n" \
  "v_mfma_f32_16x16x32_bf16 v[124:127], a[188:191], v[248:251], v[124:127]\n" \
  "s_waitcnt vmcnt(13)\n" \
  "v_mfma_f32_16x16x32_bf16 v[112:115], a[24:27], v[192:195], v[112:115]\n" \
  "buffer_load_dwordx4 v[248:251], v1, s[36:39], s54 offen offset:2048 sc1\n" \
  "v_mfma_f32_16x16x32_bf16 v[116:119], a[80:83], v[192:195], v[116:119]\n" \
  "v_mfma_f32_16x16x32_bf16 v[120:123], a[136:139], v[192:195], v[120:123]\n" \
  "v_mfma_f32_16x16x32_bf16 v[124:127], a[192:195], v[192:195], v[124:127]\n" \
  "s_waitcnt vmcnt(13)\n" \
  "v_mfma_f32_16x16x32_bf16 v[112:115], a[28:31], v[196:199], v[112:115]\n" \
  "buffer_load_dwordx4 v[192:195], v1, s[36:39], s54 offen offset:3072 sc1\n" \
  "v_mfma_f32_16x16x32_bf16 v[116:119], a[84:87], v[196:199], v[116:119]\n" \
  "v_mfma_f32_16x16x32_bf16 v[120:123], a[140:143], v[196:199], v[120:123]\n" \
  "v_mfma_f32_16x16x32_bf16 v[124:127], a[196:199], v[196:199], v[124:127]\n" \
  "s_waitcnt vmcnt(13)\n" \
  "v_mfma_f32_16x16x32_bf16 v[112:115], a[32:35], v[200:203], v[112:115]\n" \
  "s_add_u32 s54, s66, 4653056\n" \
  "buffer_load_dwordx4 v[196:199], v1, s[36:39], s54 offen offset:0 sc1\n" \
  "v_mfma_f32_16x16x32_bf16 v[116:119], a[88:91], v[200:203], v[116:119]\n" \
  "v_mfma_f32_16x16x32_bf16 v[120:123], a[144:147], v[200:203], v[120:123]\n" \
  "v_mfma_f32_16x16x32_bf16 v[124:127], a[200:203], v[200:203], v[124:127]\n" \
  "s_waitcnt vmcnt(13)\n" \
  "v_mfma_f32_16x16x32_bf16 v[112:115], a[36:39], v[204:207], v[112:115]\n" \
  "buffer_load_dwordx4 v[200:203], v1, s[36:39], s54 offen offset:1024 sc1\n" \
  "v_mfma_f32_16x16x32_bf16 v[116:119], a[92:95], v[204:207], v[116:119]\n" \
  "v_mfma_f32_16x16x32_bf16 v[120:123], a[148:151], v[204:207], v[120:123]\n" \
  "v_mfma_f32_16x16x32_bf16 v[124:127], a[204:207], v[204:207], v[124:127]\n" \
  "s_waitcnt vmcnt(13)\n" \
  "v_mfma_f32_16x16x32_bf16 v[112:115], a[40:43], v[208:211], v[112:115]\n" \
  "buffer_load_dwordx4 v[204:207], v1, s[36:39], s54 offen offset:2048 sc1\n" \
  "v_mfma_f32_16x16x32_bf16 v[116:119], a[96:99], v[208:211], v[116:119]\n" \
  "v_mfma_f32_16x16x32_bf16 v[120:123], a[152:155], v[208:211], v[120:123]\n" \
  "v_mfma_f32_16x16x32_bf16 v[124:127], a[208:211], v[208:211], v[124:127]\n" \
  "s_waitcnt vmcnt(13)\n" \
  "v_mfma_f32_16x16x32_bf16 v[112:115], a[44:47], v[212:215], v[112:115]\n" \
  "buffer_load_dwordx4 v[208:211], v1, s[36:39], s54 offen offset:3072 sc1\n" \
  "v_mfma_f32_16x16x32_bf16 v[116:119], a[100:103], v[212:215], v[116:119]\n" \
  "v_mfma_f32_16x16x32_bf16 v[120:123], a[156:159], v[212:215], v[120:123]\n" \
  "v_mfma_f32_16x16x32_bf16 v[124:127], a[212:215], v[212:215], v[124:127]\n" \
  "s_waitcnt vmcnt(13)\n" \
  "v_mfma_f32_16x16x32_bf16 v[112:115], a[48:51], v[216:219], v[112:115]\n" \
  "s_add_u32 s54, s66, 4657152\n" \
  "buffer_load_dwordx4 v[212:215], v1, s[36:39], s54 offen offset:0 sc1\n" \
  "v_mfma_f32_16x16x32_bf16 v[116:119], a[104:107], v[216:219], v[116:119]\n" \
  "v_mfma_f32_16x16x32_bf16 v[120:123], a[160:163], v[216:219], v[120:123]\n" \
  "v_mfma_f32_16x16x32_bf16 v[124:127], a[216:219], v[216:219], v[124:127]\n" \
  "s_waitcnt vmcnt(13)\n" \
  "v_mfma_f32_16x16x32_bf16 v[112:115], a[52:55], v[220:223], v[112:115]\n" \
  "buffer_load_dwordx4 v[216:219], v1, s[36:39], s54 offen offset:1024 sc1\n" \
  "v_mfma_f32_16x16x32_bf16 v[116:119], a[108:111], v[220:223], v[116:119]\n" \
  "v_mfma_f32_16x16x32_bf16 v[120:123], a[164:167], v[220:223], v[120:123]\n" \
  "v_mfma_f32_16x16x32_bf16 v[124:127], a[220:223], v[220:223], v[124:127]\n" \
  "s_waitcnt vmcnt(13)\n" \
  "v_mfma_f32_16x16x32_bf16 v[128:131], a[0:3], v[224:227], 0\n" \
  "s_add_u32 s54, s66, 5308416\n" \
  "buffer_load_dwordx4 v[220:223], v1, s[36:39], s54 offen offset:0 sc1\n" \
  "v_mfma_f32_16x16x32_bf16 v[132:135], a[56:59], v[224:227], 0\n" \
  "v_mfma_f32_16x16x32_bf16 v[136:139], a[112:115], v[224:227], 0\n" \
  "v_mfma_f32_16x16x32_bf16 v[140:143], a[168:171], v[224:227], 0\n" \
  "s_waitcnt vmcnt(13)\n" \
  "v_mfma_f32_16x16x32_bf16 v[128:131], a[4:7], v[228:231], v[128:131]\n" \
  "buffer_load_dwordx4 v[224:227], v1, s[36:39], s54 offen offset:1024 sc1\n" \
  "v_mfma_f32_16x16x32_bf16 v[132:135], a[60:63], v[228:231], v[132:135]\n" \
  "v_mfma_f32_16x16x32_bf16 v[136:139], a[116:119], v[228:231], v[136:139]\n" \
  "v_mfma_f32_16x16x32_bf16 v[140:143], a[172:175], v[228:231], v[140:143]\n" \
  "s_waitcnt vmcnt(13)\n" \
  "v_mfma_f32_16x16x32_bf16 v[128:131], a[8:11], v[232:235], v[128:131]\n" \
  "buffer_load_dwordx4 v[228:231], v1, s[36:39], s54 offen offset:2048 sc1\n" \
  "v_mfma_f32_16x16x32_bf16 v[132:135], a[64:67], v[232:235], v[132:135]\n" \
  "v_mfma_f32_16x16x32_bf16 v[136:139], a[120:123], v[232:235], v[136:139]\n" \
  "v_mfma_f32_16x16x32_bf16 v[140:143], a[176:179], v[232:235], v[140:143]\n" \
  "s_waitcnt vmcnt(13)\n" \
  "v_mfma_f32_16x16x32_bf16 v[128:131], a[12:15], v[236:239], v[128:131]\n" \
  "buffer_load_dwordx4 v[232:235], v1, s[36:39], s54 offen offset:3072 sc1\n" \
  "v_mfma_f32_16x16x32_bf16 v[132:135], a[68:71], v[236:239], v[132:135]\n" \
  "v_mfma_f32_16x16x32_bf16 v[136:139], a[124:127], v[236:239], v[136:139]\n" \
  "v_mfma_f32_16x16x32_bf16 v[140:143], a[180:183], v[236:239], v[140:143]\n" \
  "s_waitcnt vmcnt(13)\n" \
  "v_mfma_f32_16x16x32_bf16 v[128:131], a[16:19], v[240:243], v[128:131]\n" \
  "s_add_u32 s54, s66, 5312512\n" \
  "buffer_load_dwordx4 v[236:239], v1, s[36:39], s54 offen offset:0 sc1\n" \
  "v_mfma_f32_16x16x32_bf16 v[132:135], a[72:75], v[240:243], v[132:135]\n" \
  "v_mfma_f32_16x16x32_bf16 v[136:139], a[128:131], v[240:243], v[136:139]\n" \
  "v_mfma_f32_16x16x32_bf16 v[140:143], a[184:187], v[240:243], v[140:143]\n" \
  "s_waitcnt vmcnt(13)\n" \
  "v_mfma_f32_16x16x32_bf16 v[128:131], a[20:23], v[244:247], v[128:131]\n" \
  "buffer_load_dwordx4 v[240:243], v1, s[36:39], s54 offen offset:1024 sc1\n" \
  "v_mfma_f32_16x16x32_bf16 v[132:135], a[76:79], v[244:247], v[132:135]\n" \
  "v_mfma_f32_16x16x32_bf16 v[136:139], a[132:135], v[244:247], v[136:139]\n" \
  "v_mfma_f32_16x16x32_bf16 v[140:143], a[188:191], v[244:247], v[140:143]\n" \
  "s_waitcnt vmcnt(13)\n" \
  "v_mfma_f32_16x16x32_bf16 v[128:131], a[24:27], v[248:251], v[128:131]\n" \
  "buffer_load_dwordx4 v[244:247], v1, s[36:39], s54 offen offset:2048 sc1\n" \
  "v_mfma_f32_16x16x32_bf16 v[132:135], a[80:83], v[248:251], v[132:135]\n" \
  "v_mfma_f32_16x16x32_bf16 v[136:139], a[136:139], v[248:251], v[136:139]\n" \
  "v_mfma_f32_16x16x32_bf16 v[140:143], a[192:195], v[248:251], v[140:143]\n" \
  "s_waitcnt vmcnt(13)\n" \
  "v_mfma_f32_16x16x32_bf16 v[128:131], a[28:31], v[192:195], v[128:131]\n" \
  "buffer_load_dwordx4 v[248:251], v1, s[36:39], s54 offen offset:3072 sc1\n" \
  "v_mfma_f32_16x16x32_bf16 v[132:135], a[84:87], v[192:195], v[132:135]\n" \
  "v_mfma_f32_16x16x32_bf16 v[136:139], a[140:143], v[192:195], v[136:139]\n" \
  "v_mfma_f32_16x16x32_bf16 v[140:143], a[196:199], v[192:195], v[140:143]\n" \
  "s_waitcnt vmcnt(13)\n" \
  "v_mfma_f32_16x16x32_bf16 v[128:131], a[32:35], v[196:199], v[128:131]\n" \
  "s_add_u32 s54, s66, 5316608\n" \
  "buffer_load_dwordx4 v[192:195], v1, s[36:39], s54 offen offset:0 sc1\n" \
  "v_mfma_f32_16x16x32_bf16 v[132:135], a[88:91], v[196:199], v[132:135]\n" \
  "v_mfma_f32_16x16x32_bf16 v[136:139], a[144:147], v[196:199], v[136:139]\n" \
  "v_mfma_f32_16x16x32_bf16 v[140:143], a[200:203], v[196:199], v[140:143]\n" \
  "s_waitcnt vmcnt(13)\n" \
  "v_mfma_f32_16x16x32_bf16 v[128:131], a[36:39], v[200:203], v[128:131]\n" \
  "buffer_load_dwordx4 v[196:199], v1, s[36:39], s54 offen offset:1024 sc1\n" \
  "v_mfma_f32_16x16x32_bf16 v[132:135], a[92:95], v[200:203], v[132:135]\n" \
  "v_mfma_f32_16x16x32_bf16 v[136:139], a[148:151], v[200:203], v[136:139]\n" \
  "v_mfma_f32_16x16x32_bf16 v[140:143], a[204:207], v[200:203], v[140:143]\n" \
  "s_waitcnt vmcnt(13)\n" \
  "v_mfma_f32_16x16x32_bf16 v[128:131], a[40:43], v[204:207], v[128:131]\n" \
  "buffer_load_dwordx4 v[200:203], v1, s[36:39], s54 offen offset:2048 sc1\n" \
  "v_mfma_f32_16x16x32_bf16 v[132:135], a[96:99], v[204:207], v[132:135]\n" \
  "v_mfma_f32_16x16x32_bf16 v[136:139], a[152:155], v[204:207], v[136:139]\n" \
  "v_mfma_f32_16x16x32_bf16 v[140:143], a[208:211], v[204:207], v[140:143]\n" \
  "s_waitcnt vmcnt(13)\n" \
  "v_mfma_f32_16x16x32_bf16 v[128:131], a[44:47], v[208:211], v[128:131]\n" \
  "buffer_load_dwordx4 v[204:207], v1, s[36:39], s54 offen offset:3072 sc1\n" \
  "v_mfma_f32_16x16x32_bf16 v[132:135], a[100:103], v[208:211], v[132:135]\n" \
  "v_mfma_f32_16x16x32_bf16 v[136:139], a[156:159], v[208:211], v[136:139]\n" \
  "v_mfma_f32_16x16x32_bf16 v[140:143], a[212:215], v[208:211], v[140:143]\n" \
  "s_waitcnt vmcnt(13)\n" \
  "v_mfma_f32_16x16x32_bf16 v[128:131], a[48:51], v[212:215], v[128:131]\n" \
  "s_add_u32 s54, s66, 5320704\n" \
  "buffer_load_dwordx4 v[208:211], v1, s[36:39], s54 offen offset:0 sc1\n" \
  "v_mfma_f32_16x16x32_bf16 v[132:135], a[104:107], v[212:215], v[132:135]\n" \
  "v_mfma_f32_16x16x32_bf16 v[136:139], a[160:163], v[212:215], v[136:139]\n" \
  "v_mfma_f32_16x16x32_bf16 v[140:143], a[216:219], v[212:215], v[140:143]\n" \
  "s_waitcnt vmcnt(13)\n" \
  "v_mfma_f32_16x16x32_bf16 v[128:131], a[52:55], v[216:219], v[128:131]\n" \
  "buffer_load_dwordx4 v[212:215], v1, s[36:39], s54 offen offset:1024 sc1\n" \
  "v_mfma_f32_16x16x32_bf16 v[132:135], a[108:111], v[216:219], v[132:135]\n" \
  "v_mfma_f32_16x16x32_bf16 v[136:139], a[164:167], v[216:219], v[136:139]\n" \
  "v_mfma_f32_16x16x32_bf16 v[140:143], a[220:223], v[216:219], v[140:143]\n" \
  "s_waitcnt vmcnt(13)\n" \
  "v_mfma_f32_16x16x32_bf16 v[144:147], a[0:3], v[220:223], 0\n" \
  "s_add_u32 s54, s66, 5971968\n" \
  "buffer_load_dwordx4 v[216:219], v1, s[36:39], s54 offen offset:0 sc1\n" \
  "v_mfma_f32_16x16x32_bf16 v[148:151], a[56:59], v[220:223], 0\n" \
  "v_mfma_f32_16x16x32_bf16 v[152:155], a[112:115], v[220:223], 0\n" \
  "v_mfma_f32_16x16x32_bf16 v[156:159], a[168:171], v[220:223], 0\n" \
  "s_waitcnt vmcnt(13)\n" \
  "v_mfma_f32_16x16x32_bf16 v[144:147], a[4:7], v[224:227], v[144:147]\n" \
  "buffer_load_dwordx4 v[220:223], v1, s[36:39], s54 offen offset:1024 sc1\n" \
  "v_mfma_f32_16x16x32_bf16 v[148:151], a[60:63], v[224:227], v[148:151]\n" \
  "v_mfma_f32_16x16x32_bf16 v[152:155], a[116:119], v[224:227], v[152:155]\n" \
  "v_mfma_f32_16x16x32_bf16 v[156:159], a[172:175], v[224:227], v[156:159]\n" \
  "s_waitcnt vmcnt(13)\n" \
  "v_mfma_f32_16x16x32_bf16 v[144:147], a[8:11], v[228:231], v[144:147]\n" \
  "buffer_load_dwordx4 v[224:227], v1, s[36:39], s54 offen offset:2048 sc1\n" \
  "v_mfma_f32_16x16x32_bf16 v[148:151], a[64:67], v[228:231], v[148:151]\n" \
  "v_mfma_f32_16x16x32_bf16 v[152:155], a[120:123], v[228:231], v[152:155]\n" \
  "v_mfma_f32_16x16x32_bf16 v[156:159], a[176:179], v[228:231], v[156:159]\n" \
  "s_waitcnt vmcnt(13)\n" \
  "v_mfma_f32_16x16x32_bf16 v[144:147], a[12:15], v[232:235], v[144:147]\n" \
  "buffer_load_dwordx4 v[228:231], v1, s[36:39], s54 offen offset:3072 sc1\n" \
  "v_mfma_f32_16x16x32_bf16 v[148:151], a[68:71], v[232:235], v[148:151]\n" \
  "v_mfma_f32_16x16x32_bf16 v[152:155], a[124:127], v[232:235], v[152:155]\n" \
  "v_mfma_f32_16x16x32_bf16 v[156:159], a[180:183], v[232:235], v[156:159]\n" \
  "s_waitcnt vmcnt(13)\n" \
  "v_mfma_f32_16x16x32_bf16 v[144:147], a[16:19], v[236:239], v[144:147]\n" \
  "s_add_u32 s54, s66, 5976064\n" \
  "buffer_load_dwordx4 v[232:235], v1, s[36:39], s54 offen offset:0 sc1\n" \
  "v_mfma_f32_16x16x32_bf16 v[148:151], a[72:75], v[236:239], v[148:151]\n" \
  "v_mfma_f32_16x16x32_bf16 v[152:155], a[128:131], v[236:239], v[152:155]\n" \
  "v_mfma_f32_16x16x32_bf16 v[156:159], a[184:187], v[236:239], v[156:159]\n" \
  "s_waitcnt vmcnt(13)\n" \
  "v_mfma_f32_16x16x32_bf16 v[144:147], a[20:23], v[240:243], v[144:147]\n" \
  "buffer_load_dwordx4 v[236:239], v1, s[36:39], s54 offen offset:1024 sc1\n" \
  "v_mfma_f32_16x16x32_bf16 v[148:151], a[76:79], v[240:243], v[148:151]\n" \
  "v_mfma_f32_16x16x32_bf16 v[152:155], a[132:135], v[240:243], v[152:155]\n" \
  "v_mfma_f32_16x16x32_bf16 v[156:159], a[188:191], v[240:243], v[156:159]\n" \
  "s_waitcnt vmcnt(13)\n" \
  "v_mfma_f32_16x16x32_bf16 v[144:147], a[24:27], v[244:247], v[144:147]\n" \
  "buffer_load_dwordx4 v[240:243], v1, s[36:39], s54 offen offset:2048 sc1\n" \
  "v_mfma_f32_16x16x32_bf16 v[148:151], a[80:83], v[244:247], v[148:151]\n" \
  "v_mfma_f32_16x16x32_bf16 v[152:155], a[136:139], v[244:247], v[152:155]\n" \
  "v_mfma_f32_16x16x32_bf16 v[156:159], a[192:195], v[244:247], v[156:159]\n" \
  "s_waitcnt vmcnt(13)\n" \
  "v_mfma_f32_16x16x32_bf16 v[144:147], a[28:31], v[248:251], v[144:147]\n" \
  "buffer_load_dwordx4 v[244:247], v1, s[36:39], s54 offen offset:3072 sc1\n" \
  "v_mfma_f32_16x16x32_bf16 v[148:151], a[84:87], v[248:251], v[148:151]\n" \
  "v_mfma_f32_16x16x32_bf16 v[152:155], a[140:143], v[248:251], v[152:155]\n" \
  "v_mfma_f32_16x16x32_bf16 v[156:159], a[196:199], v[248:251], v[156:159]\n" \
  "s_waitcnt vmcnt(13)\n" \
  "v_mfma_f32_16x16x32_bf16 v[144:147], a[32:35], v[192:195], v[144:147]\n" \
  "s_add_u32 s54, s66, 5980160\n" \
  "buffer_load_dwordx4 v[248:251], v1, s[36:39], s54 offen offset:0 sc1\n" \
  "v_mfma_f32_16x16x32_bf16 v[148:151], a[88:91], v[192:195], v[148:151]\n" \
  "v_mfma_f32_16x16x32_bf16 v[152:155], a[144:147], v[192:195], v[152:155]\n" \
  "v_mfma_f32_16x16x32_bf16 v[156:159], a[200:203], v[192:195], v[156:159]\n" \
  "s_waitcnt vmcnt(13)\n" \
  "v_mfma_f32_16x16x32_bf16 v[144:147], a[36:39], v[196:199], v[144:147]\n" \
  "buffer_load_dwordx4 v[192:195], v1, s[36:39], s54 offen offset:1024 sc1\n" \
  "v_mfma_f32_16x16x32_bf16 v[148:151], a[92:95], v[196:199], v[148:151]\n" \
  "v_mfma_f32_16x16x32_bf16 v[152:155], a[148:151], v[196:199], v[152:155]\n" \
  "v_mfma_f32_16x16x32_bf16 v[156:159], a[204:207], v[196:199], v[156:159]\n" \
  "s_waitcnt vmcnt(13)\n" \
  "v_mfma_f32_16x16x32_bf16 v[144:147], a[40:43], v[200:203], v[144:147]\n" \
  "buffer_load_dwordx4 v[196:199], v1, s[36:39], s54 offen offset:2048 sc1\n" \
  "v_mfma_f32_16x16x32_bf16 v[148:151], a[96:99], v[200:203], v[148:151]\n" \
  "v_mfma_f32_16x16x32_bf16 v[152:155], a[152:155], v[200:203], v[152:155]\n" \
  "v_mfma_f32_16x16x32_bf16 v[156:159], a[208:211], v[200:203], v[156:159]\n" \
  "s_waitcnt vmcnt(13)\n" \
  "v_mfma_f32_16x16x32_bf16 v[144:147], a[44:47], v[204:207], v[144:147]\n" \
  "buffer_load_dwordx4 v[200:203], v1, s[36:39], s54 offen offset:3072 sc1\n" \
  "v_mfma_f32_16x16x32_bf16 v[148:151], a[100:103], v[204:207], v[148:151]\n" \
  "v_mfma_f32_16x16x32_bf16 v[152:155], a[156:159], v[204:207], v[152:155]\n" \
  "v_mfma_f32_16x16x32_bf16 v[156:159], a[212:215], v[204:207], v[156:159]\n" \
  "s_waitcnt vmcnt(13)\n" \
  "v_mfma_f32_16x16x32_bf16 v[144:147], a[48:51], v[208:211], v[144:147]\n" \
  "s_add_u32 s54, s66, 5984256\n" \
  "buffer_load_dwordx4 v[204:207], v1, s[36:39], s54 offen offset:0 sc1\n" \
  "v_mfma_f32_16x16x32_bf16 v[148:151], a[104:107], v[208:211], v[148:151]\n" \
  "v_mfma_f32_16x16x32_bf16 v[152:155], a[160:163], v[208:211], v[152:155]\n" \
  "v_mfma_f32_16x16x32_bf16 v[156:159], a[216:219], v[208:211], v[156:159]\n" \
  "s_waitcnt vmcnt(13)\n" \
  "v_mfma_f32_16x16x32_bf16 v[144:147], a[52:55], v[212:215], v[144:147]\n" \
  "buffer_load_dwordx4 v[208:211], v1, s[36:39], s54 offen offset:1024 sc1\n" \
  "v_mfma_f32_16x16x32_bf16 v[148:151], a[108:111], v[212:215], v[148:151]\n" \
  "v_mfma_f32_16x16x32_bf16 v[152:155], a[164:167], v[212:215], v[152:155]\n" \
  "v_mfma_f32_16x16x32_bf16 v[156:159], a[220:223], v[212:215], v[156:159]\n" \
  "s_waitcnt vmcnt(13)\n" \
  "v_mfma_f32_16x16x32_bf16 v[160:163], a[0:3], v[216:219], 0\n" \
  "s_add_u32 s54, s66, s53\n" \
  "s_add_u32 s54, s54, 0\n" \
  "buffer_load_dwordx4 v[212:215], v1, s[36:39], s54 offen offset:0 sc1\n" \
  "v_mfma_f32_16x16x32_bf16 v[164:167], a[56:59], v[216:219], 0\n" \
  "v_mfma_f32_16x16x32_bf16 v[168:171], a[112:115], v[216:219], 0\n" \
  "v_mfma_f32_16x16x32_bf16 v[172:175], a[168:171], v[216:219], 0\n" \
  "s_waitcnt vmcnt(13)\n" \
  "v_mfma_f32_16x16x32_bf16 v[160:163], a[4:7], v[220:223], v[160:163]\n" \
  "buffer_load_dwordx4 v[216:219], v1, s[36:39], s54 offen offset:1024 sc1\n" \
  "v_mfma_f32_16x16x32_bf16 v[164:167], a[60:63], v[220:223], v[164:167]\n" \
  "v_mfma_f32_16x16x32_bf16 v[168:171], a[116:119], v[220:223], v[168:171]\n" \
  "v_mfma_f32_16x16x32_bf16 v[172:175], a[172:175], v[220:223], v[172:175]\n" \
  "s_waitcnt vmcnt(13)\n" \
  "v_mfma_f32_16x16x32_bf16 v[160:163], a[8:11], v[224:227], v[160:163]\n" \
  "buffer_load_dwordx4 v[220:223], v1, s[36:39], s54 offen offset:2048 sc1\n" \
  "v_mfma_f32_16x16x32_bf16 v[164:167], a[64:67], v[224:227], v[164:167]\n" \
  "v_mfma_f32_16x16x32_bf16 v[168:171], a[120:123], v[224:227], v[168:171]\n" \
  "v_mfma_f32_16x16x32_bf16 v[172:175], a[176:179], v[224:227], v[172:175]\n" \
  "s_waitcnt vmcnt(13)\n" \
  "v_mfma_f32_16x16x32_bf16 v[160:163], a[12:15], v[228:231], v[160:163]\n" \
  "buffer_load_dwordx4 v[224:227], v1, s[36:39], s54 offen offset:3072 sc1\n" \
  "v_mfma_f32_16x16x32_bf16 v[164:167], a[68:71], v[228:231], v[164:167]\n" \
  "v_mfma_f32_16x16x32_bf16 v[168:171], a[124:127], v[228:231], v[168:171]\n" \
  "v_mfma_f32_16x16x32_bf16 v[172:175], a[180:183], v[228:231], v[172:175]\n" \
  "s_waitcnt vmcnt(13)\n" \
  "v_mfma_f32_16x16x32_bf16 v[160:163], a[16:19], v[232:235], v[160:163]\n" \
  "s_add_u32 s54, s66, s53\n" \
  "s_add_u32 s54, s54, 4096\n" \
  "buffer_load_dwordx4 v[228:231], v1, s[36:39], s54 offen offset:0 sc1\n" \
  "v_mfma_f32_16x16x32_bf16 v[164:167], a[72:75], v[232:235], v[164:167]\n" \
  "v_mfma_f32_16x16x32_bf16 v[168:171], a[128:131], v[232:235], v[168:171]\n" \
  "v_mfma_f32_16x16x32_bf16 v[172:175], a[184:187], v[232:235], v[172:175]\n" \
  "s_waitcnt vmcnt(13)\n" \
  "v_mfma_f32_16x16x32_bf16 v[160:163], a[20:23], v[236:239], v[160:163]\n" \
  "buffer_load_dwordx4 v[232:235], v1, s[36:39], s54 offen offset:1024 sc1\n" \
  "v_mfma_f32_16x16x32_bf16 v[164:167], a[76:79], v[236:239], v[164:167]\n" \
  "v_mfma_f32_16x16x32_bf16 v[168:171], a[132:135], v[236:239], v[168:171]\n" \
  "v_mfma_f32_16x16x32_bf16 v[172:175], a[188:191], v[236:239], v[172:175]\n" \
  "s_waitcnt vmcnt(13)\n" \
  "v_mfma_f32_16x16x32_bf16 v[160:163], a[24:27], v[240:243], v[160:163]\n" \
  "buffer_load_dwordx4 v[236:239], v1, s[36:39], s54 offen offset:2048 sc1\n" \
  "v_mfma_f32_16x16x32_bf16 v[164:167], a[80:83], v[240:243], v[164:167]\n" \
  "v_mfma_f32_16x16x32_bf16 v[168:171], a[136:139], v[240:243], v[168:171]\n" \
  "v_mfma_f32_16x16x32_bf16 v[172:175], a[192:195], v[240:243], v[172:175]\n" \
  "s_waitcnt vmcnt(13)\n" \
  "v_mfma_f32_16x16x32_bf16 v[160:163], a[28:31], v[244:247], v[160:163]\n" \
  "buffer_load_dwordx4 v[240:243], v1, s[36:39], s54 offen offset:3072 sc1\n" \
  "v_mfma_f32_16x16x32_bf16 v[164:167], a[84:87], v[244:247], v[164:167]\n" \
  "v_mfma_f32_16x16x32_bf16 v[168:171], a[140:143], v[244:247], v[168:171]\n" \
  "v_mfma_f32_16x16x32_bf16 v[172:175], a[196:199], v[244:247], v[172:175]\n" \
  "s_waitcnt vmcnt(13)\n" \
  "v_mfma_f32_16x16x32_bf16 v[160:163], a[32:35], v[248:251], v[160:163]\n" \
  "s_add_u32 s54, s66, s53\n" \
  "s_add_u32 s54, s54, 8192\n" \
  "buffer_load_dwordx4 v[244:247], v1, s[36:39], s54 offen offset:0 sc1\n" \
  "v_mfma_f32_16x16x32_bf16 v[164:167], a[88:91], v[248:251], v[164:167]\n" \
  "v_mfma_f32_16x16x32_bf16 v[168:171], a[144:147], v[248:251], v[168:171]\n" \
  "v_mfma_f32_16x16x32_bf16 v[172:175], a[200:203], v[248:251], v[172:175]\n" \
  "s_waitcnt vmcnt(13)\n" \
  "v_mfma_f32_16x16x32_bf16 v[160:163], a[36:39], v[192:195], v[160:163]\n" \
  "buffer_load_dwordx4 v[248:251], v1, s[36:39], s54 offen offset:1024 sc1\n" \
  "v_mfma_f32_16x16x32_bf16 v[164:167], a[92:95], v[192:195], v[164:167]\n" \
  "v_mfma_f32_16x16x32_bf16 v[168:171], a[148:151], v[192:195], v[168:171]\n" \
  "v_mfma_f32_16x16x32_bf16 v[172:175], a[204:207], v[192:195], v[172:175]\n" \
  "s_waitcnt vmcnt(13)\n" \
  "v_mfma_f32_16x16x32_bf16 v[160:163], a[40:43], v[196:199], v[160:163]\n" \
  "buffer_load_dwordx4 v[192:195], v1, s[36:39], s54 offen offset:2048 sc1\n" \
  "v_mfma_f32_16x16x32_bf16 v[164:167], a[96:99], v[196:199], v[164:167]\n" \
  "v_mfma_f32_16x16x32_bf16 v[168:171], a[152:155], v[196:199], v[168:171]\n" \
  "v_mfma_f32_16x16x32_bf16 v[172:175], a[208:211], v[196:199], v[172:175]\n" \
  "s_waitcnt vmcnt(13)\n" \
  "v_mfma_f32_16x16x32_bf16 v[160:163], a[44:47], v[200:203], v[160:163]\n" \
  "buffer_load_dwordx4 v[196:199], v1, s[36:39], s54 offen offset:3072 sc1\n" \
  "v_mfma_f32_16x16x32_bf16 v[164:167], a[100:103], v[200:203], v[164:167]\n" \
  "v_mfma_f32_16x16x32_bf16 v[168:171], a[156:159], v[200:203], v[168:171]\n" \
  "v_mfma_f32_16x16x32_bf16 v[172:175], a[212:215], v[200:203], v[172:175]\n" \
  "s_waitcnt vmcnt(13)\n" \
  "v_mfma_f32_16x16x32_bf16 v[160:163], a[48:51], v[204:207], v[160:163]\n" \
  "s_add_u32 s54, s66, s53\n" \
  "s_add_u32 s54, s54, 12288\n" \
  "buffer_load_dwordx4 v[200:203], v1, s[36:39], s54 offen offset:0 sc1\n" \
  "v_mfma_f32_16x16x32_bf16 v[164:167], a[104:107], v[204:207], v[164:167]\n" \
  "v_mfma_f32_16x16x32_bf16 v[168:171], a[160:163], v[204:207], v[168:171]\n" \
  "v_mfma_f32_16x16x32_bf16 v[172:175], a[216:219], v[204:207], v[172:175]\n" \
  "s_waitcnt vmcnt(13)\n" \
  "v_mfma_f32_16x16x32_bf16 v[160:163], a[52:55], v[208:211], v[160:163]\n" \
  "buffer_load_dwordx4 v[204:207], v1, s[36:39], s54 offen offset:1024 sc1\n" \
  "v_mfma_f32_16x16x32_bf16 v[164:167], a[108:111], v[208:211], v[164:167]\n" \
  "v_mfma_f32_16x16x32_bf16 v[168:171], a[164:167], v[208:211], v[168:171]\n" \
  "v_mfma_f32_16x16x32_bf16 v[172:175], a[220:223], v[208:211], v[172:175]\n" \
  "s_waitcnt vmcnt(13)\n" \
  "v_mfma_f32_16x16x32_bf16 v[176:179], a[0:3], v[212:215], 0\n" \
  "v_mfma_f32_16x16x32_bf16 v[180:183], a[56:59], v[212:215], 0\n" \
  "v_mfma_f32_16x16x32_bf16 v[184:187], a[112:115], v[212:215], 0\n" \
  "v_mfma_f32_16x16x32_bf16 v[188:191], a[168:171], v[212:215], 0\n" \
  "s_waitcnt vmcnt(12)\n" \
  "v_mfma_f32_16x16x32_bf16 v[176:179], a[4:7], v[216:219], v[176:179]\n" \
  "v_mfma_f32_16x16x32_bf16 v[180:183], a[60:63], v[216:219], v[180:183]\n" \
  "v_mfma_f32_16x16x32_bf16 v[184:187], a[116:119], v[216:219], v[184:187]\n" \
  "v_mfma_f32_16x16x32_bf16 v[188:191], a[172:175], v[216:219], v[188:191]\n" \
  "s_waitcnt vmcnt(11)\n" \
  "v_mfma_f32_16x16x32_bf16 v[176:179], a[8:11], v[220:223], v[176:179]\n" \
  "v_mfma_f32_16x16x32_bf16 v[180:183], a[64:67], v[220:223], v[180:183]\n" \
  "v_mfma_f32_16x16x32_bf16 v[184:187], a[120:123], v[220:223], v[184:187]\n" \
  "v_mfma_f32_16x16x32_bf16 v[188:191], a[176:179], v[220:223], v[188:191]\n" \
  "s_waitcnt vmcnt(10)\n" \
  "v_mfma_f32_16x16x32_bf16 v[176:179], a[12:15], v[224:227], v[176:179]\n" \
  "v_mfma_f32_16x16x32_bf16 v[180:183], a[68:71], v[224:227], v[180:183]\n" \
  "v_mfma_f32_16x16x32_bf16 v[184:187], a[124:127], v[224:227], v[184:187]\n" \
  "v_mfma_f32_16x16x32_bf16 v[188:191], a[180:183], v[224:227], v[188:191]\n" \
  "s_waitcnt vmcnt(9)\n" \
  "v_mfma_f32_16x16x32_bf16 v[176:179], a[16:19], v[228:231], v[176:179]\n" \
  "v_mfma_f32_16x16x32_bf16 v[180:183], a[72:75], v[228:231], v[180:183]\n" \
  "v_mfma_f32_16x16x32_bf16 v[184:187], a[128:131], v[228:231], v[184:187]\n" \
  "v_mfma_f32_16x16x32_bf16 v[188:191], a[184:187], v[228:231], v[188:191]\n" \
  "s_waitcnt vmcnt(8)\n" \
  "v_mfma_f32_16x16x32_bf16 v[176:179], a[20:23], v[232:235], v[176:179]\n" \
  "v_mfma_f32_16x16x32_bf16 v[180:183], a[76:79], v[232:235], v[180:183]\n" \
  "v_mfma_f32_16x16x32_bf16 v[184:187], a[132:135], v[232:235], v[184:187]\n" \
  "v_mfma_f32_16x16x32_bf16 v[188:191], a[188:191], v[232:235], v[188:191]\n" \
  "s_waitcnt vmcnt(7)\n" \
  "v_mfma_f32_16x16x32_bf16 v[176:179], a[24:27], v[236:239], v[176:179]\n" \
  "v_mfma_f32_16x16x32_bf16 v[180:183], a[80:83], v[236:239], v[180:183]\n" \
  "v_mfma_f32_16x16x32_bf16 v[184:187], a[136:139], v[236:239], v[184:187]\n" \
  "v_mfma_f32_16x16x32_bf16 v[188:191], a[192:195], v[236:239], v[188:191]\n" \
  "s_waitcnt vmcnt(6)\n" \
  "v_mfma_f32_16x16x32_bf16 v[176:179], a[28:31], v[240:243], v[176:179]\n" \
  "v_mfma_f32_16x16x32_bf16 v[180:183], a[84:87], v[240:243], v[180:183]\n" \
  "v_mfma_f32_16x16x32_bf16 v[184:187], a[140:143], v[240:243], v[184:187]\n" \
  "v_mfma_f32_16x16x32_bf16 v[188:191], a[196:199], v[240:243], v[188:191]\n" \
  "s_waitcnt vmcnt(5)\n" \
  "v_mfma_f32_16x16x32_bf16 v[176:179], a[32:35], v[244:247], v[176:179]\n" \
  "v_mfma_f32_16x16x32_bf16 v[180:183], a[88:91], v[244:247], v[180:183]\n" \
  "v_mfma_f32_16x16x32_bf16 v[184:187], a[144:147], v[244:247], v[184:187]\n" \
  "v_mfma_f32_16x16x32_bf16 v[188:191], a[200:203], v[244:247], v[188:191]\n" \
  "s_waitcnt vmcnt(4)\n" \
  "v_mfma_f32_16x16x32_bf16 v[176:179], a[36:39], v[248:251], v[176:179]\n" \
  "v_mfma_f32_16x16x32_bf16 v[180:183], a[92:95], v[248:251], v[180:183]\n" \
  "v_mfma_f32_16x16x32_bf16 v[184:187], a[148:151], v[248:251], v[184:187]\n" \
  "v_mfma_f32_16x16x32_bf16 v[188:191], a[204:207], v[248:251], v[188:191]\n" \
  "s_waitcnt vmcnt(3)\n" \
  "v_mfma_f32_16x16x32_bf16 v[176:179], a[40:43], v[192:195], v[176:179]\n" \
  "v_mfma_f32_16x16x32_bf16 v[180:183], a[96:99], v[192:195], v[180:183]\n" \
  "v_mfma_f32_16x16x32_bf16 v[184:187], a[152:155], v[192:195], v[184:187]\n" \
  "v_mfma_f32_16x16x32_bf16 v[188:191], a[208:211], v[192:195], v[188:191]\n" \
  "s_waitcnt vmcnt(2)\n" \
  "v_mfma_f32_16x16x32_bf16 v[176:179], a[44:47], v[196:199], v[176:179]\n" \
  "v_mfma_f32_16x16x32_bf16 v[180:183], a[100:103], v[196:199], v[180:183]\n" \
  "v_mfma_f32_16x16x32_bf16 v[184:187], a[156:159], v[196:199], v[184:187]\n" \
  "v_mfma_f32_16x16x32_bf16 v[188:191], a[212:215], v[196:199], v[188:191]\n" \
  "s_waitcnt vmcnt(1)\n" \
  "v_mfma_f32_16x16x32_bf16 v[176:179], a[48:51], v[200:203], v[176:179]\n" \
  "v_mfma_f32_16x16x32_bf16 v[180:183], a[104:107], v[200:203], v[180:183]\n" \
  "v_mfma_f32_16x16x32_bf16 v[184:187], a[160:163], v[200:203], v[184:187]\n" \
  "v_mfma_f32_16x16x32_bf16 v[188:191], a[216:219], v[200:203], v[188:191]\n" \
  "s_waitcnt vmcnt(0)\n" \
  "v_mfma_f32_16x16x32_bf16 v[176:179], a[52:55], v[204:207], v[176:179]\n" \
  "v_mfma_f32_16x16x32_bf16 v[180:183], a[108:111], v[204:207], v[180:183]\n" \
  "v_mfma_f32_16x16x32_bf16 v[184:187], a[164:167], v[204:207], v[184:187]\n" \
  "v_mfma_f32_16x16x32_bf16 v[188:191], a[220:223], v[204:207], v[188:191]\n" \
  "s_lshl_b32 s52, s63, 2\n" \
  "s_add_u32 s52, s52, s48\n" \
  "s_lshl_b32 s52, s52, 1\n" \
  "s_add_u32 s52, s52, 0\n" \
  "s_mul_i32 s52, s52, 327680\n" \
  "s_lshl_b32 s53, s64, 7\n" \
  "s_add_u32 s52, s52, s53\n" \
  "s_add_u32 s80, s24, s52\n" \
  "s_addc_u32 s81, s25, 0\n" \
  "s_add_u32 m0, s72, 0\n" \
  "s_nop 0\n" \
  "global_load_lds_dwordx4 v11, s[80:81]\n" \
  "s_waitcnt vmcnt(4)\n" \
  "s_add_u32 s80, s80, 10240\n" \
  "s_addc_u32 s81, s81, 0\n" \
  "s_add_u32 m0, s72, 1024\n" \
  "s_nop 0\n" \
  "global_load_lds_dwordx4 v11, s[80:81]\n" \
  "s_waitcnt vmcnt(4)\n" \
  "s_add_u32 s80, s80, 10240\n" \
  "s_addc_u32 s81, s81, 0\n" \
  "s_add_u32 m0, s72, 2048\n" \
  "s_nop 0\n" \
  "global_load_lds_dwordx4 v11, s[80:81]\n" \
  "s_waitcnt vmcnt(4)\n" \
  "s_add_u32 s80, s80, 10240\n" \
  "s_addc_u32 s81, s81, 0\n" \
  "s_add_u32 m0, s72, 3072\n" \
  "s_nop 0\n" \
  "global_load_lds_dwordx4 v11, s[80:81]\n" \
  "s_waitcnt vmcnt(4)\n" \
  "s_add_u32 s80, s80, 10240\n" \
  "s_addc_u32 s81, s81, 0\n" \
  "s_add_u32 m0, s72, 4096\n" \
  "s_nop 0\n" \
  "global_load_lds_dwordx4 v11, s[80:81]\n" \
  "s_waitcnt vmcnt(4)\n" \
  "s_add_u32 s80, s80, 10240\n" \
  "s_addc_u32 s81, s81, 0\n" \
  "s_add_u32 m0, s72, 5120\n" \
  "s_nop 0\n" \
  "global_load_lds_dwordx4 v11, s[80:81]\n" \
  "s_waitcnt vmcnt(4)\n" \
  "s_add_u32 s80, s80, 10240\n" \
  "s_addc_u32 s81, s81, 0\n" \
  "s_add_u32 m0, s72, 6144\n" \
  "s_nop 0\n" \
  "global_load_lds_dwordx4 v11, s[80:81]\n" \
  "s_waitcnt vmcnt(4)\n" \
  "s_add_u32 s80, s80, 10240\n" \
  "s_addc_u32 s81, s81, 0\n" \
  "s_add_u32 m0, s72, 7168\n" \
  "s_nop 0\n" \
  "global_load_lds_dwordx4 v11, s[80:81]\n" \
  "s_waitcnt vmcnt(4)\n" \
  "s_add_u32 s80, s80, 10240\n" \
  "s_addc_u32 s81, s81, 0\n" \
  "s_add_u32 m0, s72, 8192\n" \
  "s_nop 0\n" \
  "global_load_lds_dwordx4 v11, s[80:81]\n" \
  "s_waitcnt vmcnt(4)\n" \
  "s_add_u32 s80, s80, 10240\n" \
  "s_addc_u32 s81, s81, 0\n" \
  "s_add_u32 m0, s72, 9216\n" \
  "s_nop 0\n" \
  "global_load_lds_dwordx4 v11, s[80:81]\n" \
  "s_waitcnt vmcnt(4)\n" \
  "s_add_u32 s80, s80, 10240\n" \
  "s_addc_u32 s81, s81, 0\n" \
  "s_add_u32 m0, s72, 10240\n" \
  "s_nop 0\n" \
  "global_load_lds_dwordx4 v11, s[80:81]\n" \
  "s_waitcnt vmcnt(4)\n" \
  "s_add_u32 s80, s80, 10240\n" \
  "s_addc_u32 s81, s81, 0\n" \
  "s_add_u32 m0, s72, 11264\n" \
  "s_nop 0\n" \
  "global_load_lds_dwordx4 v11, s[80:81]\n" \
  "s_waitcnt vmcnt(4)\n" \
  "s_add_u32 s80, s80, 10240\n" \
  "s_addc_u32 s81, s81, 0\n" \
  "s_add_u32 m0, s72, 12288\n" \
  "s_nop 0\n" \
  "global_load_lds_dwordx4 v11, s[80:81]\n" \
  "s_waitcnt vmcnt(4)\n" \
  "s_add_u32 s80, s80, 10240\n" \
  "s_addc_u32 s81, s81, 0\n" \
  "s_add_u32 m0, s72, 13312\n" \
  "s_nop 0\n" \
  "global_load_lds_dwordx4 v11, s[80:81]\n" \
  "s_waitcnt vmcnt(4)\n" \
  "s_add_u32 s80, s80, 10240\n" \
  "s_addc_u32 s81, s81, 0\n" \
  "s_add_u32 m0, s72, 14336\n" \
  "s_nop 0\n" \
  "global_load_lds_dwordx4 v11, s[80:81]\n" \
  "s_waitcnt vmcnt(4)\n" \
  "s_add_u32 s80, s80, 10240\n" \
  "s_addc_u32 s81, s81, 0\n" \
  "s_add_u32 m0, s72, 15360\n" \
  "s_nop 0\n" \
  "global_load_lds_dwordx4 v11, s[80:81]\n" \
  "s_waitcnt vmcnt(4)\n" \
  "s_add_u32 s80, s80, 10240\n" \
  "s_addc_u32 s81, s81, 0\n" \
  "s_add_u32 m0, s72, 16384\n" \
  "s_nop 0\n" \
  "global_load_lds_dwordx4 v11, s[80:81]\n" \
  "s_waitcnt vmcnt(4)\n" \
  "s_add_u32 s80, s80, 10240\n" \
  "s_addc_u32 s81, s81, 0\n" \
  "s_add_u32 m0, s72, 17408\n" \
  "s_nop 0\n" \
  "global_load_lds_dwordx4 v11, s[80:81]\n" \
  "s_waitcnt vmcnt(4)\n" \
  "s_add_u32 s80, s80, 10240\n" \
  "s_addc_u32 s81, s81, 0\n" \
  "s_add_u32 m0, s72, 18432\n" \
  "s_nop 0\n" \
  "global_load_lds_dwordx4 v11, s[80:81]\n" \
  "s_waitcnt vmcnt(4)\n" \
  "s_add_u32 s80, s80, 10240\n" \
  "s_addc_u32 s81, s81, 0\n" \
  "s_add_u32 m0, s72, 19456\n" \
  "s_nop 0\n" \
  "global_load_lds_dwordx4 v11, s[80:81]\n" \
  "s_waitcnt vmcnt(4)\n" \
  "s_add_u32 s80, s80, 10240\n" \
  "s_addc_u32 s81, s81, 0\n" \
  "s_add_u32 m0, s72, 20480\n" \
  "s_nop 0\n" \
  "global_load_lds_dwordx4 v11, s[80:81]\n" \
  "s_waitcnt vmcnt(4)\n" \
  "s_add_u32 s80, s80, 10240\n" \
  "s_addc_u32 s81, s81, 0\n" \
  "s_add_u32 m0, s72, 21504\n" \
  "s_nop 0\n" \
  "global_load_lds_dwordx4 v11, s[80:81]\n" \
  "s_waitcnt vmcnt(4)\n" \
  "s_add_u32 s80, s80, 10240\n" \
  "s_addc_u32 s81, s81, 0\n" \
  "s_add_u32 m0, s72, 22528\n" \
  "s_nop 0\n" \
  "global_load_lds_dwordx4 v11, s[80:81]\n" \
  "s_waitcnt vmcnt(4)\n" \
  "s_add_u32 s80, s80, 10240\n" \
  "s_addc_u32 s81, s81, 0\n" \
  "s_add_u32 m0, s72, 23552\n" \
  "s_nop 0\n" \
  "global_load_lds_dwordx4 v11, s[80:81]\n" \
  "s_waitcnt vmcnt(4)\n" \
  "s_add_u32 s80, s80, 10240\n" \
  "s_addc_u32 s81, s81, 0\n" \
  "s_add_u32 m0, s72, 24576\n" \
  "s_nop 0\n" \
  "global_load_lds_dwordx4 v11, s[80:81]\n" \
  "s_waitcnt vmcnt(4)\n" \
  "s_add_u32 s80, s80, 10240\n" \
  "s_addc_u32 s81, s81, 0\n" \
  "s_add_u32 m0, s72, 25600\n" \
  "s_nop 0\n" \
  "global_load_lds_dwordx4 v11, s[80:81]\n" \
  "s_waitcnt vmcnt(4)\n" \
  "s_add_u32 s80, s80, 10240\n" \
  "s_addc_u32 s81, s81, 0\n" \
  "s_add_u32 m0, s72, 26624\n" \
  "s_nop 0\n" \
  "global_load_lds_dwordx4 v11, s[80:81]\n" \
  "s_waitcnt vmcnt(4)\n" \
  "s_add_u32 s80, s80, 10240\n" \
  "s_addc_u32 s81, s81, 0\n" \
  "s_add_u32 m0, s72, 27648\n" \
  "s_nop 0\n" \
  "global_load_lds_dwordx4 v11, s[80:81]\n" \
  "s_waitcnt vmcnt(4)\n" \
  "s_add_u32 s80, s80, 10240\n" \
  "s_addc_u32 s81, s81, 0\n" \
  "s_add_u32 m0, s72, 28672\n" \
  "s_nop 0\n" \
  "global_load_lds_dwordx4 v11, s[80:81]\n" \
  "s_waitcnt vmcnt(4)\n" \
  "s_add_u32 s80, s80, 10240\n" \
  "s_addc_u32 s81, s81, 0\n" \
  "s_add_u32 m0, s72, 29696\n" \
  "s_nop 0\n" \
  "global_load_lds_dwordx4 v11, s[80:81]\n" \
  "s_waitcnt vmcnt(4)\n" \
  "s_add_u32 s80, s80, 10240\n" \
  "s_addc_u32 s81, s81, 0\n" \
  "s_add_u32 m0, s72, 30720\n" \
  "s_nop 0\n" \
  "global_load_lds_dwordx4 v11, s[80:81]\n" \
  "s_waitcnt vmcnt(4)\n" \
  "s_add_u32 s80, s80, 10240\n" \
  "s_addc_u32 s81, s81, 0\n" \
  "s_add_u32 m0, s72, 31744\n" \
  "s_nop 0\n" \
  "global_load_lds_dwordx4 v11, s[80:81]\n" \
  "s_waitcnt vmcnt(4)\n" \
  "s_lshl_b32 s52, s48, 12\n" \
  "s_add_u32 s52, s52, 131072\n" \
  "v_add_u32 v8, s52, v1\n" \
  "v_bfe_u32 v4, v0, 2, 2\n" \
  "v_lshlrev_b32 v7, 10, v4\n" \
  "v_and_b32 v4, 3, v0\n" \
  "v_lshl_add_u32 v7, v4, 8, v7\n" \
  "v_lshl_add_u32 v7, v3, 4, v7\n" \
  "s_lshl_b32 s52, s48, 6\n" \
  "v_add_u32 v7, s52, v7\n" \
  "v_add_u32 v9, 131072, v7\n" \
  "ds_write_b128 v8, v[16:19] offset:0\n" \
  "ds_write_b128 v8, v[20:23] offset:1024\n" \
  "ds_write_b128 v8, v[24:27] offset:2048\n" \
  "ds_write_b128 v8, v[28:31] offset:3072\n" \
  "ds_write_b128 v8, v[32:35] offset:16384\n" \
  "ds_write_b128 v8, v[36:39] offset:17408\n" \
  "ds_write_b128 v8, v[40:43] offset:18432\n" \
  "ds_write_b128 v8, v[44:47] offset:19456\n" \
  "s_waitcnt lgkmcnt(0)\n" \
  "s_barrier\n" \
  "ds_read_b128 v[216:219], v9 offset:0\n" \
  "ds_read_b128 v[220:223], v9 offset:4096\n" \
  "ds_read_b128 v[224:227], v9 offset:8192\n" \
  "ds_read_b128 v[228:231], v9 offset:12288\n" \
  "ds_read_b128 v[232:235], v9 offset:16384\n" \
  "ds_read_b128 v[236:239], v9 offset:20480\n" \
  "ds_read_b128 v[240:243], v9 offset:24576\n" \
  "ds_read_b128 v[244:247], v9 offset:28672\n" \
  "s_waitcnt lgkmcnt(0)\n" \
  "s_barrier\n" \
  "v_pk_add_f32 v[216:217], v[216:217], v[220:221]\n" \
  "v_pk_add_f32 v[218:219], v[218:219], v[222:223]\n" \
  "v_pk_add_f32 v[224:225], v[224:225], v[228:229]\n" \
  "v_pk_add_f32 v[226:227], v[226:227], v[230:231]\n" \
  "v_pk_add_f32 v[216:217], v[216:217], v[224:225]\n" \
  "v_pk_add_f32 v[218:219], v[218:219], v[226:227]\n" \
  "v_mul_f32 v248, 0xbfb8aa3b, v216\n" \
  "v_mul_f32 v249, 0xbfb8aa3b, v217\n" \
  "v_mul_f32 v250, 0xbfb8aa3b, v218\n" \
  "v_mul_f32 v251, 0xbfb8aa3b, v219\n" \
  "v_exp_f32 v248, v248\n" \
  "v_exp_f32 v249, v249\n" \
  "v_exp_f32 v250, v250\n" \
  "v_exp_f32 v251, v251\n" \
  "v_add_f32 v248, 1.0, v248\n" \
  "v_add_f32 v249, 1.0, v249\n" \
  "v_add_f32 v250, 1.0, v250\n" \
  "v_add_f32 v251, 1.0, v251\n" \
  "v_rcp_f32 v248, v248\n" \
  "v_rcp_f32 v249, v249\n" \
  "v_rcp_f32 v250, v250\n" \
  "v_rcp_f32 v251, v251\n" \
  "v_mul_f32 v216, v216, v248\n" \
  "v_mul_f32 v217, v217, v249\n" \
  "v_mul_f32 v218, v218, v250\n" \
  "v_mul_f32 v219, v219, v251\n" \
  "v_cvt_pk_bf16_f32 v192, v216, v217\n" \
  "v_cvt_pk_bf16_f32 v193, v218, v219\n" \
  "v_pk_add_f32 v[232:233], v[232:233], v[236:237]\n" \
  "v_pk_add_f32 v[234:235], v[234:235], v[238:239]\n" \
  "v_pk_add_f32 v[240:241], v[240:241], v[244:245]\n" \
  "v_pk_add_f32 v[242:243], v[242:243], v[246:247]\n" \
  "v_pk_add_f32 v[232:233], v[232:233], v[240:241]\n" \
  "v_pk_add_f32 v[234:235], v[234:235], v[242:243]\n" \
  "v_mul_f32 v248, 0xbfb8aa3b, v232\n" \
  "v_mul_f32 v249, 0xbfb8aa3b, v233\n" \
  "v_mul_f32 v250, 0xbfb8aa3b, v234\n" \
  "v_mul_f32 v251, 0xbfb8aa3b, v235\n" \
  "v_exp_f32 v248, v248\n" \
  "v_exp_f32 v249, v249\n" \
  "v_exp_f32 v250, v250\n" \
  "v_exp_f32 v251, v251\n" \
  "v_add_f32 v248, 1.0, v248\n" \
  "v_add_f32 v249, 1.0, v249\n" \
  "v_add_f32 v250, 1.0, v250\n" \
  "v_add_f32 v251, 1.0, v251\n" \
  "v_rcp_f32 v248, v248\n" \
  "v_rcp_f32 v249, v249\n" \
  "v_rcp_f32 v250, v250\n" \
  "v_rcp_f32 v251, v251\n" \
  "v_mul_f32 v232, v232, v248\n" \
  "v_mul_f32 v233, v233, v249\n" \
  "v_mul_f32 v234, v234, v250\n" \
  "v_mul_f32 v235, v235, v251\n" \
  "v_cvt_pk_bf16_f32 v194, v232, v233\n" \
  "v_cvt_pk_bf16_f32 v195, v234, v235\n" \
  "ds_write_b128 v8, v[48:51] offset:0\n" \
  "ds_write_b128 v8, v[52:55] offset:1024\n" \
  "ds_write_b128 v8, v[56:59] offset:2048\n" \
  "ds_write_b128 v8, v[60:63] offset:3072\n" \
  "ds_write_b128 v8, v[64:67] offset:16384\n" \
  "ds_write_b128 v8, v[68:71] offset:17408\n" \
  "ds_write_b128 v8, v[72:75] offset:18432\n" \
  "ds_write_b128 v8, v[76:79] offset:19456\n" \
  "s_waitcnt lgkmcnt(0)\n" \
  "s_barrier\n" \
  "ds_read_b128 v[216:219], v9 offset:0\n" \
  "ds_read_b128 v[220:223], v9 offset:4096\n" \
  "ds_read_b128 v[224:227], v9 offset:8192\n" \
  "ds_read_b128 v[228:231], v9 offset:12288\n" \
  "ds_read_b128 v[232:235], v9 offset:16384\n" \
  "ds_read_b128 v[236:239], v9 offset:20480\n" \
  "ds_read_b128 v[240:243], v9 offset:24576\n" \
  "ds_read_b128 v[244:247], v9 offset:28672\n" \
  "s_waitcnt lgkmcnt(0)\n" \
  "s_barrier\n" \
  "v_pk_add_f32 v[216:217], v[216:217], v[220:221]\n" \
  "v_pk_add_f32 v[218:219], v[218:219], v[222:223]\n" \
  "v_pk_add_f32 v[224:225], v[224:225], v[228:229]\n" \
  "v_pk_add_f32 v[226:227], v[226:227], v[230:231]\n" \
  "v_pk_add_f32 v[216:217], v[216:217], v[224:225]\n" \
  "v_pk_add_f32 v[218:219], v[218:219], v[226:227]\n" \
  "v_mul_f32 v248, 0xbfb8aa3b, v216\n" \
  "v_mul_f32 v249, 0xbfb8aa3b, v217\n" \
  "v_mul_f32 v250, 0xbfb8aa3b, v218\n" \
  "v_mul_f32 v251, 0xbfb8aa3b, v219\n" \
  "v_exp_f32 v248, v248\n" \
  "v_exp_f32 v249, v249\n" \
  "v_exp_f32 v250, v250\n" \
  "v_exp_f32 v251, v251\n" \
  "v_add_f32 v248, 1.0, v248\n" \
  "v_add_f32 v249, 1.0, v249\n" \
  "v_add_f32 v250, 1.0, v250\n" \
  "v_add_f32 v251, 1.0, v251\n" \
  "v_rcp_f32 v248, v248\n" \
  "v_rcp_f32 v249, v249\n" \
  "v_rcp_f32 v250, v250\n" \
  "v_rcp_f32 v251, v251\n" \
  "v_mul_f32 v216, v216, v248\n" \
  "v_mul_f32 v217, v217, v249\n" \
  "v_mul_f32 v218, v218, v250\n" \
  "v_mul_f32 v219, v219, v251\n" \
  "v_cvt_pk_bf16_f32 v196, v216, v217\n" \
  "v_cvt_pk_bf16_f32 v197, v218, v219\n" \
  "v_pk_add_f32 v[232:233], v[232:233], v[236:237]\n" \
  "v_pk_add_f32 v[234:235], v[234:235], v[238:239]\n" \
  "v_pk_add_f32 v[240:241], v[240:241], v[244:245]\n" \
  "v_pk_add_f32 v[242:243], v[242:243], v[246:247]\n" \
  "v_pk_add_f32 v[232:233], v[232:233], v[240:241]\n" \
  "v_pk_add_f32 v[234:235], v[234:235], v[242:243]\n" \
  "v_mul_f32 v248, 0xbfb8aa3b, v232\n" \
  "v_mul_f32 v249, 0xbfb8aa3b, v233\n" \
  "v_mul_f32 v250, 0xbfb8aa3b, v234\n" \
  "v_mul_f32 v251, 0xbfb8aa3b, v235\n" \
  "v_exp_f32 v248, v248\n" \
  "v_exp_f32 v249, v249\n" \
  "v_exp_f32 v250, v250\n" \
  "v_exp_f32 v251, v251\n" \
  "v_add_f32 v248, 1.0, v248\n" \
  "v_add_f32 v249, 1.0, v249\n" \
  "v_add_f32 v250, 1.0, v250\n" \
  "v_add_f32 v251, 1.0, v251\n" \
  "v_rcp_f32 v248, v248\n" \
  "v_rcp_f32 v249, v249\n" \
  "v_rcp_f32 v250, v250\n" \
  "v_rcp_f32 v251, v251\n" \
  "v_mul_f32 v232, v232, v248\n" \
  "v_mul_f32 v233, v233, v249\n" \
  "v_mul_f32 v234, v234, v250\n" \
  "v_mul_f32 v235, v235, v251\n" \
  "v_cvt_pk_bf16_f32 v198, v232, v233\n" \
  "v_cvt_pk_bf16_f32 v199, v234, v235\n" \
  "ds_write_b128 v8, v[80:83] offset:0\n" \
  "ds_write_b128 v8, v[84:87] offset:1024\n" \
  "ds_write_b128 v8, v[88:91] offset:2048\n" \
  "ds_write_b128 v8, v[92:95] offset:3072\n" \
  "ds_write_b128 v8, v[96:99] offset:16384\n" \
  "ds_write_b128 v8, v[100:103] offset:17408\n" \
  "ds_write_b128 v8, v[104:107] offset:18432\n" \
  "ds_write_b128 v8, v[108:111] offset:19456\n" \
  "s_waitcnt lgkmcnt(0)\n" \
  "s_barrier\n" \
  "ds_read_b128 v[216:219], v9 offset:0\n" \
  "ds_read_b128 v[220:223], v9 offset:4096\n" \
  "ds_read_b128 v[224:227], v9 offset:8192\n" \
  "ds_read_b128 v[228:231], v9 offset:12288\n" \
  "ds_read_b128 v[232:235], v9 offset:16384\n" \
  "ds_read_b128 v[236:239], v9 offset:20480\n" \
  "ds_read_b128 v[240:243], v9 offset:24576\n" \
  "ds_read_b128 v[244:247], v9 offset:28672\n" \
  "s_waitcnt lgkmcnt(0)\n" \
  "s_barrier\n" \
  "v_pk_add_f32 v[216:217], v[216:217], v[220:221]\n" \
  "v_pk_add_f32 v[218:219], v[218:219], v[222:223]\n" \
  "v_pk_add_f32 v[224:225], v[224:225], v[228:229]\n" \
  "v_pk_add_f32 v[226:227], v[226:227], v[230:231]\n" \
  "v_pk_add_f32 v[216:217], v[216:217], v[224:225]\n" \
  "v_pk_add_f32 v[218:219], v[218:219], v[226:227]\n" \
  "v_mul_f32 v248, 0xbfb8aa3b, v216\n" \
  "v_mul_f32 v249, 0xbfb8aa3b, v217\n" \
  "v_mul_f32 v250, 0xbfb8aa3b, v218\n" \
  "v_mul_f32 v251, 0xbfb8aa3b, v219\n" \
  "v_exp_f32 v248, v248\n" \
  "v_exp_f32 v249, v249\n" \
  "v_exp_f32 v250, v250\n" \
  "v_exp_f32 v251, v251\n" \
  "v_add_f32 v248, 1.0, v248\n" \
  "v_add_f32 v249, 1.0, v249\n" \
  "v_add_f32 v250, 1.0, v250\n" \
  "v_add_f32 v251, 1.0, v251\n" \
  "v_rcp_f32 v248, v248\n" \
  "v_rcp_f32 v249, v249\n" \
  "v_rcp_f32 v250, v250\n" \
  "v_rcp_f32 v251, v251\n" \
  "v_mul_f32 v216, v216, v248\n" \
  "v_mul_f32 v217, v217, v249\n" \
  "v_mul_f32 v218, v218, v250\n" \
  "v_mul_f32 v219, v219, v251\n" \
  "v_cvt_pk_bf16_f32 v200, v216, v217\n" \
  "v_cvt_pk_bf16_f32 v201, v218, v219\n" \
  "v_pk_add_f32 v[232:233], v[232:233], v[236:237]\n" \
  "v_pk_add_f32 v[234:235], v[234:235], v[238:239]\n" \
  "v_pk_add_f32 v[240:241], v[240:241], v[244:245]\n" \
  "v_pk_add_f32 v[242:243], v[242:243], v[246:247]\n" \
  "v_pk_add_f32 v[232:233], v[232:233], v[240:241]\n" \
  "v_pk_add_f32 v[234:235], v[234:235], v[242:243]\n" \
  "v_mul_f32 v248, 0xbfb8aa3b, v232\n" \
  "v_mul_f32 v249, 0xbfb8aa3b, v233\n" \
  "v_mul_f32 v250, 0xbfb8aa3b, v234\n" \
  "v_mul_f32 v251, 0xbfb8aa3b, v235\n" \
  "v_exp_f32 v248, v248\n" \
  "v_exp_f32 v249, v249\n" \
  "v_exp_f32 v250, v250\n" \
  "v_exp_f32 v251, v251\n" \
  "v_add_f32 v248, 1.0, v248\n" \
  "v_add_f32 v249, 1.0, v249\n" \
  "v_add_f32 v250, 1.0, v250\n" \
  "v_add_f32 v251, 1.0, v251\n" \
  "v_rcp_f32 v248, v248\n" \
  "v_rcp_f32 v249, v249\n" \
  "v_rcp_f32 v250, v250\n" \
  "v_rcp_f32 v251, v251\n" \
  "v_mul_f32 v232, v232, v248\n" \
  "v_mul_f32 v233, v233, v249\n" \
  "v_mul_f32 v234, v234, v250\n" \
  "v_mul_f32 v235, v235, v251\n" \
  "v_cvt_pk_bf16_f32 v202, v232, v233\n" \
  "v_cvt_pk_bf16_f32 v203, v234, v235\n" \
  "ds_write_b128 v8, v[112:115] offset:0\n" \
  "ds_write_b128 v8, v[116:119] offset:1024\n" \
  "ds_write_b128 v8, v[120:123] offset:2048\n" \
  "ds_write_b128 v8, v[124:127] offset:3072\n" \
  "ds_write_b128 v8, v[128:131] offset:16384\n" \
  "ds_write_b128 v8, v[132:135] offset:17408\n" \
  "ds_write_b128 v8, v[136:139] offset:18432\n" \
  "ds_write_b128 v8, v[140:143] offset:19456\n" \
  "s_waitcnt lgkmcnt(0)\n" \
  "s_barrier\n" \
  "ds_read_b128 v[216:219], v9 offset:0\n" \
  "ds_read_b128 v[220:223], v9 offset:4096\n" \
  "ds_read_b128 v[224:227], v9 offset:8192\n" \
  "ds_read_b128 v[228:231], v9 offset:12288\n" \
  "ds_read_b128 v[232:235], v9 offset:16384\n" \
  "ds_read_b128 v[236:239], v9 offset:20480\n" \
  "ds_read_b128 v[240:243], v9 offset:24576\n" \
  "ds_read_b128 v[244:247], v9 offset:28672\n" \
  "s_waitcnt lgkmcnt(0)\n" \
  "s_barrier\n" \
  "v_pk_add_f32 v[216:217], v[216:217], v[220:221]\n" \
  "v_pk_add_f32 v[218:219], v[218:219], v[222:223]\n" \
  "v_pk_add_f32 v[224:225], v[224:225], v[228:229]\n" \
  "v_pk_add_f32 v[226:227], v[226:227], v[230:231]\n" \
  "v_pk_add_f32 v[216:217], v[216:217], v[224:225]\n" \
  "v_pk_add_f32 v[218:219], v[218:219], v[226:227]\n" \
  "v_mul_f32 v248, 0xbfb8aa3b, v216\n" \
  "v_mul_f32 v249, 0xbfb8aa3b, v217\n" \
  "v_mul_f32 v250, 0xbfb8aa3b, v218\n" \
  "v_mul_f32 v251, 0xbfb8aa3b, v219\n" \
  "v_exp_f32 v248, v248\n" \
  "v_exp_f32 v249, v249\n" \
  "v_exp_f32 v250, v250\n" \
  "v_exp_f32 v251, v251\n" \
  "v_add_f32 v248, 1.0, v248\n" \
  "v_add_f32 v249, 1.0, v249\n" \
  "v_add_f32 v250, 1.0, v250\n" \
  "v_add_f32 v251, 1.0, v251\n" \
  "v_rcp_f32 v248, v248\n" \
  "v_rcp_f32 v249, v249\n" \
  "v_rcp_f32 v250, v250\n" \
  "v_rcp_f32 v251, v251\n" \
  "v_mul_f32 v216, v216, v248\n" \
  "v_mul_f32 v217, v217, v249\n" \
  "v_mul_f32 v218, v218, v250\n" \
  "v_mul_f32 v219, v219, v251\n" \
  "v_cvt_pk_bf16_f32 v204, v216, v217\n" \
  "v_cvt_pk_bf16_f32 v205, v218, v219\n" \
  "v_pk_add_f32 v[232:233], v[232:233], v[236:237]\n" \
  "v_pk_add_f32 v[234:235], v[234:235], v[238:239]\n" \
  "v_pk_add_f32 v[240:241], v[240:241], v[244:245]\n" \
  "v_pk_add_f32 v[242:243], v[242:243], v[246:247]\n" \
  "v_pk_add_f32 v[232:233], v[232:233], v[240:241]\n" \
  "v_pk_add_f32 v[234:235], v[234:235], v[242:243]\n" \
  "v_mul_f32 v248, 0xbfb8aa3b, v232\n" \
  "v_mul_f32 v249, 0xbfb8aa3b, v233\n" \
  "v_mul_f32 v250, 0xbfb8aa3b, v234\n" \
  "v_mul_f32 v251, 0xbfb8aa3b, v235\n" \
  "v_exp_f32 v248, v248\n" \
  "v_exp_f32 v249, v249\n" \
  "v_exp_f32 v250, v250\n" \
  "v_exp_f32 v251, v251\n" \
  "v_add_f32 v248, 1.0, v248\n" \
  "v_add_f32 v249, 1.0, v249\n" \
  "v_add_f32 v250, 1.0, v250\n" \
  "v_add_f32 v251, 1.0, v251\n" \
  "v_rcp_f32 v248, v248\n" \
  "v_rcp_f32 v249, v249\n" \
  "v_rcp_f32 v250, v250\n" \
  "v_rcp_f32 v251, v251\n" \
  "v_mul_f32 v232, v232, v248\n" \
  "v_mul_f32 v233, v233, v249\n" \
  "v_mul_f32 v234, v234, v250\n" \
  "v_mul_f32 v235, v235, v251\n" \
  "v_cvt_pk_bf16_f32 v206, v232, v233\n" \
  "v_cvt_pk_bf16_f32 v207, v234, v235\n" \
  "ds_write_b128 v8, v[144:147] offset:0\n" \
  "ds_write_b128 v8, v[148:151] offset:1024\n" \
  "ds_write_b128 v8, v[152:155] offset:2048\n" \
  "ds_write_b128 v8, v[156:159] offset:3072\n" \
  "ds_write_b128 v8, v[160:163] offset:16384\n" \
  "ds_write_b128 v8, v[164:167] offset:17408\n" \
  "ds_write_b128 v8, v[168:171] offset:18432\n" \
  "ds_write_b128 v8, v[172:175] offset:19456\n" \
  "s_waitcnt lgkmcnt(0)\n" \
  "s_barrier\n" \
  "ds_read_b128 v[216:219], v9 offset:0\n" \
  "ds_read_b128 v[220:223], v9 offset:4096\n" \
  "ds_read_b128 v[224:227], v9 offset:8192\n" \
  "ds_read_b128 v[228:231], v9 offset:12288\n" \
  "ds_read_b128 v[232:235], v9 offset:16384\n" \
  "ds_read_b128 v[236:239], v9 offset:20480\n" \
  "ds_read_b128 v[240:243], v9 offset:24576\n" \
  "ds_read_b128 v[244:247], v9 offset:28672\n" \
  "s_waitcnt lgkmcnt(0)\n" \
  "s_barrier\n" \
  "v_pk_add_f32 v[216:217], v[216:217], v[220:221]\n" \
  "v_pk_add_f32 v[218:219], v[218:219], v[222:223]\n" \
  "v_pk_add_f32 v[224:225], v[224:225], v[228:229]\n" \
  "v_pk_add_f32 v[226:227], v[226:227], v[230:231]\n" \
  "v_pk_add_f32 v[216:217], v[216:217], v[224:225]\n" \
  "v_pk_add_f32 v[218:219], v[218:219], v[226:227]\n" \
  "v_mul_f32 v248, 0xbfb8aa3b, v216\n" \
  "v_mul_f32 v249, 0xbfb8aa3b, v217\n" \
  "v_mul_f32 v250, 0xbfb8aa3b, v218\n" \
  "v_mul_f32 v251, 0xbfb8aa3b, v219\n" \
  "v_exp_f32 v248, v248\n" \
  "v_exp_f32 v249, v249\n" \
  "v_exp_f32 v250, v250\n" \
  "v_exp_f32 v251, v251\n" \
  "v_add_f32 v248, 1.0, v248\n" \
  "v_add_f32 v249, 1.0, v249\n" \
  "v_add_f32 v250, 1.0, v250\n" \
  "v_add_f32 v251, 1.0, v251\n" \
  "v_rcp_f32 v248, v248\n" \
  "v_rcp_f32 v249, v249\n" \
  "v_rcp_f32 v250, v250\n" \
  "v_rcp_f32 v251, v251\n" \
  "v_mul_f32 v216, v216, v248\n" \
  "v_mul_f32 v217, v217, v249\n" \
  "v_mul_f32 v218, v218, v250\n" \
  "v_mul_f32 v219, v219, v251\n" \
  "v_cvt_pk_bf16_f32 v208, v216, v217\n" \
  "v_cvt_pk_bf16_f32 v209, v218, v219\n" \
  "v_pk_add_f32 v[232:233], v[232:233], v[236:237]\n" \
  "v_pk_add_f32 v[234:235], v[234:235], v[238:239]\n" \
  "v_pk_add_f32 v[240:241], v[240:241], v[244:245]\n" \
  "v_pk_add_f32 v[242:243], v[242:243], v[246:247]\n" \
  "v_pk_add_f32 v[232:233], v[232:233], v[240:241]\n" \
  "v_pk_add_f32 v[234:235], v[234:235], v[242:243]\n" \
  "v_mul_f32 v248, 0xbfb8aa3b, v232\n" \
  "v_mul_f32 v249, 0xbfb8aa3b, v233\n" \
  "v_mul_f32 v250, 0xbfb8aa3b, v234\n" \
  "v_mul_f32 v251, 0xbfb8aa3b, v235\n" \
  "v_exp_f32 v248, v248\n" \
  "v_exp_f32 v249, v249\n" \
  "v_exp_f32 v250, v250\n" \
  "v_exp_f32 v251, v251\n" \
  "v_add_f32 v248, 1.0, v248\n" \
  "v_add_f32 v249, 1.0, v249\n" \
  "v_add_f32 v250, 1.0, v250\n" \
  "v_add_f32 v251, 1.0, v251\n" \
  "v_rcp_f32 v248, v248\n" \
  "v_rcp_f32 v249, v249\n" \
  "v_rcp_f32 v250, v250\n" \
  "v_rcp_f32 v251, v251\n" \
  "v_mul_f32 v232, v232, v248\n" \
  "v_mul_f32 v233, v233, v249\n" \
  "v_mul_f32 v234, v234, v250\n" \
  "v_mul_f32 v235, v235, v251\n" \
  "v_cvt_pk_bf16_f32 v210, v232, v233\n" \
  "v_cvt_pk_bf16_f32 v211, v234, v235\n" \
  "ds_write_b128 v8, v[176:179] offset:0\n" \
  "ds_write_b128 v8, v[180:183] offset:1024\n" \
  "ds_write_b128 v8, v[184:187] offset:2048\n" \
  "ds_write_b128 v8, v[188:191] offset:3072\n" \
  "s_waitcnt lgkmcnt(0)\n" \
  "s_barrier\n" \
  "ds_read_b128 v[216:219], v9 offset:0\n" \
  "ds_read_b128 v[220:223], v9 offset:4096\n" \
  "ds_read_b128 v[224:227], v9 offset:8192\n" \
  "ds_read_b128 v[228:231], v9 offset:12288\n" \
  "s_waitcnt lgkmcnt(0)\n" \
  "s_barrier\n" \
  "v_pk_add_f32 v[216:217], v[216:217], v[220:221]\n" \
  "v_pk_add_f32 v[218:219], v[218:219], v[222:223]\n" \
  "v_pk_add_f32 v[224:225], v[224:225], v[228:229]\n" \
  "v_pk_add_f32 v[226:227], v[226:227], v[230:231]\n" \
  "v_pk_add_f32 v[216:217], v[216:217], v[224:225]\n" \
  "v_pk_add_f32 v[218:219], v[218:219], v[226:227]\n" \
  "v_mul_f32 v248, 0xbfb8aa3b, v216\n" \
  "v_mul_f32 v249, 0xbfb8aa3b, v217\n" \
  "v_mul_f32 v250, 0xbfb8aa3b, v218\n" \
  "v_mul_f32 v251, 0xbfb8aa3b, v219\n" \
  "v_exp_f32 v248, v248\n" \
  "v_exp_f32 v249, v249\n" \
  "v_exp_f32 v250, v250\n" \
  "v_exp_f32 v251, v251\n" \
  "v_add_f32 v248, 1.0, v248\n" \
  "v_add_f32 v249, 1.0, v249\n" \
  "v_add_f32 v250, 1.0, v250\n" \
  "v_add_f32 v251, 1.0, v251\n" \
  "v_rcp_f32 v248, v248\n" \
  "v_rcp_f32 v249, v249\n" \
  "v_rcp_f32 v250, v250\n" \
  "v_rcp_f32 v251, v251\n" \
  "v_mul_f32 v216, v216, v248\n" \
  "v_mul_f32 v217, v217, v249\n" \
  "v_mul_f32 v218, v218, v250\n" \
  "v_mul_f32 v219, v219, v251\n" \
  "v_cvt_pk_bf16_f32 v212, v216, v217\n" \
  "v_cvt_pk_bf16_f32 v213, v218, v219\n" \
  "s_barrier\n" \
  "s_lshl_b32 s52, s48, 9\n" \
  "s_add_u32 s52, s52, 139264\n" \
  "v_lshrrev_b32 v10, 1, v1\n" \
  "v_add_u32 v10, s52, v10\n" \
  "ds_write_b64 v10, v[192:193] offset:0\n" \
  "ds_write_b64 v10, v[194:195] offset:2048\n" \
  "ds_write_b64 v10, v[196:197] offset:4096\n" \
  "ds_write_b64 v10, v[198:199] offset:6144\n" \
  "ds_write_b64 v10, v[200:201] offset:8192\n" \
  "ds_write_b64 v10, v[202:203] offset:10240\n" \
  "ds_write_b64 v10, v[204:205] offset:12288\n" \
  "ds_write_b64 v10, v[206:207] offset:14336\n" \
  "ds_write_b64 v10, v[208:209] offset:16384\n" \
  "ds_write_b64 v10, v[210:211] offset:18432\n" \
  "ds_write_b64 v10, v[212:213] offset:20480\n" \
  "s_waitcnt vmcnt(0)\n" \
  "ds_read_b64_tr_b16 a[0:1], v12 offset:0\n" \
  "ds_read_b64_tr_b16 a[2:3], v12 offset:2048\n" \
  "ds_read_b64_tr_b16 a[64:65], v13 offset:0\n" \
  "ds_read_b64_tr_b16 a[66:67], v13 offset:2048\n" \
  "ds_read_b64_tr_b16 a[128:129], v14 offset:0\n" \
  "ds_read_b64_tr_b16 a[130:131], v14 offset:2048\n" \
  "ds_read_b64_tr_b16 a[192:193], v15 offset:0\n" \
  "ds_read_b64_tr_b16 a[194:195], v15 offset:2048\n" \
  "ds_read_b64_tr_b16 a[4:5], v12 offset:4096\n" \
  "ds_read_b64_tr_b16 a[6:7], v12 offset:6144\n" \
  "ds_read_b64_tr_b16 a[68:69], v13 offset:4096\n" \
  "ds_read_b64_tr_b16 a[70:71], v13 offset:6144\n" \
  "ds_read_b64_tr_b16 a[132:133], v14 offset:4096\n" \
  "ds_read_b64_tr_b16 a[134:135], v14 offset:6144\n" \
  "ds_read_b64_tr_b16 a[196:197], v15 offset:4096\n" \
  "ds_read_b64_tr_b16 a[198:199], v15 offset:6144\n" \
  "ds_read_b64_tr_b16 a[8:9], v12 offset:8192\n" \
  "ds_read_b64_tr_b16 a[10:11], v12 offset:10240\n" \
  "ds_read_b64_tr_b16 a[72:73], v13 offset:8192\n" \
  "ds_read_b64_tr_b16 a[74:75], v13 offset:10240\n" \
  "ds_read_b64_tr_b16 a[136:137], v14 offset:8192\n" \
  "ds_read_b64_tr_b16 a[138:139], v14 offset:10240\n" \
  "ds_read_b64_tr_b16 a[200:201], v15 offset:8192\n" \
  "ds_read_b64_tr_b16 a[202:203], v15 offset:10240\n" \
  "ds_read_b64_tr_b16 a[12:13], v12 offset:12288\n" \
  "ds_read_b64_tr_b16 a[14:15], v12 offset:14336\n" \
  "ds_read_b64_tr_b16 a[76:77], v13 offset:12288\n" \
  "ds_read_b64_tr_b16 a[78:79], v13 offset:14336\n" \
  "ds_read_b64_tr_b16 a[140:141], v14 offset:12288\n" \
  "ds_read_b64_tr_b16 a[142:143], v14 offset:14336\n" \
  "ds_read_b64_tr_b16 a[204:205], v15 offset:12288\n" \
  "ds_read_b64_tr_b16 a[206:207], v15 offset:14336\n" \
  "ds_read_b64_tr_b16 a[16:17], v12 offset:16384\n" \
  "ds_read_b64_tr_b16 a[18:19], v12 offset:18432\n" \
  "ds_read_b64_tr_b16 a[80:81], v13 offset:16384\n" \
  "ds_read_b64_tr_b16 a[82:83], v13 offset:18432\n" \
  "ds_read_b64_tr_b16 a[144:145], v14 offset:16384\n" \
  "ds_read_b64_tr_b16 a[146:147], v14 offset:18432\n" \
  "ds_read_b64_tr_b16 a[208:209], v15 offset:16384\n" \
  "ds_read_b64_tr_b16 a[210:211], v15 offset:18432\n" \
  "ds_read_b64_tr_b16 a[20:21], v12 offset:20480\n" \
  "ds_read_b64_tr_b16 a[22:23], v12 offset:22528\n" \
  "ds_read_b64_tr_b16 a[84:85], v13 offset:20480\n" \
  "ds_read_b64_tr_b16 a[86:87], v13 offset:22528\n" \
  "ds_read_b64_tr_b16 a[148:149], v14 offset:20480\n" \
  "ds_read_b64_tr_b16 a[150:151], v14 offset:22528\n" \
  "ds_read_b64_tr_b16 a[212:213], v15 offset:20480\n" \
  "ds_read_b64_tr_b16 a[214:215], v15 offset:22528\n" \
  "ds_read_b64_tr_b16 a[24:25], v12 offset:24576\n" \
  "ds_read_b64_tr_b16 a[26:27], v12 offset:26624\n" \
  "ds_read_b64_tr_b16 a[88:89], v13 offset:24576\n" \
  "ds_read_b64_tr_b16 a[90:91], v13 offset:26624\n" \
  "ds_read_b64_tr_b16 a[152:153], v14 offset:24576\n" \
  "ds_read_b64_tr_b16 a[154:155], v14 offset:26624\n" \
  "ds_read_b64_tr_b16 a[216:217], v15 offset:24576\n" \
  "ds_read_b64_tr_b16 a[218:219], v15 offset:26624\n" \
  "ds_read_b64_tr_b16 a[28:29], v12 offset:28672\n" \
  "ds_read_b64_tr_b16 a[30:31], v12 offset:30720\n" \
  "ds_read_b64_tr_b16 a[92:93], v13 offset:28672\n" \
  "ds_read_b64_tr_b16 a[94:95], v13 offset:30720\n" \
  "ds_read_b64_tr_b16 a[156:157], v14 offset:28672\n" \
  "ds_read_b64_tr_b16 a[158:159], v14 offset:30720\n" \
  "ds_read_b64_tr_b16 a[220:221], v15 offset:28672\n" \
  "ds_read_b64_tr_b16 a[222:223], v15 offset:30720\n" \
  "s_waitcnt lgkmcnt(0)\n" \
  "s_lshl_b32 s52, s63, 2\n" \
  "s_add_u32 s52, s52, s48\n" \
  "s_lshl_b32 s52, s52, 1\n" \
  "s_add_u32 s52, s52, 1\n" \
  "s_mul_i32 s52, s52, 327680\n" \
  "s_lshl_b32 s53, s64, 7\n" \
  "s_add_u32 s52, s52, s53\n" \
  "s_add_u32 s80, s24, s52\n" \
  "s_addc_u32 s81, s25, 0\n" \
  "s_add_u32 m0, s72, 0\n" \
  "s_nop 0\n" \
  "global_load_lds_dwordx4 v11, s[80:81]\n" \
  "s_waitcnt vmcnt(4)\n" \
  "s_add_u32 s80, s80, 10240\n" \
  "s_addc_u32 s81, s81, 0\n" \
  "s_add_u32 m0, s72, 1024\n" \
  "s_nop 0\n" \
  "global_load_lds_dwordx4 v11, s[80:81]\n" \
  "s_waitcnt vmcnt(4)\n" \
  "s_add_u32 s80, s80, 10240\n" \
  "s_addc_u32 s81, s81, 0\n" \
  "s_add_u32 m0, s72, 2048\n" \
  "s_nop 0\n" \
  "global_load_lds_dwordx4 v11, s[80:81]\n" \
  "s_waitcnt vmcnt(4)\n" \
  "s_add_u32 s80, s80, 10240\n" \
  "s_addc_u32 s81, s81, 0\n" \
  "s_add_u32 m0, s72, 3072\n" \
  "s_nop 0\n" \
  "global_load_lds_dwordx4 v11, s[80:81]\n" \
  "s_waitcnt vmcnt(4)\n" \
  "s_add_u32 s80, s80, 10240\n" \
  "s_addc_u32 s81, s81, 0\n" \
  "s_add_u32 m0, s72, 4096\n" \
  "s_nop 0\n" \
  "global_load_lds_dwordx4 v11, s[80:81]\n" \
  "s_waitcnt vmcnt(4)\n" \
  "s_add_u32 s80, s80, 10240\n" \
  "s_addc_u32 s81, s81, 0\n" \
  "s_add_u32 m0, s72, 5120\n" \
  "s_nop 0\n" \
  "global_load_lds_dwordx4 v11, s[80:81]\n" \
  "s_waitcnt vmcnt(4)\n" \
  "s_add_u32 s80, s80, 10240\n" \
  "s_addc_u32 s81, s81, 0\n" \
  "s_add_u32 m0, s72, 6144\n" \
  "s_nop 0\n" \
  "global_load_lds_dwordx4 v11, s[80:81]\n" \
  "s_waitcnt vmcnt(4)\n" \
  "s_add_u32 s80, s80, 10240\n" \
  "s_addc_u32 s81, s81, 0\n" \
  "s_add_u32 m0, s72, 7168\n" \
  "s_nop 0\n" \
  "global_load_lds_dwordx4 v11, s[80:81]\n" \
  "s_waitcnt vmcnt(4)\n" \
  "s_add_u32 s80, s80, 10240\n" \
  "s_addc_u32 s81, s81, 0\n" \
  "s_add_u32 m0, s72, 8192\n" \
  "s_nop 0\n" \
  "global_load_lds_dwordx4 v11, s[80:81]\n" \
  "s_waitcnt vmcnt(4)\n" \
  "s_add_u32 s80, s80, 10240\n" \
  "s_addc_u32 s81, s81, 0\n" \
  "s_add_u32 m0, s72, 9216\n" \
  "s_nop 0\n" \
  "global_load_lds_dwordx4 v11, s[80:81]\n" \
  "s_waitcnt vmcnt(4)\n" \
  "s_add_u32 s80, s80, 10240\n" \
  "s_addc_u32 s81, s81, 0\n" \
  "s_add_u32 m0, s72, 10240\n" \
  "s_nop 0\n" \
  "global_load_lds_dwordx4 v11, s[80:81]\n" \
  "s_waitcnt vmcnt(4)\n" \
  "s_add_u32 s80, s80, 10240\n" \
  "s_addc_u32 s81, s81, 0\n" \
  "s_add_u32 m0, s72, 11264\n" \
  "s_nop 0\n" \
  "global_load_lds_dwordx4 v11, s[80:81]\n" \
  "s_waitcnt vmcnt(4)\n" \
  "s_add_u32 s80, s80, 10240\n" \
  "s_addc_u32 s81, s81, 0\n" \
  "s_add_u32 m0, s72, 12288\n" \
  "s_nop 0\n" \
  "global_load_lds_dwordx4 v11, s[80:81]\n" \
  "s_waitcnt vmcnt(4)\n" \
  "s_add_u32 s80, s80, 10240\n" \
  "s_addc_u32 s81, s81, 0\n" \
  "s_add_u32 m0, s72, 13312\n" \
  "s_nop 0\n" \
  "global_load_lds_dwordx4 v11, s[80:81]\n" \
  "s_waitcnt vmcnt(4)\n" \
  "s_add_u32 s80, s80, 10240\n" \
  "s_addc_u32 s81, s81, 0\n" \
  "s_add_u32 m0, s72, 14336\n" \
  "s_nop 0\n" \
  "global_load_lds_dwordx4 v11, s[80:81]\n" \
  "s_waitcnt vmcnt(4)\n" \
  "s_add_u32 s80, s80, 10240\n" \
  "s_addc_u32 s81, s81, 0\n" \
  "s_add_u32 m0, s72, 15360\n" \
  "s_nop 0\n" \
  "global_load_lds_dwordx4 v11, s[80:81]\n" \
  "s_waitcnt vmcnt(4)\n" \
  "s_add_u32 s80, s80, 10240\n" \
  "s_addc_u32 s81, s81, 0\n" \
  "s_add_u32 m0, s72, 16384\n" \
  "s_nop 0\n" \
  "global_load_lds_dwordx4 v11, s[80:81]\n" \
  "s_waitcnt vmcnt(4)\n" \
  "s_add_u32 s80, s80, 10240\n" \
  "s_addc_u32 s81, s81, 0\n" \
  "s_add_u32 m0, s72, 17408\n" \
  "s_nop 0\n" \
  "global_load_lds_dwordx4 v11, s[80:81]\n" \
  "s_waitcnt vmcnt(4)\n" \
  "s_add_u32 s80, s80, 10240\n" \
  "s_addc_u32 s81, s81, 0\n" \
  "s_add_u32 m0, s72, 18432\n" \
  "s_nop 0\n" \
  "global_load_lds_dwordx4 v11, s[80:81]\n" \
  "s_waitcnt vmcnt(4)\n" \
  "s_add_u32 s80, s80, 10240\n" \
  "s_addc_u32 s81, s81, 0\n" \
  "s_add_u32 m0, s72, 19456\n" \
  "s_nop 0\n" \
  "global_load_lds_dwordx4 v11, s[80:81]\n" \
  "s_waitcnt vmcnt(4)\n" \
  "s_add_u32 s80, s80, 10240\n" \
  "s_addc_u32 s81, s81, 0\n" \
  "s_add_u32 m0, s72, 20480\n" \
  "s_nop 0\n" \
  "global_load_lds_dwordx4 v11, s[80:81]\n" \
  "s_waitcnt vmcnt(4)\n" \
  "s_add_u32 s80, s80, 10240\n" \
  "s_addc_u32 s81, s81, 0\n" \
  "s_add_u32 m0, s72, 21504\n" \
  "s_nop 0\n" \
  "global_load_lds_dwordx4 v11, s[80:81]\n" \
  "s_waitcnt vmcnt(4)\n" \
  "s_add_u32 s80, s80, 10240\n" \
  "s_addc_u32 s81, s81, 0\n" \
  "s_add_u32 m0, s72, 22528\n" \
  "s_nop 0\n" \
  "global_load_lds_dwordx4 v11, s[80:81]\n" \
  "s_waitcnt vmcnt(4)\n" \
  "s_add_u32 s80, s80, 10240\n" \
  "s_addc_u32 s81, s81, 0\n" \
  "s_add_u32 m0, s72, 23552\n" \
  "s_nop 0\n" \
  "global_load_lds_dwordx4 v11, s[80:81]\n" \
  "s_waitcnt vmcnt(4)\n" \
  "s_add_u32 s80, s80, 10240\n" \
  "s_addc_u32 s81, s81, 0\n" \
  "s_add_u32 m0, s72, 24576\n" \
  "s_nop 0\n" \
  "global_load_lds_dwordx4 v11, s[80:81]\n" \
  "s_waitcnt vmcnt(4)\n" \
  "s_add_u32 s80, s80, 10240\n" \
  "s_addc_u32 s81, s81, 0\n" \
  "s_add_u32 m0, s72, 25600\n" \
  "s_nop 0\n" \
  "global_load_lds_dwordx4 v11, s[80:81]\n" \
  "s_waitcnt vmcnt(4)\n" \
  "s_add_u32 s80, s80, 10240\n" \
  "s_addc_u32 s81, s81, 0\n" \
  "s_add_u32 m0, s72, 26624\n" \
  "s_nop 0\n" \
  "global_load_lds_dwordx4 v11, s[80:81]\n" \
  "s_waitcnt vmcnt(4)\n" \
  "s_add_u32 s80, s80, 10240\n" \
  "s_addc_u32 s81, s81, 0\n" \
  "s_add_u32 m0, s72, 27648\n" \
  "s_nop 0\n" \
  "global_load_lds_dwordx4 v11, s[80:81]\n" \
  "s_waitcnt vmcnt(4)\n" \
  "s_add_u32 s80, s80, 10240\n" \
  "s_addc_u32 s81, s81, 0\n" \
  "s_add_u32 m0, s72, 28672\n" \
  "s_nop 0\n" \
  "global_load_lds_dwordx4 v11, s[80:81]\n" \
  "s_waitcnt vmcnt(4)\n" \
  "s_add_u32 s80, s80, 10240\n" \
  "s_addc_u32 s81, s81, 0\n" \
  "s_add_u32 m0, s72, 29696\n" \
  "s_nop 0\n" \
  "global_load_lds_dwordx4 v11, s[80:81]\n" \
  "s_waitcnt vmcnt(4)\n" \
  "s_add_u32 s80, s80, 10240\n" \
  "s_addc_u32 s81, s81, 0\n" \
  "s_add_u32 m0, s72, 30720\n" \
  "s_nop 0\n" \
  "global_load_lds_dwordx4 v11, s[80:81]\n" \
  "s_waitcnt vmcnt(4)\n" \
  "s_add_u32 s80, s80, 10240\n" \
  "s_addc_u32 s81, s81, 0\n" \
  "s_add_u32 m0, s72, 31744\n" \
  "s_nop 0\n" \
  "global_load_lds_dwordx4 v11, s[80:81]\n" \
  "s_waitcnt vmcnt(4)\n" \
  "s_waitcnt vmcnt(0)\n" \
  "ds_read_b64_tr_b16 a[32:33], v12 offset:0\n" \
  "ds_read_b64_tr_b16 a[34:35], v12 offset:2048\n" \
  "ds_read_b64_tr_b16 a[96:97], v13 offset:0\n" \
  "ds_read_b64_tr_b16 a[98:99], v13 offset:2048\n" \
  "ds_read_b64_tr_b16 a[160:161], v14 offset:0\n" \
  "ds_read_b64_tr_b16 a[162:163], v14 offset:2048\n" \
  "ds_read_b64_tr_b16 a[224:225], v15 offset:0\n" \
  "ds_read_b64_tr_b16 a[226:227], v15 offset:2048\n" \
  "ds_read_b64_tr_b16 a[36:37], v12 offset:4096\n" \
  "ds_read_b64_tr_b16 a[38:39], v12 offset:6144\n" \
  "ds_read_b64_tr_b16 a[100:101], v13 offset:4096\n" \
  "ds_read_b64_tr_b16 a[102:103], v13 offset:6144\n" \
  "ds_read_b64_tr_b16 a[164:165], v14 offset:4096\n" \
  "ds_read_b64_tr_b16 a[166:167], v14 offset:6144\n" \
  "ds_read_b64_tr_b16 a[228:229], v15 offset:4096\n" \
  "ds_read_b64_tr_b16 a[230:231], v15 offset:6144\n" \
  "ds_read_b64_tr_b16 a[40:41], v12 offset:8192\n" \
  "ds_read_b64_tr_b16 a[42:43], v12 offset:10240\n" \
  "ds_read_b64_tr_b16 a[104:105], v13 offset:8192\n" \
  "ds_read_b64_tr_b16 a[106:107], v13 offset:10240\n" \
  "ds_read_b64_tr_b16 a[168:169], v14 offset:8192\n" \
  "ds_read_b64_tr_b16 a[170:171], v14 offset:10240\n" \
  "ds_read_b64_tr_b16 a[232:233], v15 offset:8192\n" \
  "ds_read_b64_tr_b16 a[234:235], v15 offset:10240\n" \
  "ds_read_b64_tr_b16 a[44:45], v12 offset:12288\n" \
  "ds_read_b64_tr_b16 a[46:47], v12 offset:14336\n" \
  "ds_read_b64_tr_b16 a[108:109], v13 offset:12288\n" \
  "ds_read_b64_tr_b16 a[110:111], v13 offset:14336\n" \
  "ds_read_b64_tr_b16 a[172:173], v14 offset:12288\n" \
  "ds_read_b64_tr_b16 a[174:175], v14 offset:14336\n" \
  "ds_read_b64_tr_b16 a[236:237], v15 offset:12288\n" \
  "ds_read_b64_tr_b16 a[238:239], v15 offset:14336\n" \
  "ds_read_b64_tr_b16 a[48:49], v12 offset:16384\n" \
  "ds_read_b64_tr_b16 a[50:51], v12 offset:18432\n" \
  "ds_read_b64_tr_b16 a[112:113], v13 offset:16384\n" \
  "ds_read_b64_tr_b16 a[114:115], v13 offset:18432\n" \
  "ds_read_b64_tr_b16 a[176:177], v14 offset:16384\n" \
  "ds_read_b64_tr_b16 a[178:179], v14 offset:18432\n" \
  "ds_read_b64_tr_b16 a[240:241], v15 offset:16384\n" \
  "ds_read_b64_tr_b16 a[242:243], v15 offset:18432\n" \
  "ds_read_b64_tr_b16 a[52:53], v12 offset:20480\n" \
  "ds_read_b64_tr_b16 a[54:55], v12 offset:22528\n" \
  "ds_read_b64_tr_b16 a[116:117], v13 offset:20480\n" \
  "ds_read_b64_tr_b16 a[118:119], v13 offset:22528\n" \
  "ds_read_b64_tr_b16 a[180:181], v14 offset:20480\n" \
  "ds_read_b64_tr_b16 a[182:183], v14 offset:22528\n" \
  "ds_read_b64_tr_b16 a[244:245], v15 offset:20480\n" \
  "ds_read_b64_tr_b16 a[246:247], v15 offset:22528\n" \
  "ds_read_b64_tr_b16 a[56:57], v12 offset:24576\n" \
  "ds_read_b64_tr_b16 a[58:59], v12 offset:26624\n" \
  "ds_read_b64_tr_b16 a[120:121], v13 offset:24576\n" \
  "ds_read_b64_tr_b16 a[122:123], v13 offset:26624\n" \
  "ds_read_b64_tr_b16 a[184:185], v14 offset:24576\n" \
  "ds_read_b64_tr_b16 a[186:187], v14 offset:26624\n" \
  "ds_read_b64_tr_b16 a[248:249], v15 offset:24576\n" \
  "ds_read_b64_tr_b16 a[250:251], v15 offset:26624\n" \
  "ds_read_b64_tr_b16 a[60:61], v12 offset:28672\n" \
  "ds_read_b64_tr_b16 a[62:63], v12 offset:30720\n" \
  "ds_read_b64_tr_b16 a[124:125], v13 offset:28672\n" \
  "ds_read_b64_tr_b16 a[126:127], v13 offset:30720\n" \
  "ds_read_b64_tr_b16 a[188:189], v14 offset:28672\n" \
  "ds_read_b64_tr_b16 a[190:191], v14 offset:30720\n" \
  "ds_read_b64_tr_b16 a[252:253], v15 offset:28672\n" \
  "ds_read_b64_tr_b16 a[254:255], v15 offset:30720\n" \
  "s_waitcnt lgkmcnt(0)\n" \
  "s_barrier\n" \
  "v_add_u32 v11, 4096, v1\n" \
  "s_lshl_b32 s52, s63, 2\n" \
  "s_add_u32 s52, s52, s48\n" \
  "s_lshl_b32 s52, s52, 2\n" \
  "v_add_u32 v4, s52, v3\n" \
  "v_mul_u32_u24 v12, 1280, v4\n" \
  "v_lshl_add_u32 v12, v2, 3, v12\n" \
  "s_lshl_b32 s52, s64, 7\n" \
  "v_add_u32 v12, s52, v12\n" \
  "v_lshlrev_b32 v13, 6, v2\n" \
  "s_lshl_b32 s52, s63, 2\n" \
  "s_add_u32 s52, s52, s48\n" \
  "s_lshl_b32 s61, s52, 10\n" \
  "s_add_u32 s61, s61, 12591104\n" \
  "s_lshl_b32 s60, s52, 21\n" \
  "s_add_u32 s60, s60, 33554432\n" \
  "s_lshl_b32 s52, s48, 13\n" \
  "v_add_u32 v8, s52, v1\n" \
  "s_lshl_b32 s52, s63, 12\n" \
  "v_add_u32 v9, s52, v7\n" \
  "v_xor_b32 v14, 4096, v9\n" \
  "s_mov_b32 s66, s65\n" \
  "s_mov_b32 s68, 0\n" \
  "s_mov_b32 s67, 0\n" \
  "v_mov_b32 v188, 0\n" \
  "WLOOP4_%=:\n" \
  "s_cmp_lg_u32 s67, 0\n" \
  "s_cbranch_scc1 WGO_%=\n" \
  "s_cmp_eq_u32 s68, 0\n" \
  "s_cbranch_scc1 WPL_%=\n" \
  "s_lshl_b32 s52, s68, 12\n" \
  "s_add_u32 s52, s52, s71\n" \
  "s_sub_u32 s76, s52, 4096\n" \
  "buffer_load_dwordx4 v[180:183], v1, s[36:39], s76 offen offset:0 sc1\n" \
  "s_sub_u32 s52, s66, 6\n" \
  "s_mul_i32 s57, s52, 40960\n" \
  "buffer_load_dwordx2 v[184:185], v12, s[44:47], s57 offen offset:0 \n" \
  "s_lshl_b32 s52, s68, 11\n" \
  "s_sub_u32 s52, s52, 2048\n" \
  "v_add_u32 v6, s52, v10\n" \
  "ds_read_b64 v[186:187], v6 offset:0\n" \
  "WFC1_%=:\n" \
  "s_waitcnt vmcnt(1)\n" \
  "v_xor_b32 v4, s77, v180\n" \
  "v_xor_b32 v5, s77, v181\n" \
  "v_min_u32 v4, v4, v5\n" \
  "v_xor_b32 v5, s77, v182\n" \
  "v_min_u32 v4, v4, v5\n" \
  "v_xor_b32 v5, s77, v183\n" \
  "v_min_u32 v4, v4, v5\n" \
  "v_cmp_eq_u32 vcc, 0, v4\n" \
  "s_nop 3\n" \
  "s_cbranch_vccz WFD1_%=\n" \
  "s_sleep 1\n" \
  "s_add_u32 s62, s62, 1\n" \
  "s_cmp_gt_u32 s62, 131072\n" \
  "s_cbranch_scc1 WFAIL_%=\n" \
  "buffer_load_dwordx4 v[180:183], v1, s[36:39], s76 offen sc1\n" \
  "s_waitcnt vmcnt(0)\n" \
  "s_branch WFC1_%=\n" \
  "WFD1_%=:\n" \
  "v_pk_add_f32 v[180:181], v[180:181], v[176:177]\n" \
  "v_pk_add_f32 v[182:183], v[182:183], v[178:179]\n" \
  "s_waitcnt lgkmcnt(0)\n" \
  "v_lshlrev_b32 v4, 16, v186\n" \
  "s_waitcnt vmcnt(0)\n" \
  "v_lshlrev_b32 v5, 16, v184\n" \
  "v_fma_f32 v180, v180, v4, v5\n" \
  "v_and_b32 v4, 0xffff0000, v186\n" \
  "v_and_b32 v5, 0xffff0000, v184\n" \
  "v_fma_f32 v181, v181, v4, v5\n" \
  "v_cvt_pk_bf16_f32 v184, v180, v181\n" \
  "v_lshlrev_b32 v4, 16, v187\n" \
  "v_lshlrev_b32 v5, 16, v185\n" \
  "v_fma_f32 v182, v182, v4, v5\n" \
  "v_and_b32 v4, 0xffff0000, v187\n" \
  "v_and_b32 v5, 0xffff0000, v185\n" \
  "v_fma_f32 v183, v183, v4, v5\n" \
  "v_cvt_pk_bf16_f32 v185, v182, v183\n" \
  "buffer_store_dwordx2 v[184:185], v12, s[40:43], s57 offen offset:0 sc1\n" \
  "s_cmp_ge_u32 s66, 64\n" \
  "s_cbranch_scc1 WDONE_%=\n" \
  "WPL_%=:\n" \
  "buffer_load_dword v188, v13, s[36:39], s61 offen sc1\n" \
  "s_waitcnt vmcnt(0)\n" \
  "v_cmp_ge_u32 vcc, s66, v188\n" \
  "s_nop 4\n" \
  "s_cbranch_vccz WPD_%=\n" \
  "s_sleep 2\n" \
  "s_add_u32 s62, s62, 1\n" \
  "s_cmp_gt_u32 s62, 131072\n" \
  "s_cbranch_scc1 WFAIL_%=\n" \
  "s_branch WPL_%=\n" \
  "WPD_%=:\n" \
  "s_add_u32 s52, s66, 0\n" \
  "s_lshl_b32 s52, s52, 13\n" \
  "s_add_u32 s72, s60, s52\n" \
  "s_add_u32 s73, s72, 524288\n" \
  "s_add_u32 s74, s73, 524288\n" \
  "s_add_u32 s75, s74, 524288\n" \
  "buffer_load_dwordx4 v[16:19], v1, s[36:39], s72 offen offset:0 sc1\n" \
  "buffer_load_dwordx4 v[20:23], v11, s[36:39], s72 offen offset:0 sc1\n" \
  "buffer_load_dwordx4 v[24:27], v1, s[36:39], s72 offen offset:1024 sc1\n" \
  "buffer_load_dwordx4 v[28:31], v11, s[36:39], s72 offen offset:1024 sc1\n" \
  "buffer_load_dwordx4 v[32:35], v1, s[36:39], s72 offen offset:2048 sc1\n" \
  "buffer_load_dwordx4 v[36:39], v11, s[36:39], s72 offen offset:2048 sc1\n" \
  "buffer_load_dwordx4 v[40:43], v1, s[36:39], s72 offen offset:3072 sc1\n" \
  "buffer_load_dwordx4 v[44:47], v11, s[36:39], s72 offen offset:3072 sc1\n" \
  "buffer_load_dwordx4 v[48:51], v1, s[36:39], s73 offen offset:0 sc1\n" \
  "buffer_load_dwordx4 v[52:55], v11, s[36:39], s73 offen offset:0 sc1\n" \
  "buffer_load_dwordx4 v[56:59], v1, s[36:39], s73 offen offset:1024 sc1\n" \
  "buffer_load_dwordx4 v[60:63], v11, s[36:39], s73 offen offset:1024 sc1\n" \
  "buffer_load_dwordx4 v[64:67], v1, s[36:39], s73 offen offset:2048 sc1\n" \
  "buffer_load_dwordx4 v[68:71], v11, s[36:39], s73 offen offset:2048 sc1\n" \
  "buffer_load_dwordx4 v[72:75], v1, s[36:39], s73 offen offset:3072 sc1\n" \
  "buffer_load_dwordx4 v[76:79], v11, s[36:39], s73 offen offset:3072 sc1\n" \
  "buffer_load_dwordx4 v[80:83], v1, s[36:39], s74 offen offset:0 sc1\n" \
  "buffer_load_dwordx4 v[84:87], v11, s[36:39], s74 offen offset:0 sc1\n" \
  "buffer_load_dwordx4 v[88:91], v1, s[36:39], s74 offen offset:1024 sc1\n" \
  "buffer_load_dwordx4 v[92:95], v11, s[36:39], s74 offen offset:1024 sc1\n" \
  "buffer_load_dwordx4 v[96:99], v1, s[36:39], s74 offen offset:2048 sc1\n" \
  "buffer_load_dwordx4 v[100:103], v11, s[36:39], s74 offen offset:2048 sc1\n" \
  "buffer_load_dwordx4 v[104:107], v1, s[36:39], s74 offen offset:3072 sc1\n" \
  "buffer_load_dwordx4 v[108:111], v11, s[36:39], s74 offen offset:3072 sc1\n" \
  "buffer_load_dwordx4 v[112:115], v1, s[36:39], s75 offen offset:0 sc1\n" \
  "buffer_load_dwordx4 v[116:119], v11, s[36:39], s75 offen offset:0 sc1\n" \
  "buffer_load_dwordx4 v[120:123], v1, s[36:39], s75 offen offset:1024 sc1\n" \
  "buffer_load_dwordx4 v[124:127], v11, s[36:39], s75 offen offset:1024 sc1\n" \
  "buffer_load_dwordx4 v[128:131], v1, s[36:39], s75 offen offset:2048 sc1\n" \
  "buffer_load_dwordx4 v[132:135], v11, s[36:39], s75 offen offset:2048 sc1\n" \
  "buffer_load_dwordx4 v[136:139], v1, s[36:39], s75 offen offset:3072 sc1\n" \
  "buffer_load_dwordx4 v[140:143], v11, s[36:39], s75 offen offset:3072 sc1\n" \
  "s_mov_b32 s67, 0\n" \
  "WGO_%=:\n" \
  "s_add_u32 s53, s66, 6\n" \
  "v_cmp_ge_u32 vcc, s53, v188\n" \
  "s_cmp_lt_u32 s53, 64\n" \
  "s_cselect_b32 s54, 1, 0\n" \
  "s_nop 2\n" \
  "s_cmp_eq_u64 vcc, 0\n" \
  "s_cselect_b32 s54, s54, 0\n" \
  "s_lshl_b32 s67, s67, 1\n" \
  "s_or_b32 s67, s67, s54\n" \
  "s_cmp_eq_u32 s67, 1\n" \
  "s_cbranch_scc1 WV1_%=\n" \
  "s_cmp_eq_u32 s67, 2\n" \
  "s_cbranch_scc1 WV2_%=\n" \
  "s_cmp_eq_u32 s67, 3\n" \
  "s_cbranch_scc1 WV3_%=\n" \
  "WV0_%=:\n" \
  "s_waitcnt vmcnt(31)\n" \
  "v_mfma_f32_16x16x32_bf16 v[144:147], a[0:3], v[16:19], 0\n" \
  "v_mfma_f32_16x16x32_bf16 v[148:151], a[64:67], v[16:19], 0\n" \
  "v_mfma_f32_16x16x32_bf16 v[152:155], a[128:131], v[16:19], 0\n" \
  "v_mfma_f32_16x16x32_bf16 v[156:159], a[192:195], v[16:19], 0\n" \
  "s_waitcnt vmcnt(30)\n" \
  "v_mfma_f32_16x16x32_bf16 v[160:163], a[0:3], v[20:23], 0\n" \
  "v_mfma_f32_16x16x32_bf16 v[164:167], a[64:67], v[20:23], 0\n" \
  "v_mfma_f32_16x16x32_bf16 v[168:171], a[128:131], v[20:23], 0\n" \
  "v_mfma_f32_16x16x32_bf16 v[172:175], a[192:195], v[20:23], 0\n" \
  "s_waitcnt vmcnt(29)\n" \
  "v_mfma_f32_16x16x32_bf16 v[144:147], a[4:7], v[24:27], v[144:147]\n" \
  "v_mfma_f32_16x16x32_bf16 v[148:151], a[68:71], v[24:27], v[148:151]\n" \
  "v_mfma_f32_16x16x32_bf16 v[152:155], a[132:135], v[24:27], v[152:155]\n" \
  "v_mfma_f32_16x16x32_bf16 v[156:159], a[196:199], v[24:27], v[156:159]\n" \
  "s_waitcnt vmcnt(28)\n" \
  "v_mfma_f32_16x16x32_bf16 v[160:163], a[4:7], v[28:31], v[160:163]\n" \
  "v_mfma_f32_16x16x32_bf16 v[164:167], a[68:71], v[28:31], v[164:167]\n" \
  "v_mfma_f32_16x16x32_bf16 v[168:171], a[132:135], v[28:31], v[168:171]\n" \
  "v_mfma_f32_16x16x32_bf16 v[172:175], a[196:199], v[28:31], v[172:175]\n" \
  "s_waitcnt vmcnt(27)\n" \
  "v_mfma_f32_16x16x32_bf16 v[144:147], a[8:11], v[32:35], v[144:147]\n" \
  "v_mfma_f32_16x16x32_bf16 v[148:151], a[72:75], v[32:35], v[148:151]\n" \
  "v_mfma_f32_16x16x32_bf16 v[152:155], a[136:139], v[32:35], v[152:155]\n" \
  "v_mfma_f32_16x16x32_bf16 v[156:159], a[200:203], v[32:35], v[156:159]\n" \
  "s_waitcnt vmcnt(26)\n" \
  "v_mfma_f32_16x16x32_bf16 v[160:163], a[8:11], v[36:39], v[160:163]\n" \
  "v_mfma_f32_16x16x32_bf16 v[164:167], a[72:75], v[36:39], v[164:167]\n" \
  "v_mfma_f32_16x16x32_bf16 v[168:171], a[136:139], v[36:39], v[168:171]\n" \
  "v_mfma_f32_16x16x32_bf16 v[172:175], a[200:203], v[36:39], v[172:175]\n" \
  "s_waitcnt vmcnt(25)\n" \
  "v_mfma_f32_16x16x32_bf16 v[144:147], a[12:15], v[40:43], v[144:147]\n" \
  "v_mfma_f32_16x16x32_bf16 v[148:151], a[76:79], v[40:43], v[148:151]\n" \
  "v_mfma_f32_16x16x32_bf16 v[152:155], a[140:143], v[40:43], v[152:155]\n" \
  "v_mfma_f32_16x16x32_bf16 v[156:159], a[204:207], v[40:43], v[156:159]\n" \
  "s_waitcnt vmcnt(24)\n" \
  "v_mfma_f32_16x16x32_bf16 v[160:163], a[12:15], v[44:47], v[160:163]\n" \
  "v_mfma_f32_16x16x32_bf16 v[164:167], a[76:79], v[44:47], v[164:167]\n" \
  "v_mfma_f32_16x16x32_bf16 v[168:171], a[140:143], v[44:47], v[168:171]\n" \
  "v_mfma_f32_16x16x32_bf16 v[172:175], a[204:207], v[44:47], v[172:175]\n" \
  "s_waitcnt vmcnt(23)\n" \
  "v_mfma_f32_16x16x32_bf16 v[144:147], a[16:19], v[48:51], v[144:147]\n" \
  "v_mfma_f32_16x16x32_bf16 v[148:151], a[80:83], v[48:51], v[148:151]\n" \
  "v_mfma_f32_16x16x32_bf16 v[152:155], a[144:147], v[48:51], v[152:155]\n" \
  "v_mfma_f32_16x16x32_bf16 v[156:159], a[208:211], v[48:51], v[156:159]\n" \
  "s_waitcnt vmcnt(22)\n" \
  "v_mfma_f32_16x16x32_bf16 v[160:163], a[16:19], v[52:55], v[160:163]\n" \
  "v_mfma_f32_16x16x32_bf16 v[164:167], a[80:83], v[52:55], v[164:167]\n" \
  "v_mfma_f32_16x16x32_bf16 v[168:171], a[144:147], v[52:55], v[168:171]\n" \
  "v_mfma_f32_16x16x32_bf16 v[172:175], a[208:211], v[52:55], v[172:175]\n" \
  "s_waitcnt vmcnt(21)\n" \
  "v_mfma_f32_16x16x32_bf16 v[144:147], a[20:23], v[56:59], v[144:147]\n" \
  "v_mfma_f32_16x16x32_bf16 v[148:151], a[84:87], v[56:59], v[148:151]\n" \
  "v_mfma_f32_16x16x32_bf16 v[152:155], a[148:151], v[56:59], v[152:155]\n" \
  "v_mfma_f32_16x16x32_bf16 v[156:159], a[212:215], v[56:59], v[156:159]\n" \
  "s_waitcnt vmcnt(20)\n" \
  "v_mfma_f32_16x16x32_bf16 v[160:163], a[20:23], v[60:63], v[160:163]\n" \
  "v_mfma_f32_16x16x32_bf16 v[164:167], a[84:87], v[60:63], v[164:167]\n" \
  "v_mfma_f32_16x16x32_bf16 v[168:171], a[148:151], v[60:63], v[168:171]\n" \
  "v_mfma_f32_16x16x32_bf16 v[172:175], a[212:215], v[60:63], v[172:175]\n" \
  "s_waitcnt vmcnt(19)\n" \
  "v_mfma_f32_16x16x32_bf16 v[144:147], a[24:27], v[64:67], v[144:147]\n" \
  "v_mfma_f32_16x16x32_bf16 v[148:151], a[88:91], v[64:67], v[148:151]\n" \
  "v_mfma_f32_16x16x32_bf16 v[152:155], a[152:155], v[64:67], v[152:155]\n" \
  "v_mfma_f32_16x16x32_bf16 v[156:159], a[216:219], v[64:67], v[156:159]\n" \
  "s_waitcnt vmcnt(18)\n" \
  "v_mfma_f32_16x16x32_bf16 v[160:163], a[24:27], v[68:71], v[160:163]\n" \
  "v_mfma_f32_16x16x32_bf16 v[164:167], a[88:91], v[68:71], v[164:167]\n" \
  "v_mfma_f32_16x16x32_bf16 v[168:171], a[152:155], v[68:71], v[168:171]\n" \
  "v_mfma_f32_16x16x32_bf16 v[172:175], a[216:219], v[68:71], v[172:175]\n" \
  "s_waitcnt vmcnt(17)\n" \
  "v_mfma_f32_16x16x32_bf16 v[144:147], a[28:31], v[72:75], v[144:147]\n" \
  "v_mfma_f32_16x16x32_bf16 v[148:151], a[92:95], v[72:75], v[148:151]\n" \
  "v_mfma_f32_16x16x32_bf16 v[152:155], a[156:159], v[72:75], v[152:155]\n" \
  "v_mfma_f32_16x16x32_bf16 v[156:159], a[220:223], v[72:75], v[156:159]\n" \
  "s_waitcnt vmcnt(16)\n" \
  "v_mfma_f32_16x16x32_bf16 v[160:163], a[28:31], v[76:79], v[160:163]\n" \
  "v_mfma_f32_16x16x32_bf16 v[164:167], a[92:95], v[76:79], v[164:167]\n" \
  "v_mfma_f32_16x16x32_bf16 v[168:171], a[156:159], v[76:79], v[168:171]\n" \
  "v_mfma_f32_16x16x32_bf16 v[172:175], a[220:223], v[76:79], v[172:175]\n" \
  "s_waitcnt vmcnt(15)\n" \
  "v_mfma_f32_16x16x32_bf16 v[144:147], a[32:35], v[80:83], v[144:147]\n" \
  "v_mfma_f32_16x16x32_bf16 v[148:151], a[96:99], v[80:83], v[148:151]\n" \
  "v_mfma_f32_16x16x32_bf16 v[152:155], a[160:163], v[80:83], v[152:155]\n" \
  "v_mfma_f32_16x16x32_bf16 v[156:159], a[224:227], v[80:83], v[156:159]\n" \
  "s_waitcnt vmcnt(14)\n" \
  "v_mfma_f32_16x16x32_bf16 v[160:163], a[32:35], v[84:87], v[160:163]\n" \
  "v_mfma_f32_16x16x32_bf16 v[164:167], a[96:99], v[84:87], v[164:167]\n" \
  "v_mfma_f32_16x16x32_bf16 v[168:171], a[160:163], v[84:87], v[168:171]\n" \
  "v_mfma_f32_16x16x32_bf16 v[172:175], a[224:227], v[84:87], v[172:175]\n" \
  "s_waitcnt vmcnt(13)\n" \
  "v_mfma_f32_16x16x32_bf16 v[144:147], a[36:39], v[88:91], v[144:147]\n" \
  "v_mfma_f32_16x16x32_bf16 v[148:151], a[100:103], v[88:91], v[148:151]\n" \
  "v_mfma_f32_16x16x32_bf16 v[152:155], a[164:167], v[88:91], v[152:155]\n" \
  "v_mfma_f32_16x16x32_bf16 v[156:159], a[228:231], v[88:91], v[156:159]\n" \
  "s_waitcnt vmcnt(12)\n" \
  "v_mfma_f32_16x16x32_bf16 v[160:163], a[36:39], v[92:95], v[160:163]\n" \
  "v_mfma_f32_16x16x32_bf16 v[164:167], a[100:103], v[92:95], v[164:167]\n" \
  "v_mfma_f32_16x16x32_bf16 v[168:171], a[164:167], v[92:95], v[168:171]\n" \
  "v_mfma_f32_16x16x32_bf16 v[172:175], a[228:231], v[92:95], v[172:175]\n" \
  "s_waitcnt vmcnt(11)\n" \
  "v_mfma_f32_16x16x32_bf16 v[144:147], a[40:43], v[96:99], v[144:147]\n" \
  "v_mfma_f32_16x16x32_bf16 v[148:151], a[104:107], v[96:99], v[148:151]\n" \
  "v_mfma_f32_16x16x32_bf16 v[152:155], a[168:171], v[96:99], v[152:155]\n" \
  "v_mfma_f32_16x16x32_bf16 v[156:159], a[232:235], v[96:99], v[156:159]\n" \
  "s_waitcnt vmcnt(10)\n" \
  "v_mfma_f32_16x16x32_bf16 v[160:163], a[40:43], v[100:103], v[160:163]\n" \
  "v_mfma_f32_16x16x32_bf16 v[164:167], a[104:107], v[100:103], v[164:167]\n" \
  "v_mfma_f32_16x16x32_bf16 v[168:171], a[168:171], v[100:103], v[168:171]\n" \
  "v_mfma_f32_16x16x32_bf16 v[172:175], a[232:235], v[100:103], v[172:175]\n" \
  "s_waitcnt vmcnt(9)\n" \
  "v_mfma_f32_16x16x32_bf16 v[144:147], a[44:47], v[104:107], v[144:147]\n" \
  "v_mfma_f32_16x16x32_bf16 v[148:151], a[108:111], v[104:107], v[148:151]\n" \
  "v_mfma_f32_16x16x32_bf16 v[152:155], a[172:175], v[104:107], v[152:155]\n" \
  "v_mfma_f32_16x16x32_bf16 v[156:159], a[236:239], v[104:107], v[156:159]\n" \
  "s_waitcnt vmcnt(8)\n" \
  "v_mfma_f32_16x16x32_bf16 v[160:163], a[44:47], v[108:111], v[160:163]\n" \
  "v_mfma_f32_16x16x32_bf16 v[164:167], a[108:111], v[108:111], v[164:167]\n" \
  "v_mfma_f32_16x16x32_bf16 v[168:171], a[172:175], v[108:111], v[168:171]\n" \
  "v_mfma_f32_16x16x32_bf16 v[172:175], a[236:239], v[108:111], v[172:175]\n" \
  "s_waitcnt vmcnt(7)\n" \
  "v_mfma_f32_16x16x32_bf16 v[144:147], a[48:51], v[112:115], v[144:147]\n" \
  "v_mfma_f32_16x16x32_bf16 v[148:151], a[112:115], v[112:115], v[148:151]\n" \
  "v_mfma_f32_16x16x32_bf16 v[152:155], a[176:179], v[112:115], v[152:155]\n" \
  "v_mfma_f32_16x16x32_bf16 v[156:159], a[240:243], v[112:115], v[156:159]\n" \
  "s_waitcnt vmcnt(6)\n" \
  "v_mfma_f32_16x16x32_bf16 v[160:163], a[48:51], v[116:119], v[160:163]\n" \
  "v_mfma_f32_16x16x32_bf16 v[164:167], a[112:115], v[116:119], v[164:167]\n" \
  "v_mfma_f32_16x16x32_bf16 v[168:171], a[176:179], v[116:119], v[168:171]\n" \
  "v_mfma_f32_16x16x32_bf16 v[172:175], a[240:243], v[116:119], v[172:175]\n" \
  "s_waitcnt vmcnt(5)\n" \
  "v_mfma_f32_16x16x32_bf16 v[144:147], a[52:55], v[120:123], v[144:147]\n" \
  "v_mfma_f32_16x16x32_bf16 v[148:151], a[116:119], v[120:123], v[148:151]\n" \
  "v_mfma_f32_16x16x32_bf16 v[152:155], a[180:183], v[120:123], v[152:155]\n" \
  "v_mfma_f32_16x16x32_bf16 v[156:159], a[244:247], v[120:123], v[156:159]\n" \
  "s_waitcnt vmcnt(4)\n" \
  "v_mfma_f32_16x16x32_bf16 v[160:163], a[52:55], v[124:127], v[160:163]\n" \
  "v_mfma_f32_16x16x32_bf16 v[164:167], a[116:119], v[124:127], v[164:167]\n" \
  "v_mfma_f32_16x16x32_bf16 v[168:171], a[180:183], v[124:127], v[168:171]\n" \
  "v_mfma_f32_16x16x32_bf16 v[172:175], a[244:247], v[124:127], v[172:175]\n" \
  "s_waitcnt vmcnt(3)\n" \
  "v_mfma_f32_16x16x32_bf16 v[144:147], a[56:59], v[128:131], v[144:147]\n" \
  "v_mfma_f32_16x16x32_bf16 v[148:151], a[120:123], v[128:131], v[148:151]\n" \
  "v_mfma_f32_16x16x32_bf16 v[152:155], a[184:187], v[128:131], v[152:155]\n" \
  "v_mfma_f32_16x16x32_bf16 v[156:159], a[248:251], v[128:131], v[156:159]\n" \
  "s_waitcnt vmcnt(2)\n" \
  "v_mfma_f32_16x16x32_bf16 v[160:163], a[56:59], v[132:135], v[160:163]\n" \
  "v_mfma_f32_16x16x32_bf16 v[164:167], a[120:123], v[132:135], v[164:167]\n" \
  "v_mfma_f32_16x16x32_bf16 v[168:171], a[184:187], v[132:135], v[168:171]\n" \
  "v_mfma_f32_16x16x32_bf16 v[172:175], a[248:251], v[132:135], v[172:175]\n" \
  "s_waitcnt vmcnt(1)\n" \
  "v_mfma_f32_16x16x32_bf16 v[144:147], a[60:63], v[136:139], v[144:147]\n" \
  "v_mfma_f32_16x16x32_bf16 v[148:151], a[124:127], v[136:139], v[148:151]\n" \
  "v_mfma_f32_16x16x32_bf16 v[152:155], a[188:191], v[136:139], v[152:155]\n" \
  "v_mfma_f32_16x16x32_bf16 v[156:159], a[252:255], v[136:139], v[156:159]\n" \
  "s_waitcnt vmcnt(0)\n" \
  "v_mfma_f32_16x16x32_bf16 v[160:163], a[60:63], v[140:143], v[160:163]\n" \
  "v_mfma_f32_16x16x32_bf16 v[164:167], a[124:127], v[140:143], v[164:167]\n" \
  "v_mfma_f32_16x16x32_bf16 v[168:171], a[188:191], v[140:143], v[168:171]\n" \
  "v_mfma_f32_16x16x32_bf16 v[172:175], a[252:255], v[140:143], v[172:175]\n" \
  "ds_write_b128 v8, v[144:147] offset:0\n" \
  "ds_write_b128 v8, v[148:151] offset:1024\n" \
  "ds_write_b128 v8, v[152:155] offset:2048\n" \
  "ds_write_b128 v8, v[156:159] offset:3072\n" \
  "s_nop 0\n" \
  "ds_write_b128 v8, v[160:163] offset:4096\n" \
  "ds_write_b128 v8, v[164:167] offset:5120\n" \
  "ds_write_b128 v8, v[168:171] offset:6144\n" \
  "ds_write_b128 v8, v[172:175] offset:7168\n" \
  "s_waitcnt lgkmcnt(0)\n" \
  "s_barrier\n" \
  "ds_read_b128 v[208:211], v14 offset:0\n" \
  "ds_read_b128 v[212:215], v14 offset:8192\n" \
  "ds_read_b128 v[216:219], v14 offset:16384\n" \
  "ds_read_b128 v[220:223], v14 offset:24576\n" \
  "ds_read_b128 v[192:195], v9 offset:0\n" \
  "ds_read_b128 v[196:199], v9 offset:8192\n" \
  "ds_read_b128 v[200:203], v9 offset:16384\n" \
  "ds_read_b128 v[204:207], v9 offset:24576\n" \
  "s_waitcnt lgkmcnt(6)\n" \
  "v_pk_add_f32 v[208:209], v[208:209], v[212:213]\n" \
  "v_pk_add_f32 v[210:211], v[210:211], v[214:215]\n" \
  "s_waitcnt lgkmcnt(4)\n" \
  "v_pk_add_f32 v[216:217], v[216:217], v[220:221]\n" \
  "v_pk_add_f32 v[218:219], v[218:219], v[222:223]\n" \
  "v_pk_add_f32 v[208:209], v[208:209], v[216:217]\n" \
  "v_pk_add_f32 v[210:211], v[210:211], v[218:219]\n" \
  "s_lshl_b32 s52, s68, 12\n" \
  "s_add_u32 s52, s52, s70\n" \
  "buffer_store_dwordx4 v[208:211], v1, s[36:39], s52 offen offset:0 sc1\n" \
  "s_waitcnt lgkmcnt(2)\n" \
  "v_pk_add_f32 v[192:193], v[192:193], v[196:197]\n" \
  "v_pk_add_f32 v[194:195], v[194:195], v[198:199]\n" \
  "s_waitcnt lgkmcnt(0)\n" \
  "v_pk_add_f32 v[200:201], v[200:201], v[204:205]\n" \
  "v_pk_add_f32 v[202:203], v[202:203], v[206:207]\n" \
  "v_pk_add_f32 v[176:177], v[192:193], v[200:201]\n" \
  "v_pk_add_f32 v[178:179], v[194:195], v[202:203]\n" \
  "v_xor_b32 v8, 32768, v8\n" \
  "v_xor_b32 v9, 32768, v9\n" \
  "v_xor_b32 v14, 32768, v14\n" \
  "s_add_u32 s66, s66, 6\n" \
  "s_add_u32 s68, s68, 1\n" \
  "s_mov_b32 s67, 0\n" \
  "s_branch WLOOP4_%=\n" \
  "WV1_%=:\n" \
  "s_add_u32 s52, s66, 6\n" \
  "s_lshl_b32 s52, s52, 13\n" \
  "s_add_u32 s72, s60, s52\n" \
  "s_add_u32 s73, s72, 524288\n" \
  "s_add_u32 s74, s73, 524288\n" \
  "s_add_u32 s75, s74, 524288\n" \
  "s_waitcnt vmcnt(31)\n" \
  "v_mfma_f32_16x16x32_bf16 v[144:147], a[0:3], v[16:19], 0\n" \
  "v_mfma_f32_16x16x32_bf16 v[148:151], a[64:67], v[16:19], 0\n" \
  "v_mfma_f32_16x16x32_bf16 v[152:155], a[128:131], v[16:19], 0\n" \
  "v_mfma_f32_16x16x32_bf16 v[156:159], a[192:195], v[16:19], 0\n" \
  "buffer_load_dwordx4 v[16:19], v1, s[36:39], s72 offen offset:0 sc1\n" \
  "s_waitcnt vmcnt(31)\n" \
  "v_mfma_f32_16x16x32_bf16 v[160:163], a[0:3], v[20:23], 0\n" \
  "v_mfma_f32_16x16x32_bf16 v[164:167], a[64:67], v[20:23], 0\n" \
  "v_mfma_f32_16x16x32_bf16 v[168:171], a[128:131], v[20:23], 0\n" \
  "v_mfma_f32_16x16x32_bf16 v[172:175], a[192:195], v[20:23], 0\n" \
  "buffer_load_dwordx4 v[20:23], v11, s[36:39], s72 offen offset:0 sc1\n" \
  "s_waitcnt vmcnt(31)\n" \
  "v_mfma_f32_16x16x32_bf16 v[144:147], a[4:7], v[24:27], v[144:147]\n" \
  "v_mfma_f32_16x16x32_bf16 v[148:151], a[68:71], v[24:27], v[148:151]\n" \
  "v_mfma_f32_16x16x32_bf16 v[152:155], a[132:135], v[24:27], v[152:155]\n" \
  "v_mfma_f32_16x16x32_bf16 v[156:159], a[196:199], v[24:27], v[156:159]\n" \
  "buffer_load_dwordx4 v[24:27], v1, s[36:39], s72 offen offset:1024 sc1\n" \
  "s_waitcnt vmcnt(31)\n" \
  "v_mfma_f32_16x16x32_bf16 v[160:163], a[4:7], v[28:31], v[160:163]\n" \
  "v_mfma_f32_16x16x32_bf16 v[164:167], a[68:71], v[28:31], v[164:167]\n" \
  "v_mfma_f32_16x16x32_bf16 v[168:171], a[132:135], v[28:31], v[168:171]\n" \
  "v_mfma_f32_16x16x32_bf16 v[172:175], a[196:199], v[28:31], v[172:175]\n" \
  "buffer_load_dwordx4 v[28:31], v11, s[36:39], s72 offen offset:1024 sc1\n" \
  "s_waitcnt vmcnt(31)\n" \
  "v_mfma_f32_16x16x32_bf16 v[144:147], a[8:11], v[32:35], v[144:147]\n" \
  "v_mfma_f32_16x16x32_bf16 v[148:151], a[72:75], v[32:35], v[148:151]\n" \
  "v_mfma_f32_16x16x32_bf16 v[152:155], a[136:139], v[32:35], v[152:155]\n" \
  "v_mfma_f32_16x16x32_bf16 v[156:159], a[200:203], v[32:35], v[156:159]\n" \
  "buffer_load_dwordx4 v[32:35], v1, s[36:39], s72 offen offset:2048 sc1\n" \
  "s_waitcnt vmcnt(31)\n" \
  "v_mfma_f32_16x16x32_bf16 v[160:163], a[8:11], v[36:39], v[160:163]\n" \
  "v_mfma_f32_16x16x32_bf16 v[164:167], a[72:75], v[36:39], v[164:167]\n" \
  "v_mfma_f32_16x16x32_bf16 v[168:171], a[136:139], v[36:39], v[168:171]\n" \
  "v_mfma_f32_16x16x32_bf16 v[172:175], a[200:203], v[36:39], v[172:175]\n" \
  "buffer_load_dwordx4 v[36:39], v11, s[36:39], s72 offen offset:2048 sc1\n" \
  "s_waitcnt vmcnt(31)\n" \
  "v_mfma_f32_16x16x32_bf16 v[144:147], a[12:15], v[40:43], v[144:147]\n" \
  "v_mfma_f32_16x16x32_bf16 v[148:151], a[76:79], v[40:43], v[148:151]\n" \
  "v_mfma_f32_16x16x32_bf16 v[152:155], a[140:143], v[40:43], v[152:155]\n" \
  "v_mfma_f32_16x16x32_bf16 v[156:159], a[204:207], v[40:43], v[156:159]\n" \
  "buffer_load_dwordx4 v[40:43], v1, s[36:39], s72 offen offset:3072 sc1\n" \
  "s_waitcnt vmcnt(31)\n" \
  "v_mfma_f32_16x16x32_bf16 v[160:163], a[12:15], v[44:47], v[160:163]\n" \
  "v_mfma_f32_16x16x32_bf16 v[164:167], a[76:79], v[44:47], v[164:167]\n" \
  "v_mfma_f32_16x16x32_bf16 v[168:171], a[140:143], v[44:47], v[168:171]\n" \
  "v_mfma_f32_16x16x32_bf16 v[172:175], a[204:207], v[44:47], v[172:175]\n" \
  "buffer_load_dwordx4 v[44:47], v11, s[36:39], s72 offen offset:3072 sc1\n" \
  "s_waitcnt vmcnt(31)\n" \
  "v_mfma_f32_16x16x32_bf16 v[144:147], a[16:19], v[48:51], v[144:147]\n" \
  "v_mfma_f32_16x16x32_bf16 v[148:151], a[80:83], v[48:51], v[148:151]\n" \
  "v_mfma_f32_16x16x32_bf16 v[152:155], a[144:147], v[48:51], v[152:155]\n" \
  "v_mfma_f32_16x16x32_bf16 v[156:159], a[208:211], v[48:51], v[156:159]\n" \
  "buffer_load_dwordx4 v[48:51], v1, s[36:39], s73 offen offset:0 sc1\n" \
  "s_waitcnt vmcnt(31)\n" \
  "v_mfma_f32_16x16x32_bf16 v[160:163], a[16:19], v[52:55], v[160:163]\n" \
  "v_mfma_f32_16x16x32_bf16 v[164:167], a[80:83], v[52:55], v[164:167]\n" \
  "v_mfma_f32_16x16x32_bf16 v[168:171], a[144:147], v[52:55], v[168:171]\n" \
  "v_mfma_f32_16x16x32_bf16 v[172:175], a[208:211], v[52:55], v[172:175]\n" \
  "buffer_load_dwordx4 v[52:55], v11, s[36:39], s73 offen offset:0 sc1\n" \
  "s_waitcnt vmcnt(31)\n" \
  "v_mfma_f32_16x16x32_bf16 v[144:147], a[20:23], v[56:59], v[144:147]\n" \
  "v_mfma_f32_16x16x32_bf16 v[148:151], a[84:87], v[56:59], v[148:151]\n" \
  "v_mfma_f32_16x16x32_bf16 v[152:155], a[148:151], v[56:59], v[152:155]\n" \
  "v_mfma_f32_16x16x32_bf16 v[156:159], a[212:215], v[56:59], v[156:159]\n" \
  "buffer_load_dwordx4 v[56:59], v1, s[36:39], s73 offen offset:1024 sc1\n" \
  "s_waitcnt vmcnt(31)\n" \
  "v_mfma_f32_16x16x32_bf16 v[160:163], a[20:23], v[60:63], v[160:163]\n" \
  "v_mfma_f32_16x16x32_bf16 v[164:167], a[84:87], v[60:63], v[164:167]\n" \
  "v_mfma_f32_16x16x32_bf16 v[168:171], a[148:151], v[60:63], v[168:171]\n" \
  "v_mfma_f32_16x16x32_bf16 v[172:175], a[212:215], v[60:63], v[172:175]\n" \
  "buffer_load_dwordx4 v[60:63], v11, s[36:39], s73 offen offset:1024 sc1\n" \
  "s_waitcnt vmcnt(31)\n" \
  "v_mfma_f32_16x16x32_bf16 v[144:147], a[24:27], v[64:67], v[144:147]\n" \
  "v_mfma_f32_16x16x32_bf16 v[148:151], a[88:91], v[64:67], v[148:151]\n" \
  "v_mfma_f32_16x16x32_bf16 v[152:155], a[152:155], v[64:67], v[152:155]\n" \
  "v_mfma_f32_16x16x32_bf16 v[156:159], a[216:219], v[64:67], v[156:159]\n" \
  "buffer_load_dwordx4 v[64:67], v1, s[36:39], s73 offen offset:2048 sc1\n" \
  "s_waitcnt vmcnt(31)\n" \
  "v_mfma_f32_16x16x32_bf16 v[160:163], a[24:27], v[68:71], v[160:163]\n" \
  "v_mfma_f32_16x16x32_bf16 v[164:167], a[88:91], v[68:71], v[164:167]\n" \
  "v_mfma_f32_16x16x32_bf16 v[168:171], a[152:155], v[68:71], v[168:171]\n" \
  "v_mfma_f32_16x16x32_bf16 v[172:175], a[216:219], v[68:71], v[172:175]\n" \
  "buffer_load_dwordx4 v[68:71], v11, s[36:39], s73 offen offset:2048 sc1\n" \
  "s_waitcnt vmcnt(31)\n" \
  "v_mfma_f32_16x16x32_bf16 v[144:147], a[28:31], v[72:75], v[144:147]\n" \
  "v_mfma_f32_16x16x32_bf16 v[148:151], a[92:95], v[72:75], v[148:151]\n" \
  "v_mfma_f32_16x16x32_bf16 v[152:155], a[156:159], v[72:75], v[152:155]\n" \
  "v_mfma_f32_16x16x32_bf16 v[156:159], a[220:223], v[72:75], v[156:159]\n" \
  "buffer_load_dwordx4 v[72:75], v1, s[36:39], s73 offen offset:3072 sc1\n" \
  "s_waitcnt vmcnt(31)\n" \
  "v_mfma_f32_16x16x32_bf16 v[160:163], a[28:31], v[76:79], v[160:163]\n" \
  "v_mfma_f32_16x16x32_bf16 v[164:167], a[92:95], v[76:79], v[164:167]\n" \
  "v_mfma_f32_16x16x32_bf16 v[168:171], a[156:159], v[76:79], v[168:171]\n" \
  "v_mfma_f32_16x16x32_bf16 v[172:175], a[220:223], v[76:79], v[172:175]\n" \
  "buffer_load_dwordx4 v[76:79], v11, s[36:39], s73 offen offset:3072 sc1\n" \
  "s_waitcnt vmcnt(31)\n" \
  "v_mfma_f32_16x16x32_bf16 v[144:147], a[32:35], v[80:83], v[144:147]\n" \
  "v_mfma_f32_16x16x32_bf16 v[148:151], a[96:99], v[80:83], v[148:151]\n" \
  "v_mfma_f32_16x16x32_bf16 v[152:155], a[160:163], v[80:83], v[152:155]\n" \
  "v_mfma_f32_16x16x32_bf16 v[156:159], a[224:227], v[80:83], v[156:159]\n" \
  "buffer_load_dwordx4 v[80:83], v1, s[36:39], s74 offen offset:0 sc1\n" \
  "s_waitcnt vmcnt(31)\n" \
  "v_mfma_f32_16x16x32_bf16 v[160:163], a[32:35], v[84:87], v[160:163]\n" \
  "v_mfma_f32_16x16x32_bf16 v[164:167], a[96:99], v[84:87], v[164:167]\n" \
  "v_mfma_f32_16x16x32_bf16 v[168:171], a[160:163], v[84:87], v[168:171]\n" \
  "v_mfma_f32_16x16x32_bf16 v[172:175], a[224:227], v[84:87], v[172:175]\n" \
  "buffer_load_dwordx4 v[84:87], v11, s[36:39], s74 offen offset:0 sc1\n" \
  "s_waitcnt vmcnt(31)\n" \
  "v_mfma_f32_16x16x32_bf16 v[144:147], a[36:39], v[88:91], v[144:147]\n" \
  "v_mfma_f32_16x16x32_bf16 v[148:151], a[100:103], v[88:91], v[148:151]\n" \
  "v_mfma_f32_16x16x32_bf16 v[152:155], a[164:167], v[88:91], v[152:155]\n" \
  "v_mfma_f32_16x16x32_bf16 v[156:159], a[228:231], v[88:91], v[156:159]\n" \
  "buffer_load_dwordx4 v[88:91], v1, s[36:39], s74 offen offset:1024 sc1\n" \
  "s_waitcnt vmcnt(31)\n" \
  "v_mfma_f32_16x16x32_bf16 v[160:163], a[36:39], v[92:95], v[160:163]\n" \
  "v_mfma_f32_16x16x32_bf16 v[164:167], a[100:103], v[92:95], v[164:167]\n" \
  "v_mfma_f32_16x16x32_bf16 v[168:171], a[164:167], v[92:95], v[168:171]\n" \
  "v_mfma_f32_16x16x32_bf16 v[172:175], a[228:231], v[92:95], v[172:175]\n" \
  "buffer_load_dwordx4 v[92:95], v11, s[36:39], s74 offen offset:1024 sc1\n" \
  "s_waitcnt vmcnt(31)\n" \
  "v_mfma_f32_16x16x32_bf16 v[144:147], a[40:43], v[96:99], v[144:147]\n" \
  "buffer_load_dword v188, v13, s[36:39], s61 offen offset:0 sc1\n" \
  "v_mfma_f32_16x16x32_bf16 v[148:151], a[104:107], v[96:99], v[148:151]\n" \
  "v_mfma_f32_16x16x32_bf16 v[152:155], a[168:171], v[96:99], v[152:155]\n" \
  "v_mfma_f32_16x16x32_bf16 v[156:159], a[232:235], v[96:99], v[156:159]\n" \
  "buffer_load_dwordx4 v[96:99], v1, s[36:39], s74 offen offset:2048 sc1\n" \
  "s_waitcnt vmcnt(32)\n" \
  "v_mfma_f32_16x16x32_bf16 v[160:163], a[40:43], v[100:103], v[160:163]\n" \
  "v_mfma_f32_16x16x32_bf16 v[164:167], a[104:107], v[100:103], v[164:167]\n" \
  "v_mfma_f32_16x16x32_bf16 v[168:171], a[168:171], v[100:103], v[168:171]\n" \
  "v_mfma_f32_16x16x32_bf16 v[172:175], a[232:235], v[100:103], v[172:175]\n" \
  "buffer_load_dwordx4 v[100:103], v11, s[36:39], s74 offen offset:2048 sc1\n" \
  "s_waitcnt vmcnt(32)\n" \
  "v_mfma_f32_16x16x32_bf16 v[144:147], a[44:47], v[104:107], v[144:147]\n" \
  "v_mfma_f32_16x16x32_bf16 v[148:151], a[108:111], v[104:107], v[148:151]\n" \
  "v_mfma_f32_16x16x32_bf16 v[152:155], a[172:175], v[104:107], v[152:155]\n" \
  "v_mfma_f32_16x16x32_bf16 v[156:159], a[236:239], v[104:107], v[156:159]\n" \
  "buffer_load_dwordx4 v[104:107], v1, s[36:39], s74 offen offset:3072 sc1\n" \
  "s_waitcnt vmcnt(32)\n" \
  "v_mfma_f32_16x16x32_bf16 v[160:163], a[44:47], v[108:111], v[160:163]\n" \
  "v_mfma_f32_16x16x32_bf16 v[164:167], a[108:111], v[108:111], v[164:167]\n" \
  "v_mfma_f32_16x16x32_bf16 v[168:171], a[172:175], v[108:111], v[168:171]\n" \
  "v_mfma_f32_16x16x32_bf16 v[172:175], a[236:239], v[108:111], v[172:175]\n" \
  "buffer_load_dwordx4 v[108:111], v11, s[36:39], s74 offen offset:3072 sc1\n" \
  "s_waitcnt vmcnt(32)\n" \
  "v_mfma_f32_16x16x32_bf16 v[144:147], a[48:51], v[112:115], v[144:147]\n" \
  "v_mfma_f32_16x16x32_bf16 v[148:151], a[112:115], v[112:115], v[148:151]\n" \
  "v_mfma_f32_16x16x32_bf16 v[152:155], a[176:179], v[112:115], v[152:155]\n" \
  "v_mfma_f32_16x16x32_bf16 v[156:159], a[240:243], v[112:115], v[156:159]\n" \
  "buffer_load_dwordx4 v[112:115], v1, s[36:39], s75 offen offset:0 sc1\n" \
  "s_waitcnt vmcnt(32)\n" \
  "v_mfma_f32_16x16x32_bf16 v[160:163], a[48:51], v[116:119], v[160:163]\n" \
  "v_mfma_f32_16x16x32_bf16 v[164:167], a[112:115], v[116:119], v[164:167]\n" \
  "v_mfma_f32_16x16x32_bf16 v[168:171], a[176:179], v[116:119], v[168:171]\n" \
  "v_mfma_f32_16x16x32_bf16 v[172:175], a[240:243], v[116:119], v[172:175]\n" \
  "buffer_load_dwordx4 v[116:119], v11, s[36:39], s75 offen offset:0 sc1\n" \
  "s_waitcnt vmcnt(32)\n" \
  "v_mfma_f32_16x16x32_bf16 v[144:147], a[52:55], v[120:123], v[144:147]\n" \
  "v_mfma_f32_16x16x32_bf16 v[148:151], a[116:119], v[120:123], v[148:151]\n" \
  "v_mfma_f32_16x16x32_bf16 v[152:155], a[180:183], v[120:123], v[152:155]\n" \
  "v_mfma_f32_16x16x32_bf16 v[156:159], a[244:247], v[120:123], v[156:159]\n" \
  "buffer_load_dwordx4 v[120:123], v1, s[36:39], s75 offen offset:1024 sc1\n" \
  "s_waitcnt vmcnt(32)\n" \
  "v_mfma_f32_16x16x32_bf16 v[160:163], a[52:55], v[124:127], v[160:163]\n" \
  "v_mfma_f32_16x16x32_bf16 v[164:167], a[116:119], v[124:127], v[164:167]\n" \
  "v_mfma_f32_16x16x32_bf16 v[168:171], a[180:183], v[124:127], v[168:171]\n" \
  "v_mfma_f32_16x16x32_bf16 v[172:175], a[244:247], v[124:127], v[172:175]\n" \
  "buffer_load_dwordx4 v[124:127], v11, s[36:39], s75 offen offset:1024 sc1\n" \
  "s_waitcnt vmcnt(32)\n" \
  "v_mfma_f32_16x16x32_bf16 v[144:147], a[56:59], v[128:131], v[144:147]\n" \
  "v_mfma_f32_16x16x32_bf16 v[148:151], a[120:123], v[128:131], v[148:151]\n" \
  "v_mfma_f32_16x16x32_bf16 v[152:155], a[184:187], v[128:131], v[152:155]\n" \
  "v_mfma_f32_16x16x32_bf16 v[156:159], a[248:251], v[128:131], v[156:159]\n" \
  "buffer_load_dwordx4 v[128:131], v1, s[36:39], s75 offen offset:2048 sc1\n" \
  "s_waitcnt vmcnt(32)\n" \
  "v_mfma_f32_16x16x32_bf16 v[160:163], a[56:59], v[132:135], v[160:163]\n" \
  "v_mfma_f32_16x16x32_bf16 v[164:167], a[120:123], v[132:135], v[164:167]\n" \
  "v_mfma_f32_16x16x32_bf16 v[168:171], a[184:187], v[132:135], v[168:171]\n" \
  "v_mfma_f32_16x16x32_bf16 v[172:175], a[248:251], v[132:135], v[172:175]\n" \
  "buffer_load_dwordx4 v[132:135], v11, s[36:39], s75 offen offset:2048 sc1\n" \
  "s_waitcnt vmcnt(32)\n" \
  "v_mfma_f32_16x16x32_bf16 v[144:147], a[60:63], v[136:139], v[144:147]\n" \
  "v_mfma_f32_16x16x32_bf16 v[148:151], a[124:127], v[136:139], v[148:151]\n" \
  "v_mfma_f32_16x16x32_bf16 v[152:155], a[188:191], v[136:139], v[152:155]\n" \
  "v_mfma_f32_16x16x32_bf16 v[156:159], a[252:255], v[136:139], v[156:159]\n" \
  "buffer_load_dwordx4 v[136:139], v1, s[36:39], s75 offen offset:3072 sc1\n" \
  "s_waitcnt vmcnt(32)\n" \
  "v_mfma_f32_16x16x32_bf16 v[160:163], a[60:63], v[140:143], v[160:163]\n" \
  "v_mfma_f32_16x16x32_bf16 v[164:167], a[124:127], v[140:143], v[164:167]\n" \
  "v_mfma_f32_16x16x32_bf16 v[168:171], a[188:191], v[140:143], v[168:171]\n" \
  "v_mfma_f32_16x16x32_bf16 v[172:175], a[252:255], v[140:143], v[172:175]\n" \
  "buffer_load_dwordx4 v[140:143], v11, s[36:39], s75 offen offset:3072 sc1\n" \
  "ds_write_b128 v8, v[144:147] offset:0\n" \
  "ds_write_b128 v8, v[148:151] offset:1024\n" \
  "ds_write_b128 v8, v[152:155] offset:2048\n" \
  "ds_write_b128 v8, v[156:159] offset:3072\n" \
  "ds_write_b128 v8, v[160:163] offset:4096\n" \
  "ds_write_b128 v8, v[164:167] offset:5120\n" \
  "ds_write_b128 v8, v[168:171] offset:6144\n" \
  "ds_write_b128 v8, v[172:175] offset:7168\n" \
  "s_waitcnt vmcnt(12)\n" \
  "s_waitcnt lgkmcnt(0)\n" \
  "s_barrier\n" \
  "ds_read_b128 v[208:211], v14 offset:0\n" \
  "ds_read_b128 v[212:215], v14 offset:8192\n" \
  "ds_read_b128 v[216:219], v14 offset:16384\n" \
  "ds_read_b128 v[220:223], v14 offset:24576\n" \
  "ds_read_b128 v[192:195], v9 offset:0\n" \
  "ds_read_b128 v[196:199], v9 offset:8192\n" \
  "ds_read_b128 v[200:203], v9 offset:16384\n" \
  "ds_read_b128 v[204:207], v9 offset:24576\n" \
  "s_waitcnt lgkmcnt(6)\n" \
  "v_pk_add_f32 v[208:209], v[208:209], v[212:213]\n" \
  "v_pk_add_f32 v[210:211], v[210:211], v[214:215]\n" \
  "s_waitcnt lgkmcnt(4)\n" \
  "v_pk_add_f32 v[216:217], v[216:217], v[220:221]\n" \
  "v_pk_add_f32 v[218:219], v[218:219], v[222:223]\n" \
  "v_pk_add_f32 v[208:209], v[208:209], v[216:217]\n" \
  "v_pk_add_f32 v[210:211], v[210:211], v[218:219]\n" \
  "s_lshl_b32 s52, s68, 12\n" \
  "s_add_u32 s52, s52, s70\n" \
  "buffer_store_dwordx4 v[208:211], v1, s[36:39], s52 offen offset:0 sc1\n" \
  "s_waitcnt lgkmcnt(2)\n" \
  "v_pk_add_f32 v[192:193], v[192:193], v[196:197]\n" \
  "v_pk_add_f32 v[194:195], v[194:195], v[198:199]\n" \
  "s_waitcnt lgkmcnt(0)\n" \
  "v_pk_add_f32 v[200:201], v[200:201], v[204:205]\n" \
  "v_pk_add_f32 v[202:203], v[202:203], v[206:207]\n" \
  "v_pk_add_f32 v[176:177], v[192:193], v[200:201]\n" \
  "v_pk_add_f32 v[178:179], v[194:195], v[202:203]\n" \
  "v_xor_b32 v8, 32768, v8\n" \
  "v_xor_b32 v9, 32768, v9\n" \
  "v_xor_b32 v14, 32768, v14\n" \
  "s_add_u32 s66, s66, 6\n" \
  "s_add_u32 s68, s68, 1\n" \
  "s_mov_b32 s67, 1\n" \
  "s_branch WLOOP4_%=\n" \
  "WV2_%=:\n" \
  "s_waitcnt vmcnt(31)\n" \
  "v_mfma_f32_16x16x32_bf16 v[144:147], a[0:3], v[16:19], 0\n" \
  "v_mfma_f32_16x16x32_bf16 v[148:151], a[64:67], v[16:19], 0\n" \
  "v_mfma_f32_16x16x32_bf16 v[152:155], a[128:131], v[16:19], 0\n" \
  "v_mfma_f32_16x16x32_bf16 v[156:159], a[192:195], v[16:19], 0\n" \
  "s_waitcnt vmcnt(30)\n" \
  "v_mfma_f32_16x16x32_bf16 v[160:163], a[0:3], v[20:23], 0\n" \
  "v_mfma_f32_16x16x32_bf16 v[164:167], a[64:67], v[20:23], 0\n" \
  "v_mfma_f32_16x16x32_bf16 v[168:171], a[128:131], v[20:23], 0\n" \
  "v_mfma_f32_16x16x32_bf16 v[172:175], a[192:195], v[20:23], 0\n" \
  "s_waitcnt vmcnt(29)\n" \
  "v_mfma_f32_16x16x32_bf16 v[144:147], a[4:7], v[24:27], v[144:147]\n" \
  "v_mfma_f32_16x16x32_bf16 v[148:151], a[68:71], v[24:27], v[148:151]\n" \
  "v_mfma_f32_16x16x32_bf16 v[152:155], a[132:135], v[24:27], v[152:155]\n" \
  "v_mfma_f32_16x16x32_bf16 v[156:159], a[196:199], v[24:27], v[156:159]\n" \
  "s_waitcnt vmcnt(28)\n" \
  "v_mfma_f32_16x16x32_bf16 v[160:163], a[4:7], v[28:31], v[160:163]\n" \
  "v_mfma_f32_16x16x32_bf16 v[164:167], a[68:71], v[28:31], v[164:167]\n" \
  "v_mfma_f32_16x16x32_bf16 v[168:171], a[132:135], v[28:31], v[168:171]\n" \
  "v_mfma_f32_16x16x32_bf16 v[172:175], a[196:199], v[28:31], v[172:175]\n" \
  "s_waitcnt vmcnt(27)\n" \
  "v_mfma_f32_16x16x32_bf16 v[144:147], a[8:11], v[32:35], v[144:147]\n" \
  "v_mfma_f32_16x16x32_bf16 v[148:151], a[72:75], v[32:35], v[148:151]\n" \
  "v_mfma_f32_16x16x32_bf16 v[152:155], a[136:139], v[32:35], v[152:155]\n" \
  "v_mfma_f32_16x16x32_bf16 v[156:159], a[200:203], v[32:35], v[156:159]\n" \
  "s_waitcnt vmcnt(26)\n" \
  "v_mfma_f32_16x16x32_bf16 v[160:163], a[8:11], v[36:39], v[160:163]\n" \
  "v_mfma_f32_16x16x32_bf16 v[164:167], a[72:75], v[36:39], v[164:167]\n" \
  "v_mfma_f32_16x16x32_bf16 v[168:171], a[136:139], v[36:39], v[168:171]\n" \
  "v_mfma_f32_16x16x32_bf16 v[172:175], a[200:203], v[36:39], v[172:175]\n" \
  "s_waitcnt vmcnt(25)\n" \
  "v_mfma_f32_16x16x32_bf16 v[144:147], a[12:15], v[40:43], v[144:147]\n" \
  "v_mfma_f32_16x16x32_bf16 v[148:151], a[76:79], v[40:43], v[148:151]\n" \
  "v_mfma_f32_16x16x32_bf16 v[152:155], a[140:143], v[40:43], v[152:155]\n" \
  "v_mfma_f32_16x16x32_bf16 v[156:159], a[204:207], v[40:43], v[156:159]\n" \
  "s_waitcnt vmcnt(24)\n" \
  "v_mfma_f32_16x16x32_bf16 v[160:163], a[12:15], v[44:47], v[160:163]\n" \
  "v_mfma_f32_16x16x32_bf16 v[164:167], a[76:79], v[44:47], v[164:167]\n" \
  "v_mfma_f32_16x16x32_bf16 v[168:171], a[140:143], v[44:47], v[168:171]\n" \
  "v_mfma_f32_16x16x32_bf16 v[172:175], a[204:207], v[44:47], v[172:175]\n" \
  "s_waitcnt vmcnt(23)\n" \
  "v_mfma_f32_16x16x32_bf16 v[144:147], a[16:19], v[48:51], v[144:147]\n" \
  "v_mfma_f32_16x16x32_bf16 v[148:151], a[80:83], v[48:51], v[148:151]\n" \
  "v_mfma_f32_16x16x32_bf16 v[152:155], a[144:147], v[48:51], v[152:155]\n" \
  "v_mfma_f32_16x16x32_bf16 v[156:159], a[208:211], v[48:51], v[156:159]\n" \
  "s_waitcnt vmcnt(22)\n" \
  "v_mfma_f32_16x16x32_bf16 v[160:163], a[16:19], v[52:55], v[160:163]\n" \
  "v_mfma_f32_16x16x32_bf16 v[164:167], a[80:83], v[52:55], v[164:167]\n" \
  "v_mfma_f32_16x16x32_bf16 v[168:171], a[144:147], v[52:55], v[168:171]\n" \
  "v_mfma_f32_16x16x32_bf16 v[172:175], a[208:211], v[52:55], v[172:175]\n" \
  "s_waitcnt vmcnt(21)\n" \
  "v_mfma_f32_16x16x32_bf16 v[144:147], a[20:23], v[56:59], v[144:147]\n" \
  "v_mfma_f32_16x16x32_bf16 v[148:151], a[84:87], v[56:59], v[148:151]\n" \
  "v_mfma_f32_16x16x32_bf16 v[152:155], a[148:151], v[56:59], v[152:155]\n" \
  "v_mfma_f32_16x16x32_bf16 v[156:159], a[212:215], v[56:59], v[156:159]\n" \
  "s_waitcnt vmcnt(20)\n" \
  "v_mfma_f32_16x16x32_bf16 v[160:163], a[20:23], v[60:63], v[160:163]\n" \
  "v_mfma_f32_16x16x32_bf16 v[164:167], a[84:87], v[60:63], v[164:167]\n" \
  "v_mfma_f32_16x16x32_bf16 v[168:171], a[148:151], v[60:63], v[168:171]\n" \
  "v_mfma_f32_16x16x32_bf16 v[172:175], a[212:215], v[60:63], v[172:175]\n" \
  "s_waitcnt vmcnt(19)\n" \
  "v_mfma_f32_16x16x32_bf16 v[144:147], a[24:27], v[64:67], v[144:147]\n" \
  "v_mfma_f32_16x16x32_bf16 v[148:151], a[88:91], v[64:67], v[148:151]\n" \
  "v_mfma_f32_16x16x32_bf16 v[152:155], a[152:155], v[64:67], v[152:155]\n" \
  "v_mfma_f32_16x16x32_bf16 v[156:159], a[216:219], v[64:67], v[156:159]\n" \
  "s_waitcnt vmcnt(18)\n" \
  "v_mfma_f32_16x16x32_bf16 v[160:163], a[24:27], v[68:71], v[160:163]\n" \
  "v_mfma_f32_16x16x32_bf16 v[164:167], a[88:91], v[68:71], v[164:167]\n" \
  "v_mfma_f32_16x16x32_bf16 v[168:171], a[152:155], v[68:71], v[168:171]\n" \
  "v_mfma_f32_16x16x32_bf16 v[172:175], a[216:219], v[68:71], v[172:175]\n" \
  "s_waitcnt vmcnt(17)\n" \
  "v_mfma_f32_16x16x32_bf16 v[144:147], a[28:31], v[72:75], v[144:147]\n" \
  "v_mfma_f32_16x16x32_bf16 v[148:151], a[92:95], v[72:75], v[148:151]\n" \
  "v_mfma_f32_16x16x32_bf16 v[152:155], a[156:159], v[72:75], v[152:155]\n" \
  "v_mfma_f32_16x16x32_bf16 v[156:159], a[220:223], v[72:75], v[156:159]\n" \
  "s_waitcnt vmcnt(16)\n" \
  "v_mfma_f32_16x16x32_bf16 v[160:163], a[28:31], v[76:79], v[160:163]\n" \
  "v_mfma_f32_16x16x32_bf16 v[164:167], a[92:95], v[76:79], v[164:167]\n" \
  "v_mfma_f32_16x16x32_bf16 v[168:171], a[156:159], v[76:79], v[168:171]\n" \
  "v_mfma_f32_16x16x32_bf16 v[172:175], a[220:223], v[76:79], v[172:175]\n" \
  "s_waitcnt vmcnt(15)\n" \
  "v_mfma_f32_16x16x32_bf16 v[144:147], a[32:35], v[80:83], v[144:147]\n" \
  "v_mfma_f32_16x16x32_bf16 v[148:151], a[96:99], v[80:83], v[148:151]\n" \
  "v_mfma_f32_16x16x32_bf16 v[152:155], a[160:163], v[80:83], v[152:155]\n" \
  "v_mfma_f32_16x16x32_bf16 v[156:159], a[224:227], v[80:83], v[156:159]\n" \
  "s_waitcnt vmcnt(14)\n" \
  "v_mfma_f32_16x16x32_bf16 v[160:163], a[32:35], v[84:87], v[160:163]\n" \
  "v_mfma_f32_16x16x32_bf16 v[164:167], a[96:99], v[84:87], v[164:167]\n" \
  "v_mfma_f32_16x16x32_bf16 v[168:171], a[160:163], v[84:87], v[168:171]\n" \
  "v_mfma_f32_16x16x32_bf16 v[172:175], a[224:227], v[84:87], v[172:175]\n" \
  "s_waitcnt vmcnt(13)\n" \
  "v_mfma_f32_16x16x32_bf16 v[144:147], a[36:39], v[88:91], v[144:147]\n" \
  "v_mfma_f32_16x16x32_bf16 v[148:151], a[100:103], v[88:91], v[148:151]\n" \
  "v_mfma_f32_16x16x32_bf16 v[152:155], a[164:167], v[88:91], v[152:155]\n" \
  "v_mfma_f32_16x16x32_bf16 v[156:159], a[228:231], v[88:91], v[156:159]\n" \
  "s_waitcnt vmcnt(12)\n" \
  "v_mfma_f32_16x16x32_bf16 v[160:163], a[36:39], v[92:95], v[160:163]\n" \
  "v_mfma_f32_16x16x32_bf16 v[164:167], a[100:103], v[92:95], v[164:167]\n" \
  "v_mfma_f32_16x16x32_bf16 v[168:171], a[164:167], v[92:95], v[168:171]\n" \
  "v_mfma_f32_16x16x32_bf16 v[172:175], a[228:231], v[92:95], v[172:175]\n" \
  "s_waitcnt vmcnt(11)\n" \
  "v_mfma_f32_16x16x32_bf16 v[144:147], a[40:43], v[96:99], v[144:147]\n" \
  "s_lshl_b32 s52, s68, 12\n" \
  "s_add_u32 s52, s52, s71\n" \
  "s_sub_u32 s76, s52, 4096\n" \
  "buffer_load_dwordx4 v[180:183], v1, s[36:39], s76 offen offset:0 sc1\n" \
  "s_sub_u32 s52, s66, 6\n" \
  "s_mul_i32 s57, s52, 40960\n" \
  "buffer_load_dwordx2 v[184:185], v12, s[44:47], s57 offen offset:0 \n" \
  "v_mfma_f32_16x16x32_bf16 v[148:151], a[104:107], v[96:99], v[148:151]\n" \
  "v_mfma_f32_16x16x32_bf16 v[152:155], a[168:171], v[96:99], v[152:155]\n" \
  "v_mfma_f32_16x16x32_bf16 v[156:159], a[232:235], v[96:99], v[156:159]\n" \
  "s_waitcnt vmcnt(12)\n" \
  "v_mfma_f32_16x16x32_bf16 v[160:163], a[40:43], v[100:103], v[160:163]\n" \
  "v_mfma_f32_16x16x32_bf16 v[164:167], a[104:107], v[100:103], v[164:167]\n" \
  "v_mfma_f32_16x16x32_bf16 v[168:171], a[168:171], v[100:103], v[168:171]\n" \
  "v_mfma_f32_16x16x32_bf16 v[172:175], a[232:235], v[100:103], v[172:175]\n" \
  "s_waitcnt vmcnt(11)\n" \
  "v_mfma_f32_16x16x32_bf16 v[144:147], a[44:47], v[104:107], v[144:147]\n" \
  "v_mfma_f32_16x16x32_bf16 v[148:151], a[108:111], v[104:107], v[148:151]\n" \
  "v_mfma_f32_16x16x32_bf16 v[152:155], a[172:175], v[104:107], v[152:155]\n" \
  "v_mfma_f32_16x16x32_bf16 v[156:159], a[236:239], v[104:107], v[156:159]\n" \
  "s_waitcnt vmcnt(10)\n" \
  "v_mfma_f32_16x16x32_bf16 v[160:163], a[44:47], v[108:111], v[160:163]\n" \
  "v_mfma_f32_16x16x32_bf16 v[164:167], a[108:111], v[108:111], v[164:167]\n" \
  "v_mfma_f32_16x16x32_bf16 v[168:171], a[172:175], v[108:111], v[168:171]\n" \
  "v_mfma_f32_16x16x32_bf16 v[172:175], a[236:239], v[108:111], v[172:175]\n" \
  "s_waitcnt vmcnt(9)\n" \
  "v_mfma_f32_16x16x32_bf16 v[144:147], a[48:51], v[112:115], v[144:147]\n" \
  "v_mfma_f32_16x16x32_bf16 v[148:151], a[112:115], v[112:115], v[148:151]\n" \
  "v_mfma_f32_16x16x32_bf16 v[152:155], a[176:179], v[112:115], v[152:155]\n" \
  "v_mfma_f32_16x16x32_bf16 v[156:159], a[240:243], v[112:115], v[156:159]\n" \
  "s_waitcnt vmcnt(8)\n" \
  "v_mfma_f32_16x16x32_bf16 v[160:163], a[48:51], v[116:119], v[160:163]\n" \
  "v_mfma_f32_16x16x32_bf16 v[164:167], a[112:115], v[116:119], v[164:167]\n" \
  "v_mfma_f32_16x16x32_bf16 v[168:171], a[176:179], v[116:119], v[168:171]\n" \
  "v_mfma_f32_16x16x32_bf16 v[172:175], a[240:243], v[116:119], v[172:175]\n" \
  "s_waitcnt vmcnt(7)\n" \
  "v_mfma_f32_16x16x32_bf16 v[144:147], a[52:55], v[120:123], v[144:147]\n" \
  "v_mfma_f32_16x16x32_bf16 v[148:151], a[116:119], v[120:123], v[148:151]\n" \
  "v_mfma_f32_16x16x32_bf16 v[152:155], a[180:183], v[120:123], v[152:155]\n" \
  "v_mfma_f32_16x16x32_bf16 v[156:159], a[244:247], v[120:123], v[156:159]\n" \
  "s_waitcnt vmcnt(6)\n" \
  "v_mfma_f32_16x16x32_bf16 v[160:163], a[52:55], v[124:127], v[160:163]\n" \
  "v_mfma_f32_16x16x32_bf16 v[164:167], a[116:119], v[124:127], v[164:167]\n" \
  "v_mfma_f32_16x16x32_bf16 v[168:171], a[180:183], v[124:127], v[168:171]\n" \
  "v_mfma_f32_16x16x32_bf16 v[172:175], a[244:247], v[124:127], v[172:175]\n" \
  "s_waitcnt vmcnt(5)\n" \
  "v_mfma_f32_16x16x32_bf16 v[144:147], a[56:59], v[128:131], v[144:147]\n" \
  "v_mfma_f32_16x16x32_bf16 v[148:151], a[120:123], v[128:131], v[148:151]\n" \
  "v_mfma_f32_16x16x32_bf16 v[152:155], a[184:187], v[128:131], v[152:155]\n" \
  "v_mfma_f32_16x16x32_bf16 v[156:159], a[248:251], v[128:131], v[156:159]\n" \
  "s_waitcnt vmcnt(4)\n" \
  "v_mfma_f32_16x16x32_bf16 v[160:163], a[56:59], v[132:135], v[160:163]\n" \
  "v_mfma_f32_16x16x32_bf16 v[164:167], a[120:123], v[132:135], v[164:167]\n" \
  "v_mfma_f32_16x16x32_bf16 v[168:171], a[184:187], v[132:135], v[168:171]\n" \
  "v_mfma_f32_16x16x32_bf16 v[172:175], a[248:251], v[132:135], v[172:175]\n" \
  "s_waitcnt vmcnt(3)\n" \
  "v_mfma_f32_16x16x32_bf16 v[144:147], a[60:63], v[136:139], v[144:147]\n" \
  "v_mfma_f32_16x16x32_bf16 v[148:151], a[124:127], v[136:139], v[148:151]\n" \
  "v_mfma_f32_16x16x32_bf16 v[152:155], a[188:191], v[136:139], v[152:155]\n" \
  "v_mfma_f32_16x16x32_bf16 v[156:159], a[252:255], v[136:139], v[156:159]\n" \
  "s_waitcnt vmcnt(2)\n" \
  "v_mfma_f32_16x16x32_bf16 v[160:163], a[60:63], v[140:143], v[160:163]\n" \
  "v_mfma_f32_16x16x32_bf16 v[164:167], a[124:127], v[140:143], v[164:167]\n" \
  "v_mfma_f32_16x16x32_bf16 v[168:171], a[188:191], v[140:143], v[168:171]\n" \
  "v_mfma_f32_16x16x32_bf16 v[172:175], a[252:255], v[140:143], v[172:175]\n" \
  "ds_write_b128 v8, v[144:147] offset:0\n" \
  "ds_write_b128 v8, v[148:151] offset:1024\n" \
  "ds_write_b128 v8, v[152:155] offset:2048\n" \
  "ds_write_b128 v8, v[156:159] offset:3072\n" \
  "s_nop 0\n" \
  "ds_write_b128 v8, v[160:163] offset:4096\n" \
  "ds_write_b128 v8, v[164:167] offset:5120\n" \
  "ds_write_b128 v8, v[168:171] offset:6144\n" \
  "ds_write_b128 v8, v[172:175] offset:7168\n" \
  "s_lshl_b32 s52, s68, 11\n" \
  "s_sub_u32 s52, s52, 2048\n" \
  "v_add_u32 v6, s52, v10\n" \
  "ds_read_b64 v[186:187], v6 offset:0\n" \
  "WFC2_%=:\n" \
  "s_waitcnt vmcnt(1)\n" \
  "v_xor_b32 v4, s77, v180\n" \
  "v_xor_b32 v5, s77, v181\n" \
  "v_min_u32 v4, v4, v5\n" \
  "v_xor_b32 v5, s77, v182\n" \
  "v_min_u32 v4, v4, v5\n" \
  "v_xor_b32 v5, s77, v183\n" \
  "v_min_u32 v4, v4, v5\n" \
  "v_cmp_eq_u32 vcc, 0, v4\n" \
  "s_nop 3\n" \
  "s_cbranch_vccz WFD2_%=\n" \
  "s_sleep 1\n" \
  "s_add_u32 s62, s62, 1\n" \
  "s_cmp_gt_u32 s62, 131072\n" \
  "s_cbranch_scc1 WFAIL_%=\n" \
  "buffer_load_dwordx4 v[180:183], v1, s[36:39], s76 offen sc1\n" \
  "s_waitcnt vmcnt(0)\n" \
  "s_branch WFC2_%=\n" \
  "WFD2_%=:\n" \
  "v_pk_add_f32 v[180:181], v[180:181], v[176:177]\n" \
  "v_pk_add_f32 v[182:183], v[182:183], v[178:179]\n" \
  "s_waitcnt lgkmcnt(0)\n" \
  "v_lshlrev_b32 v4, 16, v186\n" \
  "s_waitcnt vmcnt(0)\n" \
  "v_lshlrev_b32 v5, 16, v184\n" \
  "v_fma_f32 v180, v180, v4, v5\n" \
  "v_and_b32 v4, 0xffff0000, v186\n" \
  "v_and_b32 v5, 0xffff0000, v184\n" \
  "v_fma_f32 v181, v181, v4, v5\n" \
  "v_cvt_pk_bf16_f32 v184, v180, v181\n" \
  "v_lshlrev_b32 v4, 16, v187\n" \
  "v_lshlrev_b32 v5, 16, v185\n" \
  "v_fma_f32 v182, v182, v4, v5\n" \
  "v_and_b32 v4, 0xffff0000, v187\n" \
  "v_and_b32 v5, 0xffff0000, v185\n" \
  "v_fma_f32 v183, v183, v4, v5\n" \
  "v_cvt_pk_bf16_f32 v185, v182, v183\n" \
  "buffer_store_dwordx2 v[184:185], v12, s[40:43], s57 offen offset:0 sc1\n" \
  "s_waitcnt lgkmcnt(0)\n" \
  "s_barrier\n" \
  "ds_read_b128 v[208:211], v14 offset:0\n" \
  "ds_read_b128 v[212:215], v14 offset:8192\n" \
  "ds_read_b128 v[216:219], v14 offset:16384\n" \
  "ds_read_b128 v[220:223], v14 offset:24576\n" \
  "ds_read_b128 v[192:195], v9 offset:0\n" \
  "ds_read_b128 v[196:199], v9 offset:8192\n" \
  "ds_read_b128 v[200:203], v9 offset:16384\n" \
  "ds_read_b128 v[204:207], v9 offset:24576\n" \
  "s_waitcnt lgkmcnt(6)\n" \
  "v_pk_add_f32 v[208:209], v[208:209], v[212:213]\n" \
  "v_pk_add_f32 v[210:211], v[210:211], v[214:215]\n" \
  "s_waitcnt lgkmcnt(4)\n" \
  "v_pk_add_f32 v[216:217], v[216:217], v[220:221]\n" \
  "v_pk_add_f32 v[218:219], v[218:219], v[222:223]\n" \
  "v_pk_add_f32 v[208:209], v[208:209], v[216:217]\n" \
  "v_pk_add_f32 v[210:211], v[210:211], v[218:219]\n" \
  "s_lshl_b32 s52, s68, 12\n" \
  "s_add_u32 s52, s52, s70\n" \
  "buffer_store_dwordx4 v[208:211], v1, s[36:39], s52 offen offset:0 sc1\n" \
  "s_waitcnt lgkmcnt(2)\n" \
  "v_pk_add_f32 v[192:193], v[192:193], v[196:197]\n" \
  "v_pk_add_f32 v[194:195], v[194:195], v[198:199]\n" \
  "s_waitcnt lgkmcnt(0)\n" \
  "v_pk_add_f32 v[200:201], v[200:201], v[204:205]\n" \
  "v_pk_add_f32 v[202:203], v[202:203], v[206:207]\n" \
  "v_pk_add_f32 v[176:177], v[192:193], v[200:201]\n" \
  "v_pk_add_f32 v[178:179], v[194:195], v[202:203]\n" \
  "v_xor_b32 v8, 32768, v8\n" \
  "v_xor_b32 v9, 32768, v9\n" \
  "v_xor_b32 v14, 32768, v14\n" \
  "s_add_u32 s66, s66, 6\n" \
  "s_add_u32 s68, s68, 1\n" \
  "s_mov_b32 s67, 0\n" \
  "s_branch WLOOP4_%=\n" \
  "WV3_%=:\n" \
  "s_add_u32 s52, s66, 6\n" \
  "s_lshl_b32 s52, s52, 13\n" \
  "s_add_u32 s72, s60, s52\n" \
  "s_add_u32 s73, s72, 524288\n" \
  "s_add_u32 s74, s73, 524288\n" \
  "s_add_u32 s75, s74, 524288\n" \
  "s_waitcnt vmcnt(31)\n" \
  "v_mfma_f32_16x16x32_bf16 v[144:147], a[0:3], v[16:19], 0\n" \
  "v_mfma_f32_16x16x32_bf16 v[148:151], a[64:67], v[16:19], 0\n" \
  "v_mfma_f32_16x16x32_bf16 v[152:155], a[128:131], v[16:19], 0\n" \
  "v_mfma_f32_16x16x32_bf16 v[156:159], a[192:195], v[16:19], 0\n" \
  "buffer_load_dwordx4 v[16:19], v1, s[36:39], s72 offen offset:0 sc1\n" \
  "s_waitcnt vmcnt(31)\n" \
  "v_mfma_f32_16x16x32_bf16 v[160:163], a[0:3], v[20:23], 0\n" \
  "v_mfma_f32_16x16x32_bf16 v[164:167], a[64:67], v[20:23], 0\n" \
  "v_mfma_f32_16x16x32_bf16 v[168:171], a[128:131], v[20:23], 0\n" \
  "v_mfma_f32_16x16x32_bf16 v[172:175], a[192:195], v[20:23], 0\n" \
  "buffer_load_dwordx4 v[20:23], v11, s[36:39], s72 offen offset:0 sc1\n" \
  "s_waitcnt vmcnt(31)\n" \
  "v_mfma_f32_16x16x32_bf16 v[144:147], a[4:7], v[24:27], v[144:147]\n" \
  "v_mfma_f32_16x16x32_bf16 v[148:151], a[68:71], v[24:27], v[148:151]\n" \
  "v_mfma_f32_16x16x32_bf16 v[152:155], a[132:135], v[24:27], v[152:155]\n" \
  "v_mfma_f32_16x16x32_bf16 v[156:159], a[196:199], v[24:27], v[156:159]\n" \
  "buffer_load_dwordx4 v[24:27], v1, s[36:39], s72 offen offset:1024 sc1\n" \
  "s_waitcnt vmcnt(31)\n" \
  "v_mfma_f32_16x16x32_bf16 v[160:163], a[4:7], v[28:31], v[160:163]\n" \
  "v_mfma_f32_16x16x32_bf16 v[164:167], a[68:71], v[28:31], v[164:167]\n" \
  "v_mfma_f32_16x16x32_bf16 v[168:171], a[132:135], v[28:31], v[168:171]\n" \
  "v_mfma_f32_16x16x32_bf16 v[172:175], a[196:199], v[28:31], v[172:175]\n" \
  "buffer_load_dwordx4 v[28:31], v11, s[36:39], s72 offen offset:1024 sc1\n" \
  "s_waitcnt vmcnt(31)\n" \
  "v_mfma_f32_16x16x32_bf16 v[144:147], a[8:11], v[32:35], v[144:147]\n" \
  "v_mfma_f32_16x16x32_bf16 v[148:151], a[72:75], v[32:35], v[148:151]\n" \
  "v_mfma_f32_16x16x32_bf16 v[152:155], a[136:139], v[32:35], v[152:155]\n" \
  "v_mfma_f32_16x16x32_bf16 v[156:159], a[200:203], v[32:35], v[156:159]\n" \
  "buffer_load_dwordx4 v[32:35], v1, s[36:39], s72 offen offset:2048 sc1\n" \
  "s_waitcnt vmcnt(31)\n" \
  "v_mfma_f32_16x16x32_bf16 v[160:163], a[8:11], v[36:39], v[160:163]\n" \
  "v_mfma_f32_16x16x32_bf16 v[164:167], a[72:75], v[36:39], v[164:167]\n" \
  "v_mfma_f32_16x16x32_bf16 v[168:171], a[136:139], v[36:39], v[168:171]\n" \
  "v_mfma_f32_16x16x32_bf16 v[172:175], a[200:203], v[36:39], v[172:175]\n" \
  "buffer_load_dwordx4 v[36:39], v11, s[36:39], s72 offen offset:2048 sc1\n" \
  "s_waitcnt vmcnt(31)\n" \
  "v_mfma_f32_16x16x32_bf16 v[144:147], a[12:15], v[40:43], v[144:147]\n" \
  "v_mfma_f32_16x16x32_bf16 v[148:151], a[76:79], v[40:43], v[148:151]\n" \
  "v_mfma_f32_16x16x32_bf16 v[152:155], a[140:143], v[40:43], v[152:155]\n" \
  "v_mfma_f32_16x16x32_bf16 v[156:159], a[204:207], v[40:43], v[156:159]\n" \
  "buffer_load_dwordx4 v[40:43], v1, s[36:39], s72 offen offset:3072 sc1\n" \
  "s_waitcnt vmcnt(31)\n" \
  "v_mfma_f32_16x16x32_bf16 v[160:163], a[12:15], v[44:47], v[160:163]\n" \
  "v_mfma_f32_16x16x32_bf16 v[164:167], a[76:79], v[44:47], v[164:167]\n" \
  "v_mfma_f32_16x16x32_bf16 v[168:171], a[140:143], v[44:47], v[168:171]\n" \
  "v_mfma_f32_16x16x32_bf16 v[172:175], a[204:207], v[44:47], v[172:175]\n" \
  "buffer_load_dwordx4 v[44:47], v11, s[36:39], s72 offen offset:3072 sc1\n" \
  "s_waitcnt vmcnt(31)\n" \
  "v_mfma_f32_16x16x32_bf16 v[144:147], a[16:19], v[48:51], v[144:147]\n" \
  "v_mfma_f32_16x16x32_bf16 v[148:151], a[80:83], v[48:51], v[148:151]\n" \
  "v_mfma_f32_16x16x32_bf16 v[152:155], a[144:147], v[48:51], v[152:155]\n" \
  "v_mfma_f32_16x16x32_bf16 v[156:159], a[208:211], v[48:51], v[156:159]\n" \
  "buffer_load_dwordx4 v[48:51], v1, s[36:39], s73 offen offset:0 sc1\n" \
  "s_waitcnt vmcnt(31)\n" \
  "v_mfma_f32_16x16x32_bf16 v[160:163], a[16:19], v[52:55], v[160:163]\n" \
  "v_mfma_f32_16x16x32_bf16 v[164:167], a[80:83], v[52:55], v[164:167]\n" \
  "v_mfma_f32_16x16x32_bf16 v[168:171], a[144:147], v[52:55], v[168:171]\n" \
  "v_mfma_f32_16x16x32_bf16 v[172:175], a[208:211], v[52:55], v[172:175]\n" \
  "buffer_load_dwordx4 v[52:55], v11, s[36:39], s73 offen offset:0 sc1\n" \
  "s_waitcnt vmcnt(31)\n" \
  "v_mfma_f32_16x16x32_bf16 v[144:147], a[20:23], v[56:59], v[144:147]\n" \
  "v_mfma_f32_16x16x32_bf16 v[148:151], a[84:87], v[56:59], v[148:151]\n" \
  "v_mfma_f32_16x16x32_bf16 v[152:155], a[148:151], v[56:59], v[152:155]\n" \
  "v_mfma_f32_16x16x32_bf16 v[156:159], a[212:215], v[56:59], v[156:159]\n" \
  "buffer_load_dwordx4 v[56:59], v1, s[36:39], s73 offen offset:1024 sc1\n" \
  "s_waitcnt vmcnt(31)\n" \
  "v_mfma_f32_16x16x32_bf16 v[160:163], a[20:23], v[60:63], v[160:163]\n" \
  "v_mfma_f32_16x16x32_bf16 v[164:167], a[84:87], v[60:63], v[164:167]\n" \
  "v_mfma_f32_16x16x32_bf16 v[168:171], a[148:151], v[60:63], v[168:171]\n" \
  "v_mfma_f32_16x16x32_bf16 v[172:175], a[212:215], v[60:63], v[172:175]\n" \
  "buffer_load_dwordx4 v[60:63], v11, s[36:39], s73 offen offset:1024 sc1\n" \
  "s_waitcnt vmcnt(31)\n" \
  "v_mfma_f32_16x16x32_bf16 v[144:147], a[24:27], v[64:67], v[144:147]\n" \
  "v_mfma_f32_16x16x32_bf16 v[148:151], a[88:91], v[64:67], v[148:151]\n" \
  "v_mfma_f32_16x16x32_bf16 v[152:155], a[152:155], v[64:67], v[152:155]\n" \
  "v_mfma_f32_16x16x32_bf16 v[156:159], a[216:219], v[64:67], v[156:159]\n" \
  "buffer_load_dwordx4 v[64:67], v1, s[36:39], s73 offen offset:2048 sc1\n" \
  "s_waitcnt vmcnt(31)\n" \
  "v_mfma_f32_16x16x32_bf16 v[160:163], a[24:27], v[68:71], v[160:163]\n" \
  "v_mfma_f32_16x16x32_bf16 v[164:167], a[88:91], v[68:71], v[164:167]\n" \
  "v_mfma_f32_16x16x32_bf16 v[168:171], a[152:155], v[68:71], v[168:171]\n" \
  "v_mfma_f32_16x16x32_bf16 v[172:175], a[216:219], v[68:71], v[172:175]\n" \
  "buffer_load_dwordx4 v[68:71], v11, s[36:39], s73 offen offset:2048 sc1\n" \
  "s_waitcnt vmcnt(31)\n" \
  "v_mfma_f32_16x16x32_bf16 v[144:147], a[28:31], v[72:75], v[144:147]\n" \
  "v_mfma_f32_16x16x32_bf16 v[148:151], a[92:95], v[72:75], v[148:151]\n" \
  "v_mfma_f32_16x16x32_bf16 v[152:155], a[156:159], v[72:75], v[152:155]\n" \
  "v_mfma_f32_16x16x32_bf16 v[156:159], a[220:223], v[72:75], v[156:159]\n" \
  "buffer_load_dwordx4 v[72:75], v1, s[36:39], s73 offen offset:3072 sc1\n" \
  "s_waitcnt vmcnt(31)\n" \
  "v_mfma_f32_16x16x32_bf16 v[160:163], a[28:31], v[76:79], v[160:163]\n" \
  "v_mfma_f32_16x16x32_bf16 v[164:167], a[92:95], v[76:79], v[164:167]\n" \
  "v_mfma_f32_16x16x32_bf16 v[168:171], a[156:159], v[76:79], v[168:171]\n" \
  "v_mfma_f32_16x16x32_bf16 v[172:175], a[220:223], v[76:79], v[172:175]\n" \
  "buffer_load_dwordx4 v[76:79], v11, s[36:39], s73 offen offset:3072 sc1\n" \
  "s_waitcnt vmcnt(31)\n" \
  "v_mfma_f32_16x16x32_bf16 v[144:147], a[32:35], v[80:83], v[144:147]\n" \
  "v_mfma_f32_16x16x32_bf16 v[148:151], a[96:99], v[80:83], v[148:151]\n" \
  "v_mfma_f32_16x16x32_bf16 v[152:155], a[160:163], v[80:83], v[152:155]\n" \
  "v_mfma_f32_16x16x32_bf16 v[156:159], a[224:227], v[80:83], v[156:159]\n" \
  "buffer_load_dwordx4 v[80:83], v1, s[36:39], s74 offen offset:0 sc1\n" \
  "s_waitcnt vmcnt(31)\n" \
  "v_mfma_f32_16x16x32_bf16 v[160:163], a[32:35], v[84:87], v[160:163]\n" \
  "v_mfma_f32_16x16x32_bf16 v[164:167], a[96:99], v[84:87], v[164:167]\n" \
  "v_mfma_f32_16x16x32_bf16 v[168:171], a[160:163], v[84:87], v[168:171]\n" \
  "v_mfma_f32_16x16x32_bf16 v[172:175], a[224:227], v[84:87], v[172:175]\n" \
  "buffer_load_dwordx4 v[84:87], v11, s[36:39], s74 offen offset:0 sc1\n" \
  "s_waitcnt vmcnt(31)\n" \
  "v_mfma_f32_16x16x32_bf16 v[144:147], a[36:39], v[88:91], v[144:147]\n" \
  "v_mfma_f32_16x16x32_bf16 v[148:151], a[100:103], v[88:91], v[148:151]\n" \
  "v_mfma_f32_16x16x32_bf16 v[152:155], a[164:167], v[88:91], v[152:155]\n" \
  "v_mfma_f32_16x16x32_bf16 v[156:159], a[228:231], v[88:91], v[156:159]\n" \
  "buffer_load_dwordx4 v[88:91], v1, s[36:39], s74 offen offset:1024 sc1\n" \
  "s_waitcnt vmcnt(31)\n" \
  "v_mfma_f32_16x16x32_bf16 v[160:163], a[36:39], v[92:95], v[160:163]\n" \
  "v_mfma_f32_16x16x32_bf16 v[164:167], a[100:103], v[92:95], v[164:167]\n" \
  "v_mfma_f32_16x16x32_bf16 v[168:171], a[164:167], v[92:95], v[168:171]\n" \
  "v_mfma_f32_16x16x32_bf16 v[172:175], a[228:231], v[92:95], v[172:175]\n" \
  "buffer_load_dwordx4 v[92:95], v11, s[36:39], s74 offen offset:1024 sc1\n" \
  "s_waitcnt vmcnt(31)\n" \
  "v_mfma_f32_16x16x32_bf16 v[144:147], a[40:43], v[96:99], v[144:147]\n" \
  "s_lshl_b32 s52, s68, 12\n" \
  "s_add_u32 s52, s52, s71\n" \
  "s_sub_u32 s76, s52, 4096\n" \
  "buffer_load_dwordx4 v[180:183], v1, s[36:39], s76 offen offset:0 sc1\n" \
  "s_sub_u32 s52, s66, 6\n" \
  "s_mul_i32 s57, s52, 40960\n" \
  "buffer_load_dwordx2 v[184:185], v12, s[44:47], s57 offen offset:0 \n" \
  "buffer_load_dword v188, v13, s[36:39], s61 offen offset:0 sc1\n" \
  "v_mfma_f32_16x16x32_bf16 v[148:151], a[104:107], v[96:99], v[148:151]\n" \
  "v_mfma_f32_16x16x32_bf16 v[152:155], a[168:171], v[96:99], v[152:155]\n" \
  "v_mfma_f32_16x16x32_bf16 v[156:159], a[232:235], v[96:99], v[156:159]\n" \
  "buffer_load_dwordx4 v[96:99], v1, s[36:39], s74 offen offset:2048 sc1\n" \
  "s_waitcnt vmcnt(34)\n" \
  "v_mfma_f32_16x16x32_bf16 v[160:163], a[40:43], v[100:103], v[160:163]\n" \
  "v_mfma_f32_16x16x32_bf16 v[164:167], a[104:107], v[100:103], v[164:167]\n" \
  "v_mfma_f32_16x16x32_bf16 v[168:171], a[168:171], v[100:103], v[168:171]\n" \
  "v_mfma_f32_16x16x32_bf16 v[172:175], a[232:235], v[100:103], v[172:175]\n" \
  "buffer_load_dwordx4 v[100:103], v11, s[36:39], s74 offen offset:2048 sc1\n" \
  "s_waitcnt vmcnt(34)\n" \
  "v_mfma_f32_16x16x32_bf16 v[144:147], a[44:47], v[104:107], v[144:147]\n" \
  "v_mfma_f32_16x16x32_bf16 v[148:151], a[108:111], v[104:107], v[148:151]\n" \
  "v_mfma_f32_16x16x32_bf16 v[152:155], a[172:175], v[104:107], v[152:155]\n" \
  "v_mfma_f32_16x16x32_bf16 v[156:159], a[236:239], v[104:107], v[156:159]\n" \
  "buffer_load_dwordx4 v[104:107], v1, s[36:39], s74 offen offset:3072 sc1\n" \
  "s_waitcnt vmcnt(34)\n" \
  "v_mfma_f32_16x16x32_bf16 v[160:163], a[44:47], v[108:111], v[160:163]\n" \
  "v_mfma_f32_16x16x32_bf16 v[164:167], a[108:111], v[108:111], v[164:167]\n" \
  "v_mfma_f32_16x16x32_bf16 v[168:171], a[172:175], v[108:111], v[168:171]\n" \
  "v_mfma_f32_16x16x32_bf16 v[172:175], a[236:239], v[108:111], v[172:175]\n" \
  "buffer_load_dwordx4 v[108:111], v11, s[36:39], s74 offen offset:3072 sc1\n" \
  "s_waitcnt vmcnt(34)\n" \
  "v_mfma_f32_16x16x32_bf16 v[144:147], a[48:51], v[112:115], v[144:147]\n" \
  "v_mfma_f32_16x16x32_bf16 v[148:151], a[112:115], v[112:115], v[148:151]\n" \
  "v_mfma_f32_16x16x32_bf16 v[152:155], a[176:179], v[112:115], v[152:155]\n" \
  "v_mfma_f32_16x16x32_bf16 v[156:159], a[240:243], v[112:115], v[156:159]\n" \
  "buffer_load_dwordx4 v[112:115], v1, s[36:39], s75 offen offset:0 sc1\n" \
  "s_waitcnt vmcnt(34)\n" \
  "v_mfma_f32_16x16x32_bf16 v[160:163], a[48:51], v[116:119], v[160:163]\n" \
  "v_mfma_f32_16x16x32_bf16 v[164:167], a[112:115], v[116:119], v[164:167]\n" \
  "v_mfma_f32_16x16x32_bf16 v[168:171], a[176:179], v[116:119], v[168:171]\n" \
  "v_mfma_f32_16x16x32_bf16 v[172:175], a[240:243], v[116:119], v[172:175]\n" \
  "buffer_load_dwordx4 v[116:119], v11, s[36:39], s75 offen offset:0 sc1\n" \
  "s_waitcnt vmcnt(34)\n" \
  "v_mfma_f32_16x16x32_bf16 v[144:147], a[52:55], v[120:123], v[144:147]\n" \
  "v_mfma_f32_16x16x32_bf16 v[148:151], a[116:119], v[120:123], v[148:151]\n" \
  "v_mfma_f32_16x16x32_bf16 v[152:155], a[180:183], v[120:123], v[152:155]\n" \
  "v_mfma_f32_16x16x32_bf16 v[156:159], a[244:247], v[120:123], v[156:159]\n" \
  "buffer_load_dwordx4 v[120:123], v1, s[36:39], s75 offen offset:1024 sc1\n" \
  "s_waitcnt vmcnt(34)\n" \
  "v_mfma_f32_16x16x32_bf16 v[160:163], a[52:55], v[124:127], v[160:163]\n" \
  "v_mfma_f32_16x16x32_bf16 v[164:167], a[116:119], v[124:127], v[164:167]\n" \
  "v_mfma_f32_16x16x32_bf16 v[168:171], a[180:183], v[124:127], v[168:171]\n" \
  "v_mfma_f32_16x16x32_bf16 v[172:175], a[244:247], v[124:127], v[172:175]\n" \
  "buffer_load_dwordx4 v[124:127], v11, s[36:39], s75 offen offset:1024 sc1\n" \
  "s_waitcnt vmcnt(34)\n" \
  "v_mfma_f32_16x16x32_bf16 v[144:147], a[56:59], v[128:131], v[144:147]\n" \
  "v_mfma_f32_16x16x32_bf16 v[148:151], a[120:123], v[128:131], v[148:151]\n" \
  "v_mfma_f32_16x16x32_bf16 v[152:155], a[184:187], v[128:131], v[152:155]\n" \
  "v_mfma_f32_16x16x32_bf16 v[156:159], a[248:251], v[128:131], v[156:159]\n" \
  "buffer_load_dwordx4 v[128:131], v1, s[36:39], s75 offen offset:2048 sc1\n" \
  "s_waitcnt vmcnt(34)\n" \
  "v_mfma_f32_16x16x32_bf16 v[160:163], a[56:59], v[132:135], v[160:163]\n" \
  "v_mfma_f32_16x16x32_bf16 v[164:167], a[120:123], v[132:135], v[164:167]\n" \
  "v_mfma_f32_16x16x32_bf16 v[168:171], a[184:187], v[132:135], v[168:171]\n" \
  "v_mfma_f32_16x16x32_bf16 v[172:175], a[248:251], v[132:135], v[172:175]\n" \
  "buffer_load_dwordx4 v[132:135], v11, s[36:39], s75 offen offset:2048 sc1\n" \
  "s_waitcnt vmcnt(34)\n" \
  "v_mfma_f32_16x16x32_bf16 v[144:147], a[60:63], v[136:139], v[144:147]\n" \
  "v_mfma_f32_16x16x32_bf16 v[148:151], a[124:127], v[136:139], v[148:151]\n" \
  "v_mfma_f32_16x16x32_bf16 v[152:155], a[188:191], v[136:139], v[152:155]\n" \
  "v_mfma_f32_16x16x32_bf16 v[156:159], a[252:255], v[136:139], v[156:159]\n" \
  "buffer_load_dwordx4 v[136:139], v1, s[36:39], s75 offen offset:3072 sc1\n" \
  "s_waitcnt vmcnt(34)\n" \
  "v_mfma_f32_16x16x32_bf16 v[160:163], a[60:63], v[140:143], v[160:163]\n" \
  "v_mfma_f32_16x16x32_bf16 v[164:167], a[124:127], v[140:143], v[164:167]\n" \
  "v_mfma_f32_16x16x32_bf16 v[168:171], a[188:191], v[140:143], v[168:171]\n" \
  "v_mfma_f32_16x16x32_bf16 v[172:175], a[252:255], v[140:143], v[172:175]\n" \
  "buffer_load_dwordx4 v[140:143], v11, s[36:39], s75 offen offset:3072 sc1\n" \
  "ds_write_b128 v8, v[144:147] offset:0\n" \
  "ds_write_b128 v8, v[148:151] offset:1024\n" \
  "ds_write_b128 v8, v[152:155] offset:2048\n" \
  "ds_write_b128 v8, v[156:159] offset:3072\n" \
  "ds_write_b128 v8, v[160:163] offset:4096\n" \
  "ds_write_b128 v8, v[164:167] offset:5120\n" \
  "ds_write_b128 v8, v[168:171] offset:6144\n" \
  "ds_write_b128 v8, v[172:175] offset:7168\n" \
  "s_lshl_b32 s52, s68, 11\n" \
  "s_sub_u32 s52, s52, 2048\n" \
  "v_add_u32 v6, s52, v10\n" \
  "ds_read_b64 v[186:187], v6 offset:0\n" \
  "WFC3_%=:\n" \
  "s_waitcnt vmcnt(14)\n" \
  "v_xor_b32 v4, s77, v180\n" \
  "v_xor_b32 v5, s77, v181\n" \
  "v_min_u32 v4, v4, v5\n" \
  "v_xor_b32 v5, s77, v182\n" \
  "v_min_u32 v4, v4, v5\n" \
  "v_xor_b32 v5, s77, v183\n" \
  "v_min_u32 v4, v4, v5\n" \
  "v_cmp_eq_u32 vcc, 0, v4\n" \
  "s_nop 3\n" \
  "s_cbranch_vccz WFD3_%=\n" \
  "s_sleep 1\n" \
  "s_add_u32 s62, s62, 1\n" \
  "s_cmp_gt_u32 s62, 131072\n" \
  "s_cbranch_scc1 WFAIL_%=\n" \
  "buffer_load_dwordx4 v[180:183], v1, s[36:39], s76 offen sc1\n" \
  "s_waitcnt vmcnt(0)\n" \
  "s_branch WFC3_%=\n" \
  "WFD3_%=:\n" \
  "v_pk_add_f32 v[180:181], v[180:181], v[176:177]\n" \
  "v_pk_add_f32 v[182:183], v[182:183], v[178:179]\n" \
  "s_waitcnt lgkmcnt(0)\n" \
  "v_lshlrev_b32 v4, 16, v186\n" \
  "s_waitcnt vmcnt(13)\n" \
  "v_lshlrev_b32 v5, 16, v184\n" \
  "v_fma_f32 v180, v180, v4, v5\n" \
  "v_and_b32 v4, 0xffff0000, v186\n" \
  "v_and_b32 v5, 0xffff0000, v184\n" \
  "v_fma_f32 v181, v181, v4, v5\n" \
  "v_cvt_pk_bf16_f32 v184, v180, v181\n" \
  "v_lshlrev_b32 v4, 16, v187\n" \
  "v_lshlrev_b32 v5, 16, v185\n" \
  "v_fma_f32 v182, v182, v4, v5\n" \
  "v_and_b32 v4, 0xffff0000, v187\n" \
  "v_and_b32 v5, 0xffff0000, v185\n" \
  "v_fma_f32 v183, v183, v4, v5\n" \
  "v_cvt_pk_bf16_f32 v185, v182, v183\n" \
  "buffer_store_dwordx2 v[184:185], v12, s[40:43], s57 offen offset:0 sc1\n" \
  "s_waitcnt vmcnt(13)\n" \
  "s_waitcnt lgkmcnt(0)\n" \
  "s_barrier\n" \
  "ds_read_b128 v[208:211], v14 offset:0\n" \
  "ds_read_b128 v[212:215], v14 offset:8192\n" \
  "ds_read_b128 v[216:219], v14 offset:16384\n" \
  "ds_read_b128 v[220:223], v14 offset:24576\n" \
  "ds_read_b128 v[192:195], v9 offset:0\n" \
  "ds_read_b128 v[196:199], v9 offset:8192\n" \
  "ds_read_b128 v[200:203], v9 offset:16384\n" \
  "ds_read_b128 v[204:207], v9 offset:24576\n" \
  "s_waitcnt lgkmcnt(6)\n" \
  "v_pk_add_f32 v[208:209], v[208:209], v[212:213]\n" \
  "v_pk_add_f32 v[210:211], v[210:211], v[214:215]\n" \
  "s_waitcnt lgkmcnt(4)\n" \
  "v_pk_add_f32 v[216:217], v[216:217], v[220:221]\n" \
  "v_pk_add_f32 v[218:219], v[218:219], v[222:223]\n" \
  "v_pk_add_f32 v[208:209], v[208:209], v[216:217]\n" \
  "v_pk_add_f32 v[210:211], v[210:211], v[218:219]\n" \
  "s_lshl_b32 s52, s68, 12\n" \
  "s_add_u32 s52, s52, s70\n" \
  "buffer_store_dwordx4 v[208:211], v1, s[36:39], s52 offen offset:0 sc1\n" \
  "s_waitcnt lgkmcnt(2)\n" \
  "v_pk_add_f32 v[192:193], v[192:193], v[196:197]\n" \
  "v_pk_add_f32 v[194:195], v[194:195], v[198:199]\n" \
  "s_waitcnt lgkmcnt(0)\n" \
  "v_pk_add_f32 v[200:201], v[200:201], v[204:205]\n" \
  "v_pk_add_f32 v[202:203], v[202:203], v[206:207]\n" \
  "v_pk_add_f32 v[176:177], v[192:193], v[200:201]\n" \
  "v_pk_add_f32 v[178:179], v[194:195], v[202:203]\n" \
  "v_xor_b32 v8, 32768, v8\n" \
  "v_xor_b32 v9, 32768, v9\n" \
  "v_xor_b32 v14, 32768, v14\n" \
  "s_add_u32 s66, s66, 6\n" \
  "s_add_u32 s68, s68, 1\n" \
  "s_mov_b32 s67, 1\n" \
  "s_branch WLOOP4_%=\n" \
  "WDONE_%=:\n" \
  "s_branch WBYE_%=\n" \
  "WIDLE_%=:\n" \
  "s_mov_b32 s52, 12591104\n" \
  "buffer_load_dword v5, off, s[36:39], s52 sc1\n" \
  "s_waitcnt vmcnt(0)\n" \
  "v_cmp_gt_u32 vcc, 2, v5\n" \
  "s_nop 4\n" \
  "s_cbranch_vccz WBYE_%=\n" \
  "s_sleep 32\n" \
  "s_add_u32 s62, s62, 1\n" \
  "s_cmp_gt_u32 s62, 131072\n" \
  "s_cbranch_scc1 WFAIL_%=\n" \
  "s_branch WIDLE_%=\n" \
  "WFAIL_%=:\n" \
  "v_mov_b32 v4, 1\n" \
  "s_mov_b32 s52, 12582916\n" \
  "buffer_store_dword v4, off, s[36:39], s52 sc1\n" \
  "WBYE_%=:\n" \
  "v_mov_b32 v40, 0\n" \
  "v_mov_b32 v41, 0\n" \
  "s_lshl_b32 s52, %[b], 6\n" \
  "s_lshl_b32 s53, s48, 3\n" \
  "s_add_u32 s52, s52, s53\n" \
  "s_add_u32 s52, s52, 12615680\n" \
  "buffer_store_dwordx2 v[40:41], off, s[36:39], s52 sc1\n" \
  "WEND_%=:\n" \
  "s_waitcnt vmcnt(0) lgkmcnt(0)\n"
#define WORK_ASM_CLOBBERS "v0", "v1", "v2", "v3", "v4", "v5", "v6", "v7", "v8", "v9", "v10", "v11", "v12", "v13", "v14", "v15", "v16", "v17", "v18", "v19", "v20", "v21", "v22", "v23", "v24", "v25", "v26", "v27", "v28", "v29", "v30", "v31", "v32", "v33", "v34", "v35", "v36", "v37", "v38", "v39", "v40", "v41", "v42", "v43", "v44", "v45", "v46", "v47", "v48", "v49", "v50", "v51", "v52", "v53", "v54", "v55", "v56", "v57", "v58", "v59", "v60", "v61", "v62", "v63", "v64", "v65", "v66", "v67", "v68", "v69", "v70", "v71", "v72", "v73", "v74", "v75", "v76", "v77", "v78", "v79", "v80", "v81", "v82", "v83", "v84", "v85", "v86", "v87", "v88", "v89", "v90", "v91", "v92", "v93", "v94", "v95", "v96", "v97", "v98", "v99", "v100", "v101", "v102", "v103", "v104", "v105", "v106", "v107", "v108", "v109", "v110", "v111", "v112", "v113", "v114", "v115", "v116", "v117", "v118", "v119", "v120", "v121", "v122", "v123", "v124", "v125", "v126", "v127", "v128", "v129", "v130", "v131", "v132", "v133", "v134", "v135", "v136", "v137", "v138", "v139", "v140", "v141", "v142", "v143", "v144", "v145", "v146", "v147", "v148", "v149", "v150", "v151", "v152", "v153", "v154", "v155", "v156", "v157", "v158", "v159", "v160", "v161", "v162", "v163", "v164", "v165", "v166", "v167", "v168", "v169", "v170", "v171", "v172", "v173", "v174", "v175", "v176", "v177", "v178", "v179", "v180", "v181", "v182", "v183", "v184", "v185", "v186", "v187", "v188", "v189", "v190", "v191", "v192", "v193", "v194", "v195", "v196", "v197", "v198", "v199", "v200", "v201", "v202", "v203", "v204", "v205", "v206", "v207", "v208", "v209", "v210", "v211", "v212", "v213", "v214", "v215", "v216", "v217", "v218", "v219", "v220", "v221", "v222", "v223", "v224", "v225", "v226", "v227", "v228", "v229", "v230", "v231", "v232", "v233", "v234", "v235", "v236", "v237", "v238", "v239", "v240", "v241", "v242", "v243", "v244", "v245", "v246", "v247", "v248", "v249", "v250", "v251", "v252", "v253", "v254", "v255", "a0", "a1", "a2", "a3", "a4", "a5", "a6", "a7", "a8", "a9", "a10", "a11", "a12", "a13", "a14", "a15", "a16", "a17", "a18", "a19", "a20", "a21", "a22", "a23", "a24", "a25", "a26", "a27", "a28", "a29", "a30", "a31", "a32", "a33", "a34", "a35", "a36", "a37", "a38", "a39", "a40", "a41", "a42", "a43", "a44", "a45", "a46", "a47", "a48", "a49", "a50", "a51", "a52", "a53", "a54", "a55", "a56", "a57", "a58", "a59", "a60", "a61", "a62", "a63", "a64", "a65", "a66", "a67", "a68", "a69", "a70", "a71", "a72", "a73", "a74", "a75", "a76", "a77", "a78", "a79", "a80", "a81", "a82", "a83", "a84", "a85", "a86", "a87", "a88", "a89", "a90", "a91", "a92", "a93", "a94", "a95", "a96", "a97", "a98", "a99", "a100", "a101", "a102", "a103", "a104", "a105", "a106", "a107", "a108", "a109", "a110", "a111", "a112", "a113", "a114", "a115", "a116", "a117", "a118", "a119", "a120", "a121", "a122", "a123", "a124", "a125", "a126", "a127", "a128", "a129", "a130", "a131", "a132", "a133", "a134", "a135", "a136", "a137", "a138", "a139", "a140", "a141", "a142", "a143", "a144", "a145", "a146", "a147", "a148", "a149", "a150", "a151", "a152", "a153", "a154", "a155", "a156", "a157", "a158", "a159", "a160", "a161", "a162", "a163", "a164", "a165", "a166", "a167", "a168", "a169", "a170", "a171", "a172", "a173", "a174", "a175", "a176", "a177", "a178", "a179", "a180", "a181", "a182", "a183", "a184", "a185", "a186", "a187", "a188", "a189", "a190", "a191", "a192", "a193", "a194", "a195", "a196", "a197", "a198", "a199", "a200", "a201", "a202", "a203", "a204", "a205", "a206", "a207", "a208", "a209", "a210", "a211", "a212", "a213", "a214", "a215", "a216", "a217", "a218", "a219", "a220", "a221", "a222", "a223", "a224", "a225", "a226", "a227", "a228", "a229", "a230", "a231", "a232", "a233", "a234", "a235", "a236", "a237", "a238", "a239", "a240", "a241", "a242", "a243", "a244", "a245", "a246", "a247", "a248", "a249", "a250", "a251", "a252", "a253", "a254", "a255", "s10", "s11", "s12", "s13", "s14", "s15", "s16", "s17", "s18", "s19", "s20", "s21", "s22", "s23", "s24", "s25", "s26", "s27", "s28", "s29", "s30", "s31", "s32", "s33", "s34", "s35", "s36", "s37", "s38", "s39", "s40", "s41", "s42", "s43", "s44", "s45", "s46", "s47", "s48", "s49", "s50", "s51", "s52", "s53", "s54", "s55", "s56", "s57", "s58", "s59", "s60", "s61", "s62", "s63", "s64", "s65", "s66", "s67", "s68", "s69", "s70", "s71", "s72", "s73", "s74", "s75", "s76", "s77", "s78", "s79", "s80", "s81", "s82", "s83", "s84", "s85", "s86", "s87", "s88", "s89", "s90", "s91", "s92", "s93", "s94", "s95", "s96", "s97", "s98", "s99", "vcc", "scc", "memory"

constexpr int LDS_BYTES = 160 * 1024;
__global__ void __launch_bounds__(256, 1) k_main(const u16* x, const u16* Wq, const u16* Wk, const u16* Wv, const u16* Wo, const u16* w1, const u16* w3, const u16* w2, unsigned char* ws, u16* out, const u16* mem0) {
  const int b = blockIdx.x;
  start_part(x, mem0, w1, w3, w2, Wq, Wk, Wv, ws);
  const int wv = __builtin_amdgcn_readfirstlane(threadIdx.x >> 6);
  asm volatile("s_getreg_b32 s92, hwreg(20, 0, 3)\n"
               "ENTRY_%=:\n s_and_b32 s93, s92, 3\n s_lshl_b32 s93, s93, 5\n s_lshr_b32 s94, %[b], 3\n s_add_u32 s93, s93, s94\n s_lshl_b32 s94, %[wv], 8\n s_or_b32 s93, s93, s94\n"
               "s_bitcmp1_b32 s92, 2\n s_cbranch_scc1 ROLEW_%=\n" CHAIN_ASM_TEXT "s_endpgm\n"
               "REDO_%=:\n s_mov_b32 s92, s94\n s_waitcnt vmcnt(0) lgkmcnt(0)\n s_barrier\n s_branch ENTRY_%=\n"
               "ROLEW_%=:\n" WORK_ASM_TEXT "KEND:\n"
               :: [ka] "s"(__builtin_amdgcn_kernarg_segment_ptr()), [wv] "s"(wv), [b] "s"(b) : WORK_ASM_CLOBBERS);
}

extern "C" void kernel_launch(void* const* d_in, const int* in_sizes, int n_in, void* d_out, int out_size, void* d_ws, size_t ws_size, hipStream_t stream) {
  const u16* x = (const u16*)d_in[0]; const u16* mem0 = (const u16*)d_in[1];
  const u16* Wq = (const u16*)d_in[2]; const u16* Wk = (const u16*)d_in[3]; const u16* Wv = (const u16*)d_in[4]; const u16* Wo = (const u16*)d_in[5];
  const u16* w1 = (const u16*)d_in[8]; const u16* w3 = (const u16*)d_in[9]; const u16* w2 = (const u16*)d_in[10];
  unsigned char* ws = (unsigned char*)(((uintptr_t)d_ws + PAGE - 1) & ~(uintptr_t)(PAGE - 1));
  (void)hipFuncSetAttribute((const void*)k_main, hipFuncAttributeMaxDynamicSharedMemorySize, LDS_BYTES);
  k_main<<<256, 256, LDS_BYTES, stream>>>(x, Wq, Wk, Wv, Wo, w1, w3, w2, ws, (u16*)d_out, mem0);
}
```
